```python
import torch

def _softmax(x, axis=-1):
    return torch.softmax(x, dim=axis)

def _gelu(x):
    return torch.nn.functional.gelu(x, approximate='tanh')

def _silu(x):
    return torch.nn.functional.silu(x)

def _sigmoid(x):
    return torch.sigmoid(x)

def _layernorm(x, axis=-1, eps=1e-05):
    m = x.mean(dim=axis, keepdim=True)
    v = x.var(dim=axis, keepdim=True, unbiased=False)
    return (x - m) / torch.sqrt(v + eps)

def _scatter_add(tgt, idx, src, axis=0):
    return tgt.clone().index_add_(axis, idx, src)

def _segment_sum(src, seg, n):
    out = torch.zeros((n,) + src.shape[1:], dtype=src.dtype)
    return out.index_add_(0, seg, src)

def _pad(x, pw):
    flat = tuple((v for pair in reversed(pw) for v in pair))
    return torch.nn.functional.pad(x, flat)
kernel_name = 'adaLN_cond__grouped_kv_mqa__glu_gated_glu__prompt'

def setup_inputs():

    _w = lambda a: (lambda t: t.to(torch.bfloat16) if t.is_floating_point() else t)(torch.as_tensor(a))

    def _A_setup():
        B, S, D, H = (1, 2048, 512, 32)
        Dh = D // H
        return {'x': _w((lambda *s: torch.randn(*s, dtype=torch.bfloat16) * 0.05)(B, S, D)), 'wq': _w((lambda *s: torch.randn(*s, dtype=torch.bfloat16) * 0.05)(D, H * Dh)), 'wk': _w((lambda *s: torch.randn(*s, dtype=torch.bfloat16) * 0.05)(D, Dh)), 'wv': _w((lambda *s: torch.randn(*s, dtype=torch.bfloat16) * 0.05)(D, Dh)), 'wo': _w((lambda *s: torch.randn(*s, dtype=torch.bfloat16) * 0.05)(H * Dh, D))}
    _A_in = _A_setup()

    def _B_setup():
        B, S, D, F = (1, 2048, 512, 1792)
        return {'x': _w((lambda *s: torch.randn(*s, dtype=torch.bfloat16) * 0.05)(B, S, D)), 'w_gate': _w((lambda *s: torch.randn(*s, dtype=torch.bfloat16) * 0.05)(D, F)), 'w_up': _w((lambda *s: torch.randn(*s, dtype=torch.bfloat16) * 0.05)(D, F)), 'w_down': _w((lambda *s: torch.randn(*s, dtype=torch.bfloat16) * 0.05)(F, D))}
    _B_in = _B_setup()
    return {'A_x': _A_in['x'], 'A_wq': _A_in['wq'], 'A_wk': _A_in['wk'], 'A_wv': _A_in['wv'], 'A_wo': _A_in['wo'], 'B_x': _B_in['x'], 'B_w_gate': _B_in['w_gate'], 'B_w_up': _B_in['w_up'], 'B_w_down': _B_in['w_down'], 'c': _w((lambda *s: torch.randn(*s, dtype=torch.bfloat16) * 0.1)(1, 512) * 8.0), 'w_mod': _w((lambda *s: torch.randn(*s, dtype=torch.bfloat16) * 0.1)(512, 6 * 512) * 512 ** (-0.5) * 8.0)}

def reference(A_x, A_wq, A_wk, A_wv, A_wo, B_x, B_w_gate, B_w_up, B_w_down, c, w_mod):

    def _A_ref(x, wq, wk, wv, wo):
        B, S, D = x.shape
        H = 32
        Dh = D // H
        q = (lambda x, *d: x.permute(*d))((x @ wq).reshape(B, S, H, Dh), 0, 2, 1, 3)
        k = x @ wk
        v = x @ wv
        scores = torch.einsum('bhqd,bkd->bhqk', q, k) * Dh ** (-0.5)
        p = _softmax(scores, axis=-1)
        o = torch.einsum('bhqk,bkd->bhqd', p, v)
        return (lambda x, *d: x.permute(*d))(o, 0, 2, 1, 3).reshape(B, S, H * Dh) @ wo

    def _B_ref(x, w_gate, w_up, w_down):
        g = torch.einsum('bsd,df->bsf', x, w_gate)
        u = torch.einsum('bsd,df->bsf', x, w_up)
        h = _sigmoid(g) * u
        return torch.einsum('bsf,fd->bsd', h, w_down)
    x = A_x
    mod = (_silu(c) @ w_mod).reshape(c.shape[0], 6, 1, -1)
    sa, ca, ga, sb, cb, gb = (mod[:, 0].clone(), mod[:, 1].clone(), mod[:, 2].clone(), mod[:, 3].clone(), mod[:, 4].clone(), mod[:, 5].clone())
    h = _layernorm(x, axis=-1) * (1 + ca) + sa
    x = x + ga * _A_ref(h, A_wq, A_wk, A_wv, A_wo)
    h = _layernorm(x, axis=-1) * (1 + cb) + sb
    x = x + gb * _B_ref(h, B_w_gate, B_w_up, B_w_down)
    return x
```

```cpp
#include <hip/hip_runtime.h>
#include <hip/hip_bf16.h>
#include <stdint.h>

typedef unsigned short u16;
typedef short bf16x8 __attribute__((ext_vector_type(8)));
typedef short s16x4 __attribute__((ext_vector_type(4)));
typedef float f32x16 __attribute__((ext_vector_type(16)));
typedef float f32x4 __attribute__((ext_vector_type(4)));
typedef float f32x2 __attribute__((ext_vector_type(2)));
typedef unsigned u32x4 __attribute__((ext_vector_type(4)));
typedef unsigned u32x2 __attribute__((ext_vector_type(2)));
typedef __bf16 bf2_t __attribute__((ext_vector_type(2)));
#define LAS __attribute__((address_space(3)))
#define DI __device__ __forceinline__
#define MFMA32(a, b, c) __builtin_amdgcn_mfma_f32_32x32x16_bf16((a), (b), (c), 0, 0, 0)

constexpr int S = 2048, D = 512, H = 32, DH = 16, F = 1792;
constexpr float QSCALE = 0.25f * 1.4426950408889634f;

DI float bf2f(u16 v) { return __uint_as_float((unsigned)v << 16); }
DI unsigned pk2(float lo, float hi) { f32x2 v = {lo, hi}; bf2_t r = __builtin_convertvector(v, bf2_t); return __builtin_bit_cast(unsigned, r); }
DI float lo2f(unsigned v) { return __uint_as_float(v << 16); }
DI float hi2f(unsigned v) { return __uint_as_float(v & 0xffff0000u); }

constexpr size_t WS_MOD = 0;
constexpr size_t WS_H = 65536;
constexpr size_t WS_Q = WS_H + (size_t)S * D * 2;
constexpr size_t WS_KF = WS_Q + (size_t)S * D * 2;
constexpr size_t WS_VF = WS_KF + 65536;
constexpr size_t WS_O = WS_VF + 69632;
constexpr size_t WS_X1 = WS_O + (size_t)S * D * 2;
constexpr size_t WS_HH = WS_X1 + (size_t)S * D * 2;
constexpr size_t WS_KN = WS_HH + (size_t)S * F * 2;

__global__ void __launch_bounds__(256) k_mod(const u16* __restrict__ c, const u16* __restrict__ w, float* __restrict__ mod) {
    __shared__ float red[64][33];
    __shared__ float sc[512];
    const int t = threadIdx.x;
    for (int i = t; i < 512; i += 256) { float v = bf2f(c[i]); sc[i] = v / (1.f + __expf(-v)); }
    __syncthreads();
    const int c8 = t & 3, kg = t >> 2, col0 = blockIdx.x * 32 + c8 * 8;
    float acc[8] = {};
#pragma unroll
    for (int i = 0; i < 8; ++i) {
        const int k = kg + 64 * i;
        const u32x4 v = *(const u32x4*)(w + (size_t)k * 3072 + col0);
        const float s = sc[k];
#pragma unroll
        for (int j = 0; j < 4; ++j) { acc[2 * j] += s * lo2f(v[j]); acc[2 * j + 1] += s * hi2f(v[j]); }
    }
#pragma unroll
    for (int j = 0; j < 8; ++j) red[kg][c8 * 8 + j] = acc[j];
    __syncthreads();
    if (t < 32) { float s = 0; for (int i = 0; i < 64; ++i) s += red[i][t]; mod[blockIdx.x * 32 + t] = s; }
}

__global__ void __launch_bounds__(256) k_ln(const u16* __restrict__ x, const float* __restrict__ sh, const float* __restrict__ sc, u16* __restrict__ out) {
    const int lane = threadIdx.x & 63, row = blockIdx.x * 4 + (threadIdx.x >> 6);
    const u32x4 v = *(const u32x4*)(x + (size_t)row * D + lane * 8);
    float f[8];
#pragma unroll
    for (int j = 0; j < 4; ++j) { f[2 * j] = lo2f(v[j]); f[2 * j + 1] = hi2f(v[j]); }
    float s = 0;
#pragma unroll
    for (int j = 0; j < 8; ++j) s += f[j];
    for (int o = 32; o; o >>= 1) s += __shfl_xor(s, o);
    const float m = s * (1.f / D);
    float q = 0;
#pragma unroll
    for (int j = 0; j < 8; ++j) { f[j] -= m; q += f[j] * f[j]; }
    for (int o = 32; o; o >>= 1) q += __shfl_xor(q, o);
    const float r = rsqrtf(q * (1.f / D) + 1e-5f);
    u32x4 w;
#pragma unroll
    for (int j = 0; j < 4; ++j) {
        const int cidx = lane * 8 + 2 * j;
        w[j] = pk2(f[2 * j] * r * (1.f + sc[cidx]) + sh[cidx], f[2 * j + 1] * r * (1.f + sc[cidx + 1]) + sh[cidx + 1]);
    }
    *(u32x4*)(out + (size_t)row * D + lane * 8) = w;
}

struct GArgs { const u16* A; const u16* W0; const u16* W1; const u16* W2; const u16* res; const float* gate; u16* out; char* ws; int K; int lda; };
template <int MODE>
__global__ void __launch_bounds__(256) k_gemm(GArgs g) {
    constexpr int LDT = 72;
    __shared__ __attribute__((aligned(16))) u16 sA[64 * LDT];
    __shared__ __attribute__((aligned(16))) u16 sW[64 * LDT];
    const int t = threadIdx.x, lane = t & 63, w = t >> 6, wm = w & 1, wn = w >> 1, h = lane >> 5, r32 = lane & 31;
    const int m0 = blockIdx.x * 64, by = blockIdx.y;
    f32x16 acc = {};
    for (int k0 = 0; k0 < g.K; k0 += 64) {
        for (int i = 0; i < 2; ++i) {
            const int ch = t + 256 * i, row = ch >> 3, cc = ch & 7;
            *(u32x4*)(sA + row * LDT + cc * 8) = *(const u32x4*)(g.A + (size_t)(m0 + row) * g.lda + k0 + cc * 8);
            const int k = k0 + row; u32x4 v;
            if (MODE == 0) {
                if (by < 8) v = *(const u32x4*)(g.W0 + (size_t)k * 512 + by * 64 + cc * 8);
                else if (cc < 2) v = *(const u32x4*)(g.W1 + (size_t)k * 16 + cc * 8);
                else if (cc < 4) v = *(const u32x4*)(g.W2 + (size_t)k * 16 + (cc - 2) * 8);
                else v = (u32x4){0, 0, 0, 0};
            } else if (MODE == 2) {
                const int grp = cc >> 2, isu = (cc >> 1) & 1, c8 = cc & 1;
                const u16* src = isu ? g.W1 : g.W0;
                v = *(const u32x4*)(src + (size_t)k * F + by * 32 + grp * 16 + c8 * 8);
            } else {
                v = *(const u32x4*)(g.W0 + (size_t)k * 512 + by * 64 + cc * 8);
            }
            *(u32x4*)(sW + row * LDT + cc * 8) = v;
        }
        __syncthreads();
#pragma unroll
        for (int ks = 0; ks < 4; ++ks) {
            const bf16x8 xb = *(const bf16x8*)(sA + (wm * 32 + r32) * LDT + ks * 16 + h * 8);
            const int blk = (lane >> 4) & 1, q = (lane & 15) >> 2, p = lane & 3;
            const LAS u16* wp = (const LAS u16*)sW + (ks * 16 + 8 * h + q) * LDT + wn * 32 + blk * 16 + p * 4;
            const s16x4 lo = __builtin_bit_cast(s16x4, __builtin_amdgcn_ds_read_tr16_b64_v4i16((LAS s16x4*)wp));
            const s16x4 hi = __builtin_bit_cast(s16x4, __builtin_amdgcn_ds_read_tr16_b64_v4i16((LAS s16x4*)(wp + 4 * LDT)));
            const bf16x8 wa = __builtin_shufflevector(lo, hi, 0, 1, 2, 3, 4, 5, 6, 7);
            acc = MFMA32(wa, xb, acc);
        }
        __syncthreads();
    }
    const int m = m0 + wm * 32 + r32;
    if (MODE == 0) {
        if (by < 8) {
#pragma unroll
            for (int gq = 0; gq < 4; ++gq) {
                const int n = by * 64 + wn * 32 + 8 * gq + 4 * h;
                u32x2 o = {pk2(acc[4 * gq] * QSCALE, acc[4 * gq + 1] * QSCALE), pk2(acc[4 * gq + 2] * QSCALE, acc[4 * gq + 3] * QSCALE)};
                *(u32x2*)((u16*)(g.ws + WS_Q) + (size_t)m * D + n) = o;
            }
        } else if (wn == 0) {
            u16* Kf = (u16*)(g.ws + WS_KF); u16* Vf = (u16*)(g.ws + WS_VF);
            const int tile = m >> 5, key = m & 31;
            float kn = 0;
#pragma unroll
            for (int r = 0; r < 8; ++r) {
                const int d = (r & 3) + 8 * (r >> 2) + 4 * h;
                const unsigned b = pk2(acc[r], 0.f) & 0xffff;
                const float kb = bf2f((u16)b); kn += kb * kb;
                Kf[tile * 512 + ((d >> 3) * 32 + key) * 8 + (d & 7)] = (u16)b;
            }
            kn += __shfl_xor(kn, 32);
            for (int o = 16; o; o >>= 1) kn = fmaxf(kn, __shfl_xor(kn, o));
            if (lane == 0) ((float*)(g.ws + WS_KN))[m >> 5] = kn;
            const int s = key >> 4, kk = key & 15, hh = (kk >> 2) & 1, j = (kk & 3) + 4 * (kk >> 3);
#pragma unroll
            for (int r = 8; r < 16; ++r) {
                const int d = (r & 3) + 8 * ((r >> 2) - 2) + 4 * h;
                Vf[(tile * 2 + s) * 264 + (hh * 16 + d) * 8 + j] = (u16)(pk2(acc[r], 0.f) & 0xffff);
            }
            if (kk == 0 && h == 0) { u32x4 ones = {0x3f803f80u, 0x3f803f80u, 0x3f803f80u, 0x3f803f80u}; *(u32x4*)(Vf + (tile * 2 + s) * 264 + 256) = ones; }
        }
    } else if (MODE == 2) {
#pragma unroll
        for (int gq = 0; gq < 2; ++gq) {
            float o[4];
#pragma unroll
            for (int e = 0; e < 4; ++e) { const float gv = acc[4 * gq + e], uv = acc[8 + 4 * gq + e]; o[e] = uv * __builtin_amdgcn_rcpf(1.f + __builtin_amdgcn_exp2f(-1.4426950408889634f * gv)); }
            u32x2 w2 = {pk2(o[0], o[1]), pk2(o[2], o[3])};
            *(u32x2*)(g.out + (size_t)m * F + by * 32 + wn * 16 + 8 * gq + 4 * h) = w2;
        }
    } else {
#pragma unroll
        for (int gq = 0; gq < 4; ++gq) {
            const int n = by * 64 + wn * 32 + 8 * gq + 4 * h;
            const u32x2 rv = *(const u32x2*)(g.res + (size_t)m * D + n);
            const f32x4 gt = *(const f32x4*)(g.gate + n);
            u32x2 o = {pk2(lo2f(rv[0]) + gt[0] * acc[4 * gq], hi2f(rv[0]) + gt[1] * acc[4 * gq + 1]), pk2(lo2f(rv[1]) + gt[2] * acc[4 * gq + 2], hi2f(rv[1]) + gt[3] * acc[4 * gq + 3])};
            *(u32x2*)(g.out + (size_t)m * D + n) = o;
        }
    }
}

constexpr int LDS_K = 0, LDS_V = 65536, LDS_ATT = 65536 + 128 * 528;
constexpr float SAFE_T = 64.f;
__global__ void __launch_bounds__(512, 2) k_attn(char* ws) {
    extern __shared__ __attribute__((aligned(16))) char lds[];
    const int t = threadIdx.x, lane = t & 63, w = __builtin_amdgcn_readfirstlane(t >> 6), h = lane >> 5, r32 = lane & 31;
    const int rb = blockIdx.x >> 3, hg = blockIdx.x & 7;
    {
        const u32x4* src = (const u32x4*)(ws + WS_KF);
        for (int i = t; i < 65536 / 16; i += 512) *(u32x4*)(lds + LDS_K + i * 16) = src[i];
        const u32x4* sv = (const u32x4*)(ws + WS_VF);
        for (int i = t; i < 128 * 528 / 16; i += 512) *(u32x4*)(lds + LDS_V + i * 16) = sv[i];
    }
    const int head = hg * 4 + (w & 3), kh = w >> 2;
    const u16* Q = (const u16*)(ws + WS_Q);
    bf16x8 qf[2];
#pragma unroll
    for (int qt = 0; qt < 2; ++qt) qf[qt] = *(const bf16x8*)(Q + (size_t)(rb * 64 + qt * 32 + r32) * D + head * 16 + h * 8);
    float kmax = ((const float*)(ws + WS_KN))[lane];
    for (int o = 32; o; o >>= 1) kmax = fmaxf(kmax, __shfl_xor(kmax, o));
    float qn = 0;
#pragma unroll
    for (int qt = 0; qt < 2; ++qt) { float s = 0;
#pragma unroll
        for (int j = 0; j < 8; ++j) { const float v = bf2f((u16)qf[qt][j]); s += v * v; }
        s += __shfl_xor(s, 32); qn = fmaxf(qn, s); }
    const bool safe = __all(qn * kmax * 1.05f <= SAFE_T * SAFE_T);
    __syncthreads();
    f32x16 O[2] = {};
    float mrun[2] = {0.f, 0.f};
    const int vslot = (r32 == 16 || r32 == 20) ? 32 : (h * 16 + (r32 & 15));
    const char* kp = lds + LDS_K + kh * 32768 + lane * 16;
    const char* vp = lds + LDS_V + kh * 64 * 528 + vslot * 16;
    if (safe) {
        const f32x16 zero = {};
#pragma unroll 2
        for (int tt = 0; tt < 32; ++tt) {
            const bf16x8 kf = *(const bf16x8*)(kp + tt * 1024);
            const bf16x8 v0 = *(const bf16x8*)(vp + tt * 1056);
            const bf16x8 v1 = *(const bf16x8*)(vp + tt * 1056 + 528);
#pragma unroll
            for (int qt = 0; qt < 2; ++qt) {
                f32x16 s = MFMA32(kf, qf[qt], zero);
                u32x4 p0, p1;
#pragma unroll
                for (int j = 0; j < 4; ++j) {
                    p0[j] = pk2(__builtin_amdgcn_exp2f(s[2 * j]), __builtin_amdgcn_exp2f(s[2 * j + 1]));
                    p1[j] = pk2(__builtin_amdgcn_exp2f(s[8 + 2 * j]), __builtin_amdgcn_exp2f(s[8 + 2 * j + 1]));
                }
                O[qt] = MFMA32(v0, __builtin_bit_cast(bf16x8, p0), O[qt]);
                O[qt] = MFMA32(v1, __builtin_bit_cast(bf16x8, p1), O[qt]);
            }
        }
    } else {
        mrun[0] = mrun[1] = -INFINITY;
        const f32x16 zero = {};
        for (int tt = 0; tt < 32; ++tt) {
            const bf16x8 kf = *(const bf16x8*)(kp + tt * 1024);
            const bf16x8 v0 = *(const bf16x8*)(vp + tt * 1056);
            const bf16x8 v1 = *(const bf16x8*)(vp + tt * 1056 + 528);
#pragma unroll
            for (int qt = 0; qt < 2; ++qt) {
                f32x16 s = MFMA32(kf, qf[qt], zero);
                float mx = s[0];
#pragma unroll
                for (int j = 1; j < 16; ++j) mx = fmaxf(mx, s[j]);
                mx = fmaxf(mx, __shfl_xor(mx, 32));
                const float mn = fmaxf(mrun[qt], mx);
                const float al = __builtin_amdgcn_exp2f(mrun[qt] - mn);
                mrun[qt] = mn;
#pragma unroll
                for (int j = 0; j < 16; ++j) O[qt][j] *= al;
                u32x4 p0, p1;
#pragma unroll
                for (int j = 0; j < 4; ++j) {
                    p0[j] = pk2(__builtin_amdgcn_exp2f(s[2 * j] - mn), __builtin_amdgcn_exp2f(s[2 * j + 1] - mn));
                    p1[j] = pk2(__builtin_amdgcn_exp2f(s[8 + 2 * j] - mn), __builtin_amdgcn_exp2f(s[8 + 2 * j + 1] - mn));
                }
                O[qt] = MFMA32(v0, __builtin_bit_cast(bf16x8, p0), O[qt]);
                O[qt] = MFMA32(v1, __builtin_bit_cast(bf16x8, p1), O[qt]);
            }
        }
    }
    __syncthreads();
    float* red = (float*)lds;
    if (kh == 1) {
#pragma unroll
        for (int qt = 0; qt < 2; ++qt) {
#pragma unroll
            for (int r = 0; r < 9; ++r) red[(((w & 3) * 2 + qt) * 10 + r) * 64 + lane] = O[qt][r];
            red[(((w & 3) * 2 + qt) * 10 + 9) * 64 + lane] = mrun[qt];
        }
    }
    __syncthreads();
    if (kh == 0) {
        u16* Oo = (u16*)(ws + WS_O);
#pragma unroll
        for (int qt = 0; qt < 2; ++qt) {
            const float m2 = red[(((w & 3) * 2 + qt) * 10 + 9) * 64 + lane];
            const float mn = fmaxf(mrun[qt], m2);
            const float a1 = __builtin_amdgcn_exp2f(mrun[qt] - mn), a2 = __builtin_amdgcn_exp2f(m2 - mn);
            float v[9];
#pragma unroll
            for (int r = 0; r < 9; ++r) v[r] = O[qt][r] * a1 + red[(((w & 3) * 2 + qt) * 10 + r) * 64 + lane] * a2;
            const float il = 1.f / v[8];
            const int row = rb * 64 + qt * 32 + r32;
#pragma unroll
            for (int gq = 0; gq < 2; ++gq) {
                u32x2 o = {pk2(v[4 * gq] * il, v[4 * gq + 1] * il), pk2(v[4 * gq + 2] * il, v[4 * gq + 3] * il)};
                *(u32x2*)(Oo + (size_t)row * D + head * 16 + 8 * gq + 4 * h) = o;
            }
        }
    }
}

extern "C" void kernel_launch(void* const* d_in, const int* in_sizes, int n_in, void* d_out, int out_size, void* d_ws, size_t ws_size, hipStream_t stream) {
    const u16* x = (const u16*)d_in[0];
    const u16 *wq = (const u16*)d_in[1], *wk = (const u16*)d_in[2], *wv = (const u16*)d_in[3], *wo = (const u16*)d_in[4];
    const u16 *wg = (const u16*)d_in[6], *wu = (const u16*)d_in[7], *wd = (const u16*)d_in[8];
    const u16 *c = (const u16*)d_in[9], *wm = (const u16*)d_in[10];
    char* ws = (char*)d_ws;
    float* mod = (float*)(ws + WS_MOD);
    u16* hbuf = (u16*)(ws + WS_H);
    k_mod<<<96, 256, 0, stream>>>(c, wm, mod);
    k_ln<<<S / 4, 256, 0, stream>>>(x, mod + 0, mod + 512, hbuf);
    { GArgs g{hbuf, wq, wk, wv, nullptr, nullptr, nullptr, ws, 512, 512}; k_gemm<0><<<dim3(32, 9), 256, 0, stream>>>(g); }
    hipFuncSetAttribute((const void*)k_attn, hipFuncAttributeMaxDynamicSharedMemorySize, LDS_ATT);
    k_attn<<<256, 512, LDS_ATT, stream>>>(ws);
    { GArgs g{(const u16*)(ws + WS_O), wo, nullptr, nullptr, x, mod + 1024, (u16*)(ws + WS_X1), ws, 512, 512}; k_gemm<1><<<dim3(32, 8), 256, 0, stream>>>(g); }
    k_ln<<<S / 4, 256, 0, stream>>>((const u16*)(ws + WS_X1), mod + 1536, mod + 2048, hbuf);
    { GArgs g{hbuf, wg, wu, nullptr, nullptr, nullptr, (u16*)(ws + WS_HH), ws, 512, 512}; k_gemm<2><<<dim3(32, 56), 256, 0, stream>>>(g); }
    { GArgs g{(const u16*)(ws + WS_HH), wd, nullptr, nullptr, (const u16*)(ws + WS_X1), mod + 2560, (u16*)d_out, ws, F, F}; k_gemm<3><<<dim3(32, 8), 256, 0, stream>>>(g); }
}
```

```cpp
#include <hip/hip_runtime.h>
#include <stdint.h>

struct KArgs { const void* x; const void* wq; const void* wk; const void* wv; const void* wo; const void* wg; const void* wu; const void* wd;
               const void* c; const void* wm; void* out; void* ws; };

__global__ void __launch_bounds__(256, 1) fused_block(KArgs a) {
    unsigned tid = threadIdx.x;
    asm volatile(
R"ASM(
s_mov_b64 s[8:9], %1
s_mov_b32 s10, %2
s_load_dwordx8 s[44:51], s[8:9], 0x0
s_load_dwordx8 s[52:59], s[8:9], 0x20
s_load_dwordx8 s[60:67], s[8:9], 0x40
v_mov_b32 v0, %0
s_getpc_b64 s[78:79]
LPC_%=:
s_lshr_b32 s80, s10, 3
s_lshl_b32 s80, s80, 14
v_lshlrev_b32 v5, 6, v0
v_add_u32 v5, s80, v5
s_mov_b32 s81, LCODEEND_%=-LPC_%=
v_cmp_gt_u32 vcc, s81, v5
s_mov_b64 exec, vcc
s_cbranch_execz Lnocpf_1_%=
global_load_dword v6, v5, s[78:79]
Lnocpf_1_%=:
s_mov_b64 exec, -1
v_and_b32 v1, 63, v0
v_lshrrev_b32 v2, 6, v0
v_lshlrev_b32 v3, 4, v1
v_readfirstlane_b32 s13, v2
v_lshrrev_b32 v2, 5, v1
v_and_b32 v4, 31, v1
s_and_b32 s11, s10, 7
s_lshr_b32 s12, s10, 3
s_lshr_b32 s80, s12, 3
s_lshl_b32 s14, s11, 2
s_add_u32 s14, s14, s80
s_and_b32 s15, s12, 7
s_lshr_b32 s16, s11, 1
)ASM"
R"ASM(s_lshl_b32 s16, s16, 2
s_add_u32 s16, s16, s80
s_and_b32 s17, s11, 1
s_lshl_b32 s17, s17, 3
s_add_u32 s17, s17, s15
s_and_b32 s100, s13, 1
s_lshr_b32 s101, s13, 1
s_waitcnt lgkmcnt(0)
s_load_dwordx2 s[18:19], s[66:67], 0x0
s_mov_b32 s20, s66
s_and_b32 s21, s67, 0xffff
s_mov_b32 s22, 0x2000000
s_mov_b32 s23, 0x00020000
s_mov_b32 s32, s60
s_and_b32 s33, s61, 0xffff
s_mov_b32 s34, 0x400
s_mov_b32 s35, 0x00020000
s_mov_b32 s36, s62
s_and_b32 s37, s63, 0xffff
s_mov_b32 s38, 0x300000
s_mov_b32 s39, 0x00020000
s_mov_b32 s24, s44
s_and_b32 s25, s45, 0xffff
s_mov_b32 s26, 0x200000
s_mov_b32 s27, 0x00020000
s_mov_b32 s40, s46
s_and_b32 s41, s47, 0xffff
s_mov_b32 s42, 0x80000
s_mov_b32 s43, 0x00020000
s_bitcmp1_b32 s15, 0
s_cselect_b32 s80, s50, s48
s_cselect_b32 s81, s51, s49
)ASM"
R"ASM(s_mov_b32 s68, s80
s_and_b32 s69, s81, 0xffff
s_mov_b32 s70, 0x4000
s_mov_b32 s71, 0x00020000
s_mov_b32 s28, s64
s_and_b32 s29, s65, 0xffff
s_mov_b32 s30, 0x200000
s_mov_b32 s31, 0x00020000
s_lshr_b32 s80, s12, 2
s_lshl_b32 s80, s80, 5
s_lshl_b32 s81, s11, 2
s_add_u32 s80, s80, s81
s_and_b32 s81, s12, 3
s_add_u32 s97, s80, s81
s_cmp_ge_u32 s12, 8
s_cbranch_scc1 Lnold_2_%=
s_lshr_b32 s80, s12, 1
s_lshl_b32 s80, s80, 2
s_add_u32 s80, s80, s13
s_mul_i32 s81, s80, 64
v_lshrrev_b32 v26, 3, v1
v_mul_u32_u24 v27, 8, v26
buffer_load_dwordx2 v[8:9], v27, s[32:35], s81 offen
v_mul_u32_u24 v26, 24576, v26
v_and_b32 v27, 7, v1
v_lshl_add_u32 v26, v27, 4, v26
s_mul_i32 s80, s80, 196608
s_and_b32 s81, s12, 1
s_lshl_b32 s82, s11, 1
s_add_u32 s81, s81, s82
s_lshl_b32 s81, s81, 7
s_add_u32 s80, s80, s81
)ASM"
R"ASM(buffer_load_dwordx4 v[28:31], v26, s[36:39], s80 offen
s_add_u32 s80, s80, 6144
buffer_load_dwordx4 v[32:35], v26, s[36:39], s80 offen
s_add_u32 s80, s80, 6144
buffer_load_dwordx4 v[36:39], v26, s[36:39], s80 offen
s_add_u32 s80, s80, 6144
buffer_load_dwordx4 v[40:43], v26, s[36:39], s80 offen
Lnold_2_%=:
s_cmp_lt_u32 s12, 24
s_cbranch_scc1 Lnold_3_%=
s_bfe_u32 s80, s12, 0x10002
s_lshl_b32 s80, s80, 2
s_add_u32 s80, s80, s13
s_mul_i32 s81, s80, 128
v_lshrrev_b32 v26, 3, v1
v_mul_u32_u24 v27, 16, v26
buffer_load_dwordx4 v[8:11], v27, s[32:35], s81 offen
v_mul_u32_u24 v26, 49152, v26
v_and_b32 v27, 7, v1
v_lshl_add_u32 v26, v27, 4, v26
s_mul_i32 s80, s80, 393216
s_and_b32 s81, s12, 3
s_lshl_b32 s82, s11, 2
s_add_u32 s82, s82, 16
s_add_u32 s81, s81, s82
s_lshl_b32 s81, s81, 7
s_add_u32 s80, s80, s81
buffer_load_dwordx4 v[28:31], v26, s[36:39], s80 offen
s_add_u32 s80, s80, 6144
buffer_load_dwordx4 v[32:35], v26, s[36:39], s80 offen
s_add_u32 s80, s80, 6144
buffer_load_dwordx4 v[36:39], v26, s[36:39], s80 offen
)ASM"
R"ASM(s_add_u32 s80, s80, 6144
buffer_load_dwordx4 v[40:43], v26, s[36:39], s80 offen
s_add_u32 s80, s80, 6144
buffer_load_dwordx4 v[44:47], v26, s[36:39], s80 offen
s_add_u32 s80, s80, 6144
buffer_load_dwordx4 v[48:51], v26, s[36:39], s80 offen
s_add_u32 s80, s80, 6144
buffer_load_dwordx4 v[52:55], v26, s[36:39], s80 offen
s_add_u32 s80, s80, 6144
buffer_load_dwordx4 v[56:59], v26, s[36:39], s80 offen
Lnold_3_%=:
s_lshl_b32 s80, s14, 16
s_lshl_b32 s81, s13, 14
s_add_u32 s80, s80, s81
buffer_load_dwordx4 v[60:63], v3, s[24:27], s80 offen offset:0
buffer_load_dwordx4 v[64:67], v3, s[24:27], s80 offen offset:1024
buffer_load_dwordx4 v[68:71], v3, s[24:27], s80 offen offset:2048
buffer_load_dwordx4 v[72:75], v3, s[24:27], s80 offen offset:3072
s_add_u32 s80, s80, 4096
buffer_load_dwordx4 v[76:79], v3, s[24:27], s80 offen offset:0
buffer_load_dwordx4 v[80:83], v3, s[24:27], s80 offen offset:1024
buffer_load_dwordx4 v[84:87], v3, s[24:27], s80 offen offset:2048
buffer_load_dwordx4 v[88:91], v3, s[24:27], s80 offen offset:3072
s_add_u32 s80, s80, 4096
buffer_load_dwordx4 v[92:95], v3, s[24:27], s80 offen offset:0
buffer_load_dwordx4 v[96:99], v3, s[24:27], s80 offen offset:1024
buffer_load_dwordx4 v[100:103], v3, s[24:27], s80 offen offset:2048
buffer_load_dwordx4 v[104:107], v3, s[24:27], s80 offen offset:3072
s_add_u32 s80, s80, 4096
buffer_load_dwordx4 v[108:111], v3, s[24:27], s80 offen offset:0
buffer_load_dwordx4 v[112:115], v3, s[24:27], s80 offen offset:1024
buffer_load_dwordx4 v[116:119], v3, s[24:27], s80 offen offset:2048
)ASM"
R"ASM(buffer_load_dwordx4 v[120:123], v3, s[24:27], s80 offen offset:3072
s_cmp_ge_u32 s12, 21
s_cbranch_scc1 Ltwo_5_%=
s_cmp_ge_u32 s12, 14
s_cbranch_scc1 Ltw2_4_%=
s_mul_i32 s96, s11, 14
s_add_u32 s96, s96, s12
s_mov_b32 s32, s54
s_and_b32 s33, s55, 0xffff
s_mov_b32 s34, 0x1c0000
s_mov_b32 s35, 0x00020000
s_mov_b32 s36, s56
s_and_b32 s37, s57, 0xffff
s_mov_b32 s38, 0x1c0000
s_mov_b32 s39, 0x00020000
s_mov_b32 s91, 3584
s_mov_b32 s93, 0
s_mov_b32 s94, 57344
s_and_b32 s80, s96, 3
s_lshl_b32 s81, s80, 2
s_add_u32 s81, s81, s13
s_mul_i32 s92, s81, 114688
s_lshr_b32 s82, s96, 2
s_lshl_b32 s83, s82, 7
s_add_u32 s92, s92, s83
s_lshl_b32 s95, s82, 17
s_lshl_b32 s81, s81, 11
s_add_u32 s95, s95, s81
s_add_u32 s95, s95, 0xc00000
s_branch Ltdone_6_%=
Ltw2_4_%=:
s_sub_u32 s80, s12, 14
)ASM"
R"ASM(s_mov_b32 s32, s58
s_and_b32 s33, s59, 0xffff
s_mov_b32 s34, 0x1c0000
s_mov_b32 s35, 0x00020000
s_mov_b32 s36, s58
s_and_b32 s37, s59, 0xffff
s_mov_b32 s38, 0x1c0000
s_mov_b32 s39, 0x00020000
s_mov_b32 s91, 1024
s_mov_b32 s93, 114688
s_mov_b32 s94, 229376
s_lshl_b32 s92, s80, 14
s_lshl_b32 s81, s11, 7
s_add_u32 s92, s92, s81
s_mul_i32 s81, s13, 458752
s_add_u32 s92, s92, s81
s_mul_i32 s96, s11, 7
s_add_u32 s96, s96, s80
s_lshl_b32 s95, s96, 15
s_lshl_b32 s81, s13, 13
s_add_u32 s95, s95, s81
s_add_u32 s95, s95, 0x1000000
s_add_u32 s96, s96, 112
s_branch Ltdone_6_%=
Ltwo_5_%=:
s_sub_u32 s80, s12, 21
s_mov_b32 s32, s52
s_and_b32 s33, s53, 0xffff
s_mov_b32 s34, 0x80000
s_mov_b32 s35, 0x00020000
s_mov_b32 s36, s52
s_and_b32 s37, s53, 0xffff
)ASM"
R"ASM(s_mov_b32 s38, 0x80000
s_mov_b32 s39, 0x00020000
s_mov_b32 s91, 1024
s_mov_b32 s93, 16384
s_mov_b32 s94, 32768
s_lshl_b32 s81, s80, 2
s_add_u32 s81, s81, s13
s_lshl_b32 s92, s81, 16
s_lshl_b32 s82, s11, 7
s_add_u32 s92, s92, s82
s_lshl_b32 s95, s11, 16
s_lshl_b32 s81, s81, 12
s_add_u32 s95, s95, s81
s_add_u32 s95, s95, 0x1200000
s_lshl_b32 s96, s11, 1
s_add_u32 s96, s96, s80
s_add_u32 s96, s96, 168
s_cmp_ge_u32 s12, 23
s_cselect_b32 s34, 0, s34
s_cselect_b32 s38, 0, s38
Ltdone_6_%=:
s_waitcnt lgkmcnt(0)
s_mul_hi_u32 s80, s18, 0x4c957f2d
s_mul_i32 s81, s18, 0x5851f42d
s_mul_i32 s82, s19, 0x4c957f2d
s_mul_i32 s18, s18, 0x4c957f2d
s_add_u32 s80, s80, s81
s_add_u32 s80, s80, s82
s_add_u32 s18, s18, 0xf767814f
s_addc_u32 s19, s80, 0x14057b7e
s_cmp_ge_u32 s12, 8
s_cbranch_scc1 Lnostrip2_7_%=
)ASM"
R"ASM(s_waitcnt vmcnt(16)
v_lshlrev_b32 v12, 16, v8
v_and_b32 v13, 0xffff0000, v8
v_lshlrev_b32 v14, 16, v9
v_and_b32 v15, 0xffff0000, v9
v_mul_f32 v132, 0xbfb8aa3b, v12
v_mul_f32 v133, 0xbfb8aa3b, v13
v_mul_f32 v134, 0xbfb8aa3b, v14
v_mul_f32 v135, 0xbfb8aa3b, v15
v_exp_f32 v132, v132
v_exp_f32 v133, v133
v_exp_f32 v134, v134
v_exp_f32 v135, v135
v_add_f32 v132, 1.0, v132
v_add_f32 v133, 1.0, v133
v_add_f32 v134, 1.0, v134
v_add_f32 v135, 1.0, v135
v_rcp_f32 v132, v132
v_rcp_f32 v133, v133
v_rcp_f32 v134, v134
v_rcp_f32 v135, v135
v_mul_f32 v12, v12, v132
v_mul_f32 v13, v13, v133
v_mul_f32 v14, v14, v134
v_mul_f32 v15, v15, v135
v_lshlrev_b32 v16, 16, v28
v_and_b32 v17, 0xffff0000, v28
v_lshlrev_b32 v18, 16, v29
v_and_b32 v19, 0xffff0000, v29
v_lshlrev_b32 v20, 16, v30
v_and_b32 v21, 0xffff0000, v30
v_lshlrev_b32 v22, 16, v31
)ASM"
R"ASM(v_and_b32 v23, 0xffff0000, v31
v_pk_mul_f32 v[124:125], v[16:17], v[12:13] op_sel_hi:[1,0]
v_pk_mul_f32 v[126:127], v[18:19], v[12:13] op_sel_hi:[1,0]
v_pk_mul_f32 v[128:129], v[20:21], v[12:13] op_sel_hi:[1,0]
v_pk_mul_f32 v[130:131], v[22:23], v[12:13] op_sel_hi:[1,0]
v_lshlrev_b32 v16, 16, v32
v_and_b32 v17, 0xffff0000, v32
v_lshlrev_b32 v18, 16, v33
v_and_b32 v19, 0xffff0000, v33
v_lshlrev_b32 v20, 16, v34
v_and_b32 v21, 0xffff0000, v34
v_lshlrev_b32 v22, 16, v35
v_and_b32 v23, 0xffff0000, v35
v_pk_fma_f32 v[124:125], v[16:17], v[12:13], v[124:125] op_sel:[0,1,0] op_sel_hi:[1,1,1]
v_pk_fma_f32 v[126:127], v[18:19], v[12:13], v[126:127] op_sel:[0,1,0] op_sel_hi:[1,1,1]
v_pk_fma_f32 v[128:129], v[20:21], v[12:13], v[128:129] op_sel:[0,1,0] op_sel_hi:[1,1,1]
v_pk_fma_f32 v[130:131], v[22:23], v[12:13], v[130:131] op_sel:[0,1,0] op_sel_hi:[1,1,1]
v_lshlrev_b32 v16, 16, v36
v_and_b32 v17, 0xffff0000, v36
v_lshlrev_b32 v18, 16, v37
v_and_b32 v19, 0xffff0000, v37
v_lshlrev_b32 v20, 16, v38
v_and_b32 v21, 0xffff0000, v38
v_lshlrev_b32 v22, 16, v39
v_and_b32 v23, 0xffff0000, v39
v_pk_fma_f32 v[124:125], v[16:17], v[14:15], v[124:125] op_sel_hi:[1,0,1]
v_pk_fma_f32 v[126:127], v[18:19], v[14:15], v[126:127] op_sel_hi:[1,0,1]
v_pk_fma_f32 v[128:129], v[20:21], v[14:15], v[128:129] op_sel_hi:[1,0,1]
v_pk_fma_f32 v[130:131], v[22:23], v[14:15], v[130:131] op_sel_hi:[1,0,1]
v_lshlrev_b32 v16, 16, v40
v_and_b32 v17, 0xffff0000, v40
v_lshlrev_b32 v18, 16, v41
)ASM"
R"ASM(v_and_b32 v19, 0xffff0000, v41
v_lshlrev_b32 v20, 16, v42
v_and_b32 v21, 0xffff0000, v42
v_lshlrev_b32 v22, 16, v43
v_and_b32 v23, 0xffff0000, v43
v_pk_fma_f32 v[124:125], v[16:17], v[14:15], v[124:125] op_sel:[0,1,0] op_sel_hi:[1,1,1]
v_pk_fma_f32 v[126:127], v[18:19], v[14:15], v[126:127] op_sel:[0,1,0] op_sel_hi:[1,1,1]
v_pk_fma_f32 v[128:129], v[20:21], v[14:15], v[128:129] op_sel:[0,1,0] op_sel_hi:[1,1,1]
v_pk_fma_f32 v[130:131], v[22:23], v[14:15], v[130:131] op_sel:[0,1,0] op_sel_hi:[1,1,1]
s_nop 0
v_permlane32_swap_b32 v124, v128
v_permlane32_swap_b32 v125, v129
v_permlane32_swap_b32 v126, v130
v_permlane32_swap_b32 v127, v131
v_add_f32 v124, v124, v128
v_add_f32 v125, v125, v129
v_add_f32 v126, v126, v130
v_add_f32 v127, v127, v131
s_nop 0
v_permlane16_swap_b32 v124, v126
v_permlane16_swap_b32 v125, v127
v_add_f32 v124, v124, v126
v_add_f32 v125, v125, v127
s_nop 0
v_add_f32_dpp v124, v124, v124 row_ror:8 row_mask:0xf bank_mask:0xf
v_add_f32_dpp v125, v125, v125 row_ror:8 row_mask:0xf bank_mask:0xf
v_and_b32 v26, 7, v1
v_lshlrev_b32 v26, 5, v26
v_lshrrev_b32 v27, 4, v1
v_lshlrev_b32 v27, 3, v27
v_add_u32 v26, v26, v27
s_lshl_b32 s80, s13, 8
)ASM"
R"ASM(s_add_u32 s80, s80, 161792
v_add_u32 v26, s80, v26
s_mov_b32 exec_lo, 0x00ff00ff
s_mov_b32 exec_hi, 0x00ff00ff
ds_write_b64 v26, v[124:125]
s_mov_b64 exec, -1
s_waitcnt lgkmcnt(0)
s_barrier
s_cmp_lg_u32 s13, 0
s_cbranch_scc1 Lnopub_8_%=
v_and_b32 v26, 63, v1
v_lshlrev_b32 v26, 2, v26
v_add_u32 v27, 161792, v26
ds_read_b32 v132, v27 offset:0
ds_read_b32 v133, v27 offset:256
ds_read_b32 v134, v27 offset:512
ds_read_b32 v135, v27 offset:768
s_waitcnt lgkmcnt(2)
v_add_f32 v132, v132, v133
s_waitcnt lgkmcnt(0)
v_add_f32 v134, v134, v135
v_add_f32 v132, v132, v134
v_add_u32 v132, 0x80, v132
v_and_b32 v132, 0xffffff00, v132
s_and_b32 s80, s18, 0xff
v_or_b32 v132, s80, v132
v_mov_b32 v133, s19
v_lshlrev_b32 v26, 1, v26
s_lshr_b32 s80, s12, 1
s_lshl_b32 s80, s80, 13
s_and_b32 s81, s12, 1
s_lshl_b32 s82, s11, 1
)ASM"
R"ASM(s_add_u32 s81, s81, s82
s_lshl_b32 s81, s81, 9
s_add_u32 s80, s80, s81
s_add_u32 s80, s80, 0x1280000
buffer_store_dwordx2 v[132:133], v26, s[20:23], s80 offen sc1
s_mov_b64 exec, -1
Lnopub_8_%=:
Lnostrip2_7_%=:
s_cmp_lt_u32 s12, 24
s_cbranch_scc1 Lnostrip2_9_%=
s_waitcnt vmcnt(16)
v_lshlrev_b32 v12, 16, v8
v_and_b32 v13, 0xffff0000, v8
v_lshlrev_b32 v14, 16, v9
v_and_b32 v15, 0xffff0000, v9
v_lshlrev_b32 v16, 16, v10
v_and_b32 v17, 0xffff0000, v10
v_lshlrev_b32 v18, 16, v11
v_and_b32 v19, 0xffff0000, v11
v_mul_f32 v140, 0xbfb8aa3b, v12
v_mul_f32 v141, 0xbfb8aa3b, v13
v_mul_f32 v142, 0xbfb8aa3b, v14
v_mul_f32 v143, 0xbfb8aa3b, v15
v_mul_f32 v144, 0xbfb8aa3b, v16
v_mul_f32 v145, 0xbfb8aa3b, v17
v_mul_f32 v146, 0xbfb8aa3b, v18
v_mul_f32 v147, 0xbfb8aa3b, v19
v_exp_f32 v140, v140
v_exp_f32 v141, v141
v_exp_f32 v142, v142
v_exp_f32 v143, v143
v_exp_f32 v144, v144
)ASM"
R"ASM(v_exp_f32 v145, v145
v_exp_f32 v146, v146
v_exp_f32 v147, v147
v_add_f32 v140, 1.0, v140
v_add_f32 v141, 1.0, v141
v_add_f32 v142, 1.0, v142
v_add_f32 v143, 1.0, v143
v_add_f32 v144, 1.0, v144
v_add_f32 v145, 1.0, v145
v_add_f32 v146, 1.0, v146
v_add_f32 v147, 1.0, v147
v_rcp_f32 v140, v140
v_rcp_f32 v141, v141
v_rcp_f32 v142, v142
v_rcp_f32 v143, v143
v_rcp_f32 v144, v144
v_rcp_f32 v145, v145
v_rcp_f32 v146, v146
v_rcp_f32 v147, v147
v_mul_f32 v12, v12, v140
v_mul_f32 v13, v13, v141
v_mul_f32 v14, v14, v142
v_mul_f32 v15, v15, v143
v_mul_f32 v16, v16, v144
v_mul_f32 v17, v17, v145
v_mul_f32 v18, v18, v146
v_mul_f32 v19, v19, v147
v_lshlrev_b32 v124, 16, v28
v_and_b32 v125, 0xffff0000, v28
v_lshlrev_b32 v126, 16, v29
v_and_b32 v127, 0xffff0000, v29
v_lshlrev_b32 v128, 16, v30
)ASM"
R"ASM(v_and_b32 v129, 0xffff0000, v30
v_lshlrev_b32 v130, 16, v31
v_and_b32 v131, 0xffff0000, v31
v_pk_mul_f32 v[132:133], v[124:125], v[12:13] op_sel_hi:[1,0]
v_pk_mul_f32 v[134:135], v[126:127], v[12:13] op_sel_hi:[1,0]
v_pk_mul_f32 v[136:137], v[128:129], v[12:13] op_sel_hi:[1,0]
v_pk_mul_f32 v[138:139], v[130:131], v[12:13] op_sel_hi:[1,0]
v_lshlrev_b32 v124, 16, v32
v_and_b32 v125, 0xffff0000, v32
v_lshlrev_b32 v126, 16, v33
v_and_b32 v127, 0xffff0000, v33
v_lshlrev_b32 v128, 16, v34
v_and_b32 v129, 0xffff0000, v34
v_lshlrev_b32 v130, 16, v35
v_and_b32 v131, 0xffff0000, v35
v_pk_fma_f32 v[132:133], v[124:125], v[12:13], v[132:133] op_sel:[0,1,0] op_sel_hi:[1,1,1]
v_pk_fma_f32 v[134:135], v[126:127], v[12:13], v[134:135] op_sel:[0,1,0] op_sel_hi:[1,1,1]
v_pk_fma_f32 v[136:137], v[128:129], v[12:13], v[136:137] op_sel:[0,1,0] op_sel_hi:[1,1,1]
v_pk_fma_f32 v[138:139], v[130:131], v[12:13], v[138:139] op_sel:[0,1,0] op_sel_hi:[1,1,1]
v_lshlrev_b32 v124, 16, v36
v_and_b32 v125, 0xffff0000, v36
v_lshlrev_b32 v126, 16, v37
v_and_b32 v127, 0xffff0000, v37
v_lshlrev_b32 v128, 16, v38
v_and_b32 v129, 0xffff0000, v38
v_lshlrev_b32 v130, 16, v39
v_and_b32 v131, 0xffff0000, v39
v_pk_fma_f32 v[132:133], v[124:125], v[14:15], v[132:133] op_sel_hi:[1,0,1]
v_pk_fma_f32 v[134:135], v[126:127], v[14:15], v[134:135] op_sel_hi:[1,0,1]
v_pk_fma_f32 v[136:137], v[128:129], v[14:15], v[136:137] op_sel_hi:[1,0,1]
v_pk_fma_f32 v[138:139], v[130:131], v[14:15], v[138:139] op_sel_hi:[1,0,1]
v_lshlrev_b32 v124, 16, v40
)ASM"
R"ASM(v_and_b32 v125, 0xffff0000, v40
v_lshlrev_b32 v126, 16, v41
v_and_b32 v127, 0xffff0000, v41
v_lshlrev_b32 v128, 16, v42
v_and_b32 v129, 0xffff0000, v42
v_lshlrev_b32 v130, 16, v43
v_and_b32 v131, 0xffff0000, v43
v_pk_fma_f32 v[132:133], v[124:125], v[14:15], v[132:133] op_sel:[0,1,0] op_sel_hi:[1,1,1]
v_pk_fma_f32 v[134:135], v[126:127], v[14:15], v[134:135] op_sel:[0,1,0] op_sel_hi:[1,1,1]
v_pk_fma_f32 v[136:137], v[128:129], v[14:15], v[136:137] op_sel:[0,1,0] op_sel_hi:[1,1,1]
v_pk_fma_f32 v[138:139], v[130:131], v[14:15], v[138:139] op_sel:[0,1,0] op_sel_hi:[1,1,1]
v_lshlrev_b32 v124, 16, v44
v_and_b32 v125, 0xffff0000, v44
v_lshlrev_b32 v126, 16, v45
v_and_b32 v127, 0xffff0000, v45
v_lshlrev_b32 v128, 16, v46
v_and_b32 v129, 0xffff0000, v46
v_lshlrev_b32 v130, 16, v47
v_and_b32 v131, 0xffff0000, v47
v_pk_fma_f32 v[132:133], v[124:125], v[16:17], v[132:133] op_sel_hi:[1,0,1]
v_pk_fma_f32 v[134:135], v[126:127], v[16:17], v[134:135] op_sel_hi:[1,0,1]
v_pk_fma_f32 v[136:137], v[128:129], v[16:17], v[136:137] op_sel_hi:[1,0,1]
v_pk_fma_f32 v[138:139], v[130:131], v[16:17], v[138:139] op_sel_hi:[1,0,1]
v_lshlrev_b32 v124, 16, v48
v_and_b32 v125, 0xffff0000, v48
v_lshlrev_b32 v126, 16, v49
v_and_b32 v127, 0xffff0000, v49
v_lshlrev_b32 v128, 16, v50
v_and_b32 v129, 0xffff0000, v50
v_lshlrev_b32 v130, 16, v51
v_and_b32 v131, 0xffff0000, v51
v_pk_fma_f32 v[132:133], v[124:125], v[16:17], v[132:133] op_sel:[0,1,0] op_sel_hi:[1,1,1]
)ASM"
R"ASM(v_pk_fma_f32 v[134:135], v[126:127], v[16:17], v[134:135] op_sel:[0,1,0] op_sel_hi:[1,1,1]
v_pk_fma_f32 v[136:137], v[128:129], v[16:17], v[136:137] op_sel:[0,1,0] op_sel_hi:[1,1,1]
v_pk_fma_f32 v[138:139], v[130:131], v[16:17], v[138:139] op_sel:[0,1,0] op_sel_hi:[1,1,1]
v_lshlrev_b32 v124, 16, v52
v_and_b32 v125, 0xffff0000, v52
v_lshlrev_b32 v126, 16, v53
v_and_b32 v127, 0xffff0000, v53
v_lshlrev_b32 v128, 16, v54
v_and_b32 v129, 0xffff0000, v54
v_lshlrev_b32 v130, 16, v55
v_and_b32 v131, 0xffff0000, v55
v_pk_fma_f32 v[132:133], v[124:125], v[18:19], v[132:133] op_sel_hi:[1,0,1]
v_pk_fma_f32 v[134:135], v[126:127], v[18:19], v[134:135] op_sel_hi:[1,0,1]
v_pk_fma_f32 v[136:137], v[128:129], v[18:19], v[136:137] op_sel_hi:[1,0,1]
v_pk_fma_f32 v[138:139], v[130:131], v[18:19], v[138:139] op_sel_hi:[1,0,1]
v_lshlrev_b32 v124, 16, v56
v_and_b32 v125, 0xffff0000, v56
v_lshlrev_b32 v126, 16, v57
v_and_b32 v127, 0xffff0000, v57
v_lshlrev_b32 v128, 16, v58
v_and_b32 v129, 0xffff0000, v58
v_lshlrev_b32 v130, 16, v59
v_and_b32 v131, 0xffff0000, v59
v_pk_fma_f32 v[132:133], v[124:125], v[18:19], v[132:133] op_sel:[0,1,0] op_sel_hi:[1,1,1]
v_pk_fma_f32 v[134:135], v[126:127], v[18:19], v[134:135] op_sel:[0,1,0] op_sel_hi:[1,1,1]
v_pk_fma_f32 v[136:137], v[128:129], v[18:19], v[136:137] op_sel:[0,1,0] op_sel_hi:[1,1,1]
v_pk_fma_f32 v[138:139], v[130:131], v[18:19], v[138:139] op_sel:[0,1,0] op_sel_hi:[1,1,1]
s_nop 0
v_permlane32_swap_b32 v132, v136
v_permlane32_swap_b32 v133, v137
v_permlane32_swap_b32 v134, v138
v_permlane32_swap_b32 v135, v139
)ASM"
R"ASM(v_add_f32 v132, v132, v136
v_add_f32 v133, v133, v137
v_add_f32 v134, v134, v138
v_add_f32 v135, v135, v139
s_nop 0
v_permlane16_swap_b32 v132, v134
v_permlane16_swap_b32 v133, v135
v_add_f32 v132, v132, v134
v_add_f32 v133, v133, v135
s_nop 0
v_add_f32_dpp v132, v132, v132 row_ror:8 row_mask:0xf bank_mask:0xf
v_add_f32_dpp v133, v133, v133 row_ror:8 row_mask:0xf bank_mask:0xf
v_and_b32 v26, 7, v1
v_lshlrev_b32 v26, 5, v26
v_lshrrev_b32 v27, 4, v1
v_lshlrev_b32 v27, 3, v27
v_add_u32 v26, v26, v27
s_lshl_b32 s80, s13, 8
s_add_u32 s80, s80, 161792
v_add_u32 v26, s80, v26
s_mov_b32 exec_lo, 0x00ff00ff
s_mov_b32 exec_hi, 0x00ff00ff
ds_write_b64 v26, v[132:133]
s_mov_b64 exec, -1
s_waitcnt lgkmcnt(0)
s_barrier
s_cmp_lg_u32 s13, 0
s_cbranch_scc1 Lnopub_10_%=
v_and_b32 v26, 63, v1
v_lshlrev_b32 v26, 2, v26
v_add_u32 v27, 161792, v26
ds_read_b32 v140, v27 offset:0
)ASM"
R"ASM(ds_read_b32 v141, v27 offset:256
ds_read_b32 v142, v27 offset:512
ds_read_b32 v143, v27 offset:768
s_waitcnt lgkmcnt(2)
v_add_f32 v140, v140, v141
s_waitcnt lgkmcnt(0)
v_add_f32 v142, v142, v143
v_add_f32 v140, v140, v142
v_add_u32 v140, 0x80, v140
v_and_b32 v140, 0xffffff00, v140
s_and_b32 s80, s18, 0xff
v_or_b32 v140, s80, v140
v_mov_b32 v141, s19
v_lshlrev_b32 v26, 1, v26
s_bfe_u32 s80, s12, 0x10002
s_lshl_b32 s80, s80, 14
s_and_b32 s81, s12, 3
s_lshl_b32 s82, s11, 2
s_add_u32 s81, s81, s82
s_lshl_b32 s81, s81, 9
s_add_u32 s80, s80, s81
s_add_u32 s80, s80, 0x1288000
buffer_store_dwordx2 v[140:141], v26, s[20:23], s80 offen sc1
s_mov_b64 exec, -1
Lnopub_10_%=:
Lnostrip2_9_%=:
v_lshrrev_b32 v8, 3, v1
v_lshlrev_b32 v8, 10, v8
v_lshrrev_b32 v27, 2, v1
v_and_b32 v27, 4, v27
v_xor_b32 v27, v27, v1
v_and_b32 v27, 7, v27
)ASM"
R"ASM(v_lshlrev_b32 v27, 4, v27
v_add_u32 v8, v8, v27
s_lshl_b32 s80, s13, 12
s_add_u32 m0, s80, 145408
s_nop 0
buffer_load_dwordx4 v3, s[68:71], s80 offen offset:0 lds
s_waitcnt vmcnt(16)
v_lshlrev_b32 v124, 16, v60
v_and_b32 v125, 0xffff0000, v60
v_lshlrev_b32 v126, 16, v61
v_and_b32 v127, 0xffff0000, v61
v_lshlrev_b32 v128, 16, v62
v_and_b32 v129, 0xffff0000, v62
v_lshlrev_b32 v130, 16, v63
v_and_b32 v131, 0xffff0000, v63
v_pk_add_f32 v[44:45], v[124:125], v[126:127]
v_pk_add_f32 v[46:47], v[128:129], v[130:131]
v_pk_mul_f32 v[48:49], v[124:125], v[124:125]
v_pk_add_f32 v[44:45], v[44:45], v[46:47]
v_pk_fma_f32 v[48:49], v[126:127], v[126:127], v[48:49]
v_add_f32 v10, v44, v45
v_pk_fma_f32 v[48:49], v[128:129], v[128:129], v[48:49]
v_pk_fma_f32 v[48:49], v[130:131], v[130:131], v[48:49]
v_add_f32 v28, v48, v49
buffer_load_dwordx4 v3, s[68:71], s80 offen offset:1024 lds
s_waitcnt vmcnt(16)
v_lshlrev_b32 v132, 16, v64
v_and_b32 v133, 0xffff0000, v64
v_lshlrev_b32 v134, 16, v65
v_and_b32 v135, 0xffff0000, v65
v_lshlrev_b32 v136, 16, v66
v_and_b32 v137, 0xffff0000, v66
)ASM"
R"ASM(v_lshlrev_b32 v138, 16, v67
v_and_b32 v139, 0xffff0000, v67
v_pk_add_f32 v[44:45], v[132:133], v[134:135]
v_pk_add_f32 v[46:47], v[136:137], v[138:139]
v_pk_mul_f32 v[48:49], v[132:133], v[132:133]
v_pk_add_f32 v[44:45], v[44:45], v[46:47]
v_pk_fma_f32 v[48:49], v[134:135], v[134:135], v[48:49]
v_add_f32 v11, v44, v45
v_pk_fma_f32 v[48:49], v[136:137], v[136:137], v[48:49]
v_pk_fma_f32 v[48:49], v[138:139], v[138:139], v[48:49]
v_add_f32 v29, v48, v49
buffer_load_dwordx4 v3, s[68:71], s80 offen offset:2048 lds
s_waitcnt vmcnt(16)
v_lshlrev_b32 v140, 16, v68
v_and_b32 v141, 0xffff0000, v68
v_lshlrev_b32 v142, 16, v69
v_and_b32 v143, 0xffff0000, v69
v_lshlrev_b32 v144, 16, v70
v_and_b32 v145, 0xffff0000, v70
v_lshlrev_b32 v146, 16, v71
v_and_b32 v147, 0xffff0000, v71
v_pk_add_f32 v[44:45], v[140:141], v[142:143]
v_pk_add_f32 v[46:47], v[144:145], v[146:147]
v_pk_mul_f32 v[48:49], v[140:141], v[140:141]
v_pk_add_f32 v[44:45], v[44:45], v[46:47]
v_pk_fma_f32 v[48:49], v[142:143], v[142:143], v[48:49]
v_add_f32 v12, v44, v45
v_pk_fma_f32 v[48:49], v[144:145], v[144:145], v[48:49]
v_pk_fma_f32 v[48:49], v[146:147], v[146:147], v[48:49]
v_add_f32 v30, v48, v49
buffer_load_dwordx4 v3, s[68:71], s80 offen offset:3072 lds
s_waitcnt vmcnt(16)
)ASM"
R"ASM(v_lshlrev_b32 v148, 16, v72
v_and_b32 v149, 0xffff0000, v72
v_lshlrev_b32 v150, 16, v73
v_and_b32 v151, 0xffff0000, v73
v_lshlrev_b32 v152, 16, v74
v_and_b32 v153, 0xffff0000, v74
v_lshlrev_b32 v154, 16, v75
v_and_b32 v155, 0xffff0000, v75
v_pk_add_f32 v[44:45], v[148:149], v[150:151]
v_pk_add_f32 v[46:47], v[152:153], v[154:155]
v_pk_mul_f32 v[48:49], v[148:149], v[148:149]
v_pk_add_f32 v[44:45], v[44:45], v[46:47]
v_pk_fma_f32 v[48:49], v[150:151], v[150:151], v[48:49]
v_add_f32 v13, v44, v45
v_pk_fma_f32 v[48:49], v[152:153], v[152:153], v[48:49]
v_pk_fma_f32 v[48:49], v[154:155], v[154:155], v[48:49]
v_add_f32 v31, v48, v49
s_waitcnt vmcnt(15)
v_lshlrev_b32 v156, 16, v76
v_and_b32 v157, 0xffff0000, v76
v_lshlrev_b32 v158, 16, v77
v_and_b32 v159, 0xffff0000, v77
v_lshlrev_b32 v160, 16, v78
v_and_b32 v161, 0xffff0000, v78
v_lshlrev_b32 v162, 16, v79
v_and_b32 v163, 0xffff0000, v79
v_pk_add_f32 v[44:45], v[156:157], v[158:159]
v_pk_add_f32 v[46:47], v[160:161], v[162:163]
v_pk_mul_f32 v[48:49], v[156:157], v[156:157]
v_pk_add_f32 v[44:45], v[44:45], v[46:47]
v_pk_fma_f32 v[48:49], v[158:159], v[158:159], v[48:49]
v_add_f32 v14, v44, v45
)ASM"
R"ASM(v_pk_fma_f32 v[48:49], v[160:161], v[160:161], v[48:49]
v_pk_fma_f32 v[48:49], v[162:163], v[162:163], v[48:49]
v_add_f32 v32, v48, v49
s_waitcnt vmcnt(14)
v_lshlrev_b32 v164, 16, v80
v_and_b32 v165, 0xffff0000, v80
v_lshlrev_b32 v166, 16, v81
v_and_b32 v167, 0xffff0000, v81
v_lshlrev_b32 v168, 16, v82
v_and_b32 v169, 0xffff0000, v82
v_lshlrev_b32 v170, 16, v83
v_and_b32 v171, 0xffff0000, v83
v_pk_add_f32 v[44:45], v[164:165], v[166:167]
v_pk_add_f32 v[46:47], v[168:169], v[170:171]
v_pk_mul_f32 v[48:49], v[164:165], v[164:165]
v_pk_add_f32 v[44:45], v[44:45], v[46:47]
v_pk_fma_f32 v[48:49], v[166:167], v[166:167], v[48:49]
v_add_f32 v15, v44, v45
v_pk_fma_f32 v[48:49], v[168:169], v[168:169], v[48:49]
v_pk_fma_f32 v[48:49], v[170:171], v[170:171], v[48:49]
v_add_f32 v33, v48, v49
s_waitcnt vmcnt(13)
v_lshlrev_b32 v172, 16, v84
v_and_b32 v173, 0xffff0000, v84
v_lshlrev_b32 v174, 16, v85
v_and_b32 v175, 0xffff0000, v85
v_lshlrev_b32 v176, 16, v86
v_and_b32 v177, 0xffff0000, v86
v_lshlrev_b32 v178, 16, v87
v_and_b32 v179, 0xffff0000, v87
v_pk_add_f32 v[44:45], v[172:173], v[174:175]
v_pk_add_f32 v[46:47], v[176:177], v[178:179]
)ASM"
R"ASM(v_pk_mul_f32 v[48:49], v[172:173], v[172:173]
v_pk_add_f32 v[44:45], v[44:45], v[46:47]
v_pk_fma_f32 v[48:49], v[174:175], v[174:175], v[48:49]
v_add_f32 v16, v44, v45
v_pk_fma_f32 v[48:49], v[176:177], v[176:177], v[48:49]
v_pk_fma_f32 v[48:49], v[178:179], v[178:179], v[48:49]
v_add_f32 v34, v48, v49
s_waitcnt vmcnt(12)
v_lshlrev_b32 v180, 16, v88
v_and_b32 v181, 0xffff0000, v88
v_lshlrev_b32 v182, 16, v89
v_and_b32 v183, 0xffff0000, v89
v_lshlrev_b32 v184, 16, v90
v_and_b32 v185, 0xffff0000, v90
v_lshlrev_b32 v186, 16, v91
v_and_b32 v187, 0xffff0000, v91
v_pk_add_f32 v[44:45], v[180:181], v[182:183]
v_pk_add_f32 v[46:47], v[184:185], v[186:187]
v_pk_mul_f32 v[48:49], v[180:181], v[180:181]
v_pk_add_f32 v[44:45], v[44:45], v[46:47]
v_pk_fma_f32 v[48:49], v[182:183], v[182:183], v[48:49]
v_add_f32 v17, v44, v45
v_pk_fma_f32 v[48:49], v[184:185], v[184:185], v[48:49]
v_pk_fma_f32 v[48:49], v[186:187], v[186:187], v[48:49]
v_add_f32 v35, v48, v49
s_waitcnt vmcnt(11)
v_lshlrev_b32 v188, 16, v92
v_and_b32 v189, 0xffff0000, v92
v_lshlrev_b32 v190, 16, v93
v_and_b32 v191, 0xffff0000, v93
v_lshlrev_b32 v192, 16, v94
v_and_b32 v193, 0xffff0000, v94
)ASM"
R"ASM(v_lshlrev_b32 v194, 16, v95
v_and_b32 v195, 0xffff0000, v95
v_pk_add_f32 v[44:45], v[188:189], v[190:191]
v_pk_add_f32 v[46:47], v[192:193], v[194:195]
v_pk_mul_f32 v[48:49], v[188:189], v[188:189]
v_pk_add_f32 v[44:45], v[44:45], v[46:47]
v_pk_fma_f32 v[48:49], v[190:191], v[190:191], v[48:49]
v_add_f32 v18, v44, v45
v_pk_fma_f32 v[48:49], v[192:193], v[192:193], v[48:49]
v_pk_fma_f32 v[48:49], v[194:195], v[194:195], v[48:49]
v_add_f32 v36, v48, v49
s_waitcnt vmcnt(10)
v_lshlrev_b32 v196, 16, v96
v_and_b32 v197, 0xffff0000, v96
v_lshlrev_b32 v198, 16, v97
v_and_b32 v199, 0xffff0000, v97
v_lshlrev_b32 v200, 16, v98
v_and_b32 v201, 0xffff0000, v98
v_lshlrev_b32 v202, 16, v99
v_and_b32 v203, 0xffff0000, v99
v_pk_add_f32 v[44:45], v[196:197], v[198:199]
v_pk_add_f32 v[46:47], v[200:201], v[202:203]
v_pk_mul_f32 v[48:49], v[196:197], v[196:197]
v_pk_add_f32 v[44:45], v[44:45], v[46:47]
v_pk_fma_f32 v[48:49], v[198:199], v[198:199], v[48:49]
v_add_f32 v19, v44, v45
v_pk_fma_f32 v[48:49], v[200:201], v[200:201], v[48:49]
v_pk_fma_f32 v[48:49], v[202:203], v[202:203], v[48:49]
v_add_f32 v37, v48, v49
s_waitcnt vmcnt(9)
v_lshlrev_b32 v204, 16, v100
v_and_b32 v205, 0xffff0000, v100
)ASM"
R"ASM(v_lshlrev_b32 v206, 16, v101
v_and_b32 v207, 0xffff0000, v101
v_lshlrev_b32 v208, 16, v102
v_and_b32 v209, 0xffff0000, v102
v_lshlrev_b32 v210, 16, v103
v_and_b32 v211, 0xffff0000, v103
v_pk_add_f32 v[44:45], v[204:205], v[206:207]
v_pk_add_f32 v[46:47], v[208:209], v[210:211]
v_pk_mul_f32 v[48:49], v[204:205], v[204:205]
v_pk_add_f32 v[44:45], v[44:45], v[46:47]
v_pk_fma_f32 v[48:49], v[206:207], v[206:207], v[48:49]
v_add_f32 v20, v44, v45
v_pk_fma_f32 v[48:49], v[208:209], v[208:209], v[48:49]
v_pk_fma_f32 v[48:49], v[210:211], v[210:211], v[48:49]
v_add_f32 v38, v48, v49
v_lshlrev_b32 v9, 4, v0
s_mov_b32 s98, 0x1280000
buffer_load_dwordx4 v[60:63], v9, s[20:23], s98 offen sc1
s_mov_b32 s98, 0x1282000
buffer_load_dwordx4 v[64:67], v9, s[20:23], s98 offen sc1
s_mov_b32 s98, 0x1284000
buffer_load_dwordx4 v[68:71], v9, s[20:23], s98 offen sc1
s_mov_b32 s98, 0x1286000
buffer_load_dwordx4 v[72:75], v9, s[20:23], s98 offen sc1
s_mov_b32 s98, 0x1281000
buffer_load_dwordx4 v[76:79], v9, s[20:23], s98 offen sc1
s_mov_b32 s98, 0x1283000
buffer_load_dwordx4 v[80:83], v9, s[20:23], s98 offen sc1
s_mov_b32 s98, 0x1285000
buffer_load_dwordx4 v[84:87], v9, s[20:23], s98 offen sc1
s_mov_b32 s98, 0x1287000
buffer_load_dwordx4 v[88:91], v9, s[20:23], s98 offen sc1
)ASM"
R"ASM(s_lshl_b32 s80, s13, 17
s_lshl_b32 s81, s15, 7
s_add_u32 s80, s80, s81
s_lshl_b32 s81, s13, 14
s_add_u32 s81, s81, 66560
s_mov_b32 m0, s81
s_nop 0
buffer_load_dwordx4 v8, s[40:43], s80 offen lds
s_waitcnt vmcnt(17)
v_lshlrev_b32 v212, 16, v104
v_and_b32 v213, 0xffff0000, v104
v_lshlrev_b32 v214, 16, v105
v_and_b32 v215, 0xffff0000, v105
v_lshlrev_b32 v216, 16, v106
v_and_b32 v217, 0xffff0000, v106
v_lshlrev_b32 v218, 16, v107
v_and_b32 v219, 0xffff0000, v107
v_pk_add_f32 v[44:45], v[212:213], v[214:215]
v_pk_add_f32 v[46:47], v[216:217], v[218:219]
v_pk_mul_f32 v[48:49], v[212:213], v[212:213]
v_pk_add_f32 v[44:45], v[44:45], v[46:47]
v_pk_fma_f32 v[48:49], v[214:215], v[214:215], v[48:49]
v_add_f32 v21, v44, v45
v_pk_fma_f32 v[48:49], v[216:217], v[216:217], v[48:49]
v_pk_fma_f32 v[48:49], v[218:219], v[218:219], v[48:49]
v_add_f32 v39, v48, v49
s_add_u32 s80, s80, 8192
s_add_u32 s81, s81, 1024
s_mov_b32 m0, s81
s_nop 0
buffer_load_dwordx4 v8, s[40:43], s80 offen lds
s_waitcnt vmcnt(17)
)ASM"
R"ASM(v_lshlrev_b32 v220, 16, v108
v_and_b32 v221, 0xffff0000, v108
v_lshlrev_b32 v222, 16, v109
v_and_b32 v223, 0xffff0000, v109
v_lshlrev_b32 v224, 16, v110
v_and_b32 v225, 0xffff0000, v110
v_lshlrev_b32 v226, 16, v111
v_and_b32 v227, 0xffff0000, v111
v_pk_add_f32 v[44:45], v[220:221], v[222:223]
v_pk_add_f32 v[46:47], v[224:225], v[226:227]
v_pk_mul_f32 v[48:49], v[220:221], v[220:221]
v_pk_add_f32 v[44:45], v[44:45], v[46:47]
v_pk_fma_f32 v[48:49], v[222:223], v[222:223], v[48:49]
v_add_f32 v22, v44, v45
v_pk_fma_f32 v[48:49], v[224:225], v[224:225], v[48:49]
v_pk_fma_f32 v[48:49], v[226:227], v[226:227], v[48:49]
v_add_f32 v40, v48, v49
s_add_u32 s80, s80, 8192
s_add_u32 s81, s81, 1024
s_mov_b32 m0, s81
s_nop 0
buffer_load_dwordx4 v8, s[40:43], s80 offen lds
s_waitcnt vmcnt(17)
v_lshlrev_b32 v228, 16, v112
v_and_b32 v229, 0xffff0000, v112
v_lshlrev_b32 v230, 16, v113
v_and_b32 v231, 0xffff0000, v113
v_lshlrev_b32 v232, 16, v114
v_and_b32 v233, 0xffff0000, v114
v_lshlrev_b32 v234, 16, v115
v_and_b32 v235, 0xffff0000, v115
v_pk_add_f32 v[44:45], v[228:229], v[230:231]
)ASM"
R"ASM(v_pk_add_f32 v[46:47], v[232:233], v[234:235]
v_pk_mul_f32 v[48:49], v[228:229], v[228:229]
v_pk_add_f32 v[44:45], v[44:45], v[46:47]
v_pk_fma_f32 v[48:49], v[230:231], v[230:231], v[48:49]
v_add_f32 v23, v44, v45
v_pk_fma_f32 v[48:49], v[232:233], v[232:233], v[48:49]
v_pk_fma_f32 v[48:49], v[234:235], v[234:235], v[48:49]
v_add_f32 v41, v48, v49
s_add_u32 s80, s80, 8192
s_add_u32 s81, s81, 1024
s_mov_b32 m0, s81
s_nop 0
buffer_load_dwordx4 v8, s[40:43], s80 offen lds
s_waitcnt vmcnt(17)
v_lshlrev_b32 v236, 16, v116
v_and_b32 v237, 0xffff0000, v116
v_lshlrev_b32 v238, 16, v117
v_and_b32 v239, 0xffff0000, v117
v_lshlrev_b32 v240, 16, v118
v_and_b32 v241, 0xffff0000, v118
v_lshlrev_b32 v242, 16, v119
v_and_b32 v243, 0xffff0000, v119
v_pk_add_f32 v[44:45], v[236:237], v[238:239]
v_pk_add_f32 v[46:47], v[240:241], v[242:243]
v_pk_mul_f32 v[48:49], v[236:237], v[236:237]
v_pk_add_f32 v[44:45], v[44:45], v[46:47]
v_pk_fma_f32 v[48:49], v[238:239], v[238:239], v[48:49]
v_add_f32 v24, v44, v45
v_pk_fma_f32 v[48:49], v[240:241], v[240:241], v[48:49]
v_pk_fma_f32 v[48:49], v[242:243], v[242:243], v[48:49]
v_add_f32 v42, v48, v49
s_add_u32 s80, s80, 8192
)ASM"
R"ASM(s_add_u32 s81, s81, 1024
s_mov_b32 m0, s81
s_nop 0
buffer_load_dwordx4 v8, s[40:43], s80 offen lds
s_waitcnt vmcnt(17)
v_lshlrev_b32 v244, 16, v120
v_and_b32 v245, 0xffff0000, v120
v_lshlrev_b32 v246, 16, v121
v_and_b32 v247, 0xffff0000, v121
v_lshlrev_b32 v248, 16, v122
v_and_b32 v249, 0xffff0000, v122
v_lshlrev_b32 v250, 16, v123
v_and_b32 v251, 0xffff0000, v123
v_pk_add_f32 v[44:45], v[244:245], v[246:247]
v_pk_add_f32 v[46:47], v[248:249], v[250:251]
v_pk_mul_f32 v[48:49], v[244:245], v[244:245]
v_pk_add_f32 v[44:45], v[44:45], v[46:47]
v_pk_fma_f32 v[48:49], v[246:247], v[246:247], v[48:49]
v_add_f32 v25, v44, v45
v_pk_fma_f32 v[48:49], v[248:249], v[248:249], v[48:49]
v_pk_fma_f32 v[48:49], v[250:251], v[250:251], v[48:49]
v_add_f32 v43, v48, v49
v_permlane32_swap_b32 v10, v18
v_permlane32_swap_b32 v11, v19
v_permlane32_swap_b32 v12, v20
v_permlane32_swap_b32 v13, v21
v_permlane32_swap_b32 v14, v22
v_permlane32_swap_b32 v15, v23
v_permlane32_swap_b32 v16, v24
v_permlane32_swap_b32 v17, v25
v_permlane32_swap_b32 v28, v36
v_permlane32_swap_b32 v29, v37
)ASM"
R"ASM(v_permlane32_swap_b32 v30, v38
v_permlane32_swap_b32 v31, v39
v_permlane32_swap_b32 v32, v40
v_permlane32_swap_b32 v33, v41
v_permlane32_swap_b32 v34, v42
v_permlane32_swap_b32 v35, v43
v_add_f32 v10, v10, v18
v_add_f32 v11, v11, v19
v_add_f32 v12, v12, v20
v_add_f32 v13, v13, v21
v_add_f32 v14, v14, v22
v_add_f32 v15, v15, v23
v_add_f32 v16, v16, v24
v_add_f32 v17, v17, v25
v_add_f32 v28, v28, v36
v_add_f32 v29, v29, v37
v_add_f32 v30, v30, v38
v_add_f32 v31, v31, v39
v_add_f32 v32, v32, v40
v_add_f32 v33, v33, v41
v_add_f32 v34, v34, v42
v_add_f32 v35, v35, v43
v_permlane16_swap_b32 v10, v14
v_permlane16_swap_b32 v11, v15
v_permlane16_swap_b32 v12, v16
v_permlane16_swap_b32 v13, v17
v_permlane16_swap_b32 v28, v32
v_permlane16_swap_b32 v29, v33
v_permlane16_swap_b32 v30, v34
v_permlane16_swap_b32 v31, v35
v_add_f32 v10, v10, v14
v_add_f32 v11, v11, v15
)ASM"
R"ASM(v_add_f32 v12, v12, v16
v_add_f32 v13, v13, v17
v_add_f32 v28, v28, v32
v_add_f32 v29, v29, v33
v_add_f32 v30, v30, v34
v_add_f32 v31, v31, v35
v_add_f32_dpp v10, v10, v10 row_ror:8 row_mask:0xf bank_mask:0xf
v_add_f32_dpp v11, v11, v11 row_ror:8 row_mask:0xf bank_mask:0xf
v_add_f32_dpp v12, v12, v12 row_ror:8 row_mask:0xf bank_mask:0xf
v_add_f32_dpp v13, v13, v13 row_ror:8 row_mask:0xf bank_mask:0xf
v_add_f32_dpp v28, v28, v28 row_ror:8 row_mask:0xf bank_mask:0xf
v_add_f32_dpp v29, v29, v29 row_ror:8 row_mask:0xf bank_mask:0xf
v_add_f32_dpp v30, v30, v30 row_ror:8 row_mask:0xf bank_mask:0xf
v_add_f32_dpp v31, v31, v31 row_ror:8 row_mask:0xf bank_mask:0xf
v_add_f32_dpp v10, v10, v10 row_ror:4 row_mask:0xf bank_mask:0xf
v_add_f32_dpp v11, v11, v11 row_ror:4 row_mask:0xf bank_mask:0xf
v_add_f32_dpp v12, v12, v12 row_ror:4 row_mask:0xf bank_mask:0xf
v_add_f32_dpp v13, v13, v13 row_ror:4 row_mask:0xf bank_mask:0xf
v_add_f32_dpp v28, v28, v28 row_ror:4 row_mask:0xf bank_mask:0xf
v_add_f32_dpp v29, v29, v29 row_ror:4 row_mask:0xf bank_mask:0xf
v_add_f32_dpp v30, v30, v30 row_ror:4 row_mask:0xf bank_mask:0xf
v_add_f32_dpp v31, v31, v31 row_ror:4 row_mask:0xf bank_mask:0xf
v_add_f32_dpp v10, v10, v10 row_ror:2 row_mask:0xf bank_mask:0xf
v_add_f32_dpp v11, v11, v11 row_ror:2 row_mask:0xf bank_mask:0xf
v_add_f32_dpp v12, v12, v12 row_ror:2 row_mask:0xf bank_mask:0xf
v_add_f32_dpp v13, v13, v13 row_ror:2 row_mask:0xf bank_mask:0xf
v_add_f32_dpp v28, v28, v28 row_ror:2 row_mask:0xf bank_mask:0xf
v_add_f32_dpp v29, v29, v29 row_ror:2 row_mask:0xf bank_mask:0xf
v_add_f32_dpp v30, v30, v30 row_ror:2 row_mask:0xf bank_mask:0xf
v_add_f32_dpp v31, v31, v31 row_ror:2 row_mask:0xf bank_mask:0xf
v_add_f32_dpp v10, v10, v10 row_ror:1 row_mask:0xf bank_mask:0xf
v_add_f32_dpp v11, v11, v11 row_ror:1 row_mask:0xf bank_mask:0xf
)ASM"
R"ASM(v_add_f32_dpp v12, v12, v12 row_ror:1 row_mask:0xf bank_mask:0xf
v_add_f32_dpp v13, v13, v13 row_ror:1 row_mask:0xf bank_mask:0xf
v_add_f32_dpp v28, v28, v28 row_ror:1 row_mask:0xf bank_mask:0xf
v_add_f32_dpp v29, v29, v29 row_ror:1 row_mask:0xf bank_mask:0xf
v_add_f32_dpp v30, v30, v30 row_ror:1 row_mask:0xf bank_mask:0xf
v_add_f32_dpp v31, v31, v31 row_ror:1 row_mask:0xf bank_mask:0xf
v_mul_f32 v10, 0x3b000000, v10
v_mul_f32 v11, 0x3b000000, v11
v_mul_f32 v12, 0x3b000000, v12
v_mul_f32 v13, 0x3b000000, v13
v_mul_f32 v28, 0x3b000000, v28
v_mul_f32 v29, 0x3b000000, v29
v_mul_f32 v30, 0x3b000000, v30
v_mul_f32 v31, 0x3b000000, v31
v_fma_f32 v28, -v10, v10, v28
v_fma_f32 v29, -v11, v11, v29
v_fma_f32 v30, -v12, v12, v30
v_fma_f32 v31, -v13, v13, v31
v_max_f32 v28, 0, v28
v_max_f32 v29, 0, v29
v_max_f32 v30, 0, v30
v_max_f32 v31, 0, v31
v_add_f32 v28, 0x3727c5ac, v28
v_add_f32 v29, 0x3727c5ac, v29
v_add_f32 v30, 0x3727c5ac, v30
v_add_f32 v31, 0x3727c5ac, v31
v_rsq_f32 v28, v28
v_rsq_f32 v29, v29
v_rsq_f32 v30, v30
v_rsq_f32 v31, v31
v_mul_f32_e64 v10, -v10, v28
v_mul_f32_e64 v11, -v11, v29
)ASM"
R"ASM(v_mul_f32_e64 v12, -v12, v30
v_mul_f32_e64 v13, -v13, v31
v_readlane_b32 s44, v28, 0
v_readlane_b32 s45, v10, 0
v_readlane_b32 s46, v29, 0
v_readlane_b32 s47, v11, 0
v_readlane_b32 s48, v30, 0
v_readlane_b32 s49, v12, 0
v_readlane_b32 s50, v31, 0
v_readlane_b32 s51, v13, 0
v_readlane_b32 s52, v28, 16
v_readlane_b32 s53, v10, 16
v_readlane_b32 s54, v29, 16
v_readlane_b32 s55, v11, 16
v_readlane_b32 s56, v30, 16
v_readlane_b32 s57, v12, 16
v_readlane_b32 s58, v31, 16
v_readlane_b32 s59, v13, 16
v_readlane_b32 s60, v28, 32
v_readlane_b32 s61, v10, 32
v_readlane_b32 s62, v29, 32
v_readlane_b32 s63, v11, 32
v_readlane_b32 s64, v30, 32
v_readlane_b32 s65, v12, 32
v_readlane_b32 s66, v31, 32
v_readlane_b32 s67, v13, 32
v_readlane_b32 s68, v28, 48
v_readlane_b32 s69, v10, 48
v_readlane_b32 s70, v29, 48
v_readlane_b32 s71, v11, 48
v_readlane_b32 s72, v30, 48
v_readlane_b32 s73, v12, 48
)ASM"
R"ASM(v_readlane_b32 s74, v31, 48
v_readlane_b32 s75, v13, 48
s_add_u32 s80, s80, 8192
s_add_u32 s81, s81, 1024
s_mov_b32 m0, s81
s_nop 0
buffer_load_dwordx4 v8, s[40:43], s80 offen lds
v_pk_fma_f32 v[124:125], v[124:125], s[44:45], s[44:45] op_sel:[0,0,1] op_sel_hi:[1,0,1]
v_pk_fma_f32 v[126:127], v[126:127], s[44:45], s[44:45] op_sel:[0,0,1] op_sel_hi:[1,0,1]
v_pk_fma_f32 v[128:129], v[128:129], s[44:45], s[44:45] op_sel:[0,0,1] op_sel_hi:[1,0,1]
v_pk_fma_f32 v[130:131], v[130:131], s[44:45], s[44:45] op_sel:[0,0,1] op_sel_hi:[1,0,1]
s_add_u32 s80, s80, 8192
s_add_u32 s81, s81, 1024
s_mov_b32 m0, s81
s_nop 0
buffer_load_dwordx4 v8, s[40:43], s80 offen lds
v_pk_fma_f32 v[132:133], v[132:133], s[46:47], s[46:47] op_sel:[0,0,1] op_sel_hi:[1,0,1]
v_pk_fma_f32 v[134:135], v[134:135], s[46:47], s[46:47] op_sel:[0,0,1] op_sel_hi:[1,0,1]
v_pk_fma_f32 v[136:137], v[136:137], s[46:47], s[46:47] op_sel:[0,0,1] op_sel_hi:[1,0,1]
v_pk_fma_f32 v[138:139], v[138:139], s[46:47], s[46:47] op_sel:[0,0,1] op_sel_hi:[1,0,1]
s_add_u32 s80, s80, 8192
s_add_u32 s81, s81, 1024
s_mov_b32 m0, s81
s_nop 0
buffer_load_dwordx4 v8, s[40:43], s80 offen lds
v_pk_fma_f32 v[140:141], v[140:141], s[48:49], s[48:49] op_sel:[0,0,1] op_sel_hi:[1,0,1]
v_pk_fma_f32 v[142:143], v[142:143], s[48:49], s[48:49] op_sel:[0,0,1] op_sel_hi:[1,0,1]
v_pk_fma_f32 v[144:145], v[144:145], s[48:49], s[48:49] op_sel:[0,0,1] op_sel_hi:[1,0,1]
v_pk_fma_f32 v[146:147], v[146:147], s[48:49], s[48:49] op_sel:[0,0,1] op_sel_hi:[1,0,1]
s_add_u32 s80, s80, 8192
s_add_u32 s81, s81, 1024
s_mov_b32 m0, s81
)ASM"
R"ASM(s_nop 0
buffer_load_dwordx4 v8, s[40:43], s80 offen lds
v_pk_fma_f32 v[148:149], v[148:149], s[50:51], s[50:51] op_sel:[0,0,1] op_sel_hi:[1,0,1]
v_pk_fma_f32 v[150:151], v[150:151], s[50:51], s[50:51] op_sel:[0,0,1] op_sel_hi:[1,0,1]
v_pk_fma_f32 v[152:153], v[152:153], s[50:51], s[50:51] op_sel:[0,0,1] op_sel_hi:[1,0,1]
v_pk_fma_f32 v[154:155], v[154:155], s[50:51], s[50:51] op_sel:[0,0,1] op_sel_hi:[1,0,1]
s_add_u32 s80, s80, 8192
s_add_u32 s81, s81, 1024
s_mov_b32 m0, s81
s_nop 0
buffer_load_dwordx4 v8, s[40:43], s80 offen lds
v_pk_fma_f32 v[156:157], v[156:157], s[52:53], s[52:53] op_sel:[0,0,1] op_sel_hi:[1,0,1]
v_pk_fma_f32 v[158:159], v[158:159], s[52:53], s[52:53] op_sel:[0,0,1] op_sel_hi:[1,0,1]
v_pk_fma_f32 v[160:161], v[160:161], s[52:53], s[52:53] op_sel:[0,0,1] op_sel_hi:[1,0,1]
v_pk_fma_f32 v[162:163], v[162:163], s[52:53], s[52:53] op_sel:[0,0,1] op_sel_hi:[1,0,1]
s_add_u32 s80, s80, 8192
s_add_u32 s81, s81, 1024
s_mov_b32 m0, s81
s_nop 0
buffer_load_dwordx4 v8, s[40:43], s80 offen lds
v_pk_fma_f32 v[164:165], v[164:165], s[54:55], s[54:55] op_sel:[0,0,1] op_sel_hi:[1,0,1]
v_pk_fma_f32 v[166:167], v[166:167], s[54:55], s[54:55] op_sel:[0,0,1] op_sel_hi:[1,0,1]
v_pk_fma_f32 v[168:169], v[168:169], s[54:55], s[54:55] op_sel:[0,0,1] op_sel_hi:[1,0,1]
v_pk_fma_f32 v[170:171], v[170:171], s[54:55], s[54:55] op_sel:[0,0,1] op_sel_hi:[1,0,1]
s_add_u32 s80, s80, 8192
s_add_u32 s81, s81, 1024
s_mov_b32 m0, s81
s_nop 0
buffer_load_dwordx4 v8, s[40:43], s80 offen lds
v_pk_fma_f32 v[172:173], v[172:173], s[56:57], s[56:57] op_sel:[0,0,1] op_sel_hi:[1,0,1]
v_pk_fma_f32 v[174:175], v[174:175], s[56:57], s[56:57] op_sel:[0,0,1] op_sel_hi:[1,0,1]
v_pk_fma_f32 v[176:177], v[176:177], s[56:57], s[56:57] op_sel:[0,0,1] op_sel_hi:[1,0,1]
)ASM"
R"ASM(v_pk_fma_f32 v[178:179], v[178:179], s[56:57], s[56:57] op_sel:[0,0,1] op_sel_hi:[1,0,1]
s_add_u32 s80, s80, 8192
s_add_u32 s81, s81, 1024
s_mov_b32 m0, s81
s_nop 0
buffer_load_dwordx4 v8, s[40:43], s80 offen lds
v_pk_fma_f32 v[180:181], v[180:181], s[58:59], s[58:59] op_sel:[0,0,1] op_sel_hi:[1,0,1]
v_pk_fma_f32 v[182:183], v[182:183], s[58:59], s[58:59] op_sel:[0,0,1] op_sel_hi:[1,0,1]
v_pk_fma_f32 v[184:185], v[184:185], s[58:59], s[58:59] op_sel:[0,0,1] op_sel_hi:[1,0,1]
v_pk_fma_f32 v[186:187], v[186:187], s[58:59], s[58:59] op_sel:[0,0,1] op_sel_hi:[1,0,1]
s_add_u32 s80, s80, 8192
s_add_u32 s81, s81, 1024
s_mov_b32 m0, s81
s_nop 0
buffer_load_dwordx4 v8, s[40:43], s80 offen lds
v_pk_fma_f32 v[188:189], v[188:189], s[60:61], s[60:61] op_sel:[0,0,1] op_sel_hi:[1,0,1]
v_pk_fma_f32 v[190:191], v[190:191], s[60:61], s[60:61] op_sel:[0,0,1] op_sel_hi:[1,0,1]
v_pk_fma_f32 v[192:193], v[192:193], s[60:61], s[60:61] op_sel:[0,0,1] op_sel_hi:[1,0,1]
v_pk_fma_f32 v[194:195], v[194:195], s[60:61], s[60:61] op_sel:[0,0,1] op_sel_hi:[1,0,1]
s_add_u32 s80, s80, 8192
s_add_u32 s81, s81, 1024
s_mov_b32 m0, s81
s_nop 0
buffer_load_dwordx4 v8, s[40:43], s80 offen lds
v_pk_fma_f32 v[196:197], v[196:197], s[62:63], s[62:63] op_sel:[0,0,1] op_sel_hi:[1,0,1]
v_pk_fma_f32 v[198:199], v[198:199], s[62:63], s[62:63] op_sel:[0,0,1] op_sel_hi:[1,0,1]
v_pk_fma_f32 v[200:201], v[200:201], s[62:63], s[62:63] op_sel:[0,0,1] op_sel_hi:[1,0,1]
v_pk_fma_f32 v[202:203], v[202:203], s[62:63], s[62:63] op_sel:[0,0,1] op_sel_hi:[1,0,1]
s_add_u32 s80, s80, 8192
s_add_u32 s81, s81, 1024
s_mov_b32 m0, s81
s_nop 0
)ASM"
R"ASM(buffer_load_dwordx4 v8, s[40:43], s80 offen lds
v_pk_fma_f32 v[204:205], v[204:205], s[64:65], s[64:65] op_sel:[0,0,1] op_sel_hi:[1,0,1]
v_pk_fma_f32 v[206:207], v[206:207], s[64:65], s[64:65] op_sel:[0,0,1] op_sel_hi:[1,0,1]
v_pk_fma_f32 v[208:209], v[208:209], s[64:65], s[64:65] op_sel:[0,0,1] op_sel_hi:[1,0,1]
v_pk_fma_f32 v[210:211], v[210:211], s[64:65], s[64:65] op_sel:[0,0,1] op_sel_hi:[1,0,1]
v_pk_fma_f32 v[212:213], v[212:213], s[66:67], s[66:67] op_sel:[0,0,1] op_sel_hi:[1,0,1]
v_pk_fma_f32 v[214:215], v[214:215], s[66:67], s[66:67] op_sel:[0,0,1] op_sel_hi:[1,0,1]
v_pk_fma_f32 v[216:217], v[216:217], s[66:67], s[66:67] op_sel:[0,0,1] op_sel_hi:[1,0,1]
v_pk_fma_f32 v[218:219], v[218:219], s[66:67], s[66:67] op_sel:[0,0,1] op_sel_hi:[1,0,1]
v_pk_fma_f32 v[220:221], v[220:221], s[68:69], s[68:69] op_sel:[0,0,1] op_sel_hi:[1,0,1]
v_pk_fma_f32 v[222:223], v[222:223], s[68:69], s[68:69] op_sel:[0,0,1] op_sel_hi:[1,0,1]
v_pk_fma_f32 v[224:225], v[224:225], s[68:69], s[68:69] op_sel:[0,0,1] op_sel_hi:[1,0,1]
v_pk_fma_f32 v[226:227], v[226:227], s[68:69], s[68:69] op_sel:[0,0,1] op_sel_hi:[1,0,1]
v_pk_fma_f32 v[228:229], v[228:229], s[70:71], s[70:71] op_sel:[0,0,1] op_sel_hi:[1,0,1]
v_pk_fma_f32 v[230:231], v[230:231], s[70:71], s[70:71] op_sel:[0,0,1] op_sel_hi:[1,0,1]
v_pk_fma_f32 v[232:233], v[232:233], s[70:71], s[70:71] op_sel:[0,0,1] op_sel_hi:[1,0,1]
v_pk_fma_f32 v[234:235], v[234:235], s[70:71], s[70:71] op_sel:[0,0,1] op_sel_hi:[1,0,1]
v_pk_fma_f32 v[236:237], v[236:237], s[72:73], s[72:73] op_sel:[0,0,1] op_sel_hi:[1,0,1]
v_pk_fma_f32 v[238:239], v[238:239], s[72:73], s[72:73] op_sel:[0,0,1] op_sel_hi:[1,0,1]
v_pk_fma_f32 v[240:241], v[240:241], s[72:73], s[72:73] op_sel:[0,0,1] op_sel_hi:[1,0,1]
v_pk_fma_f32 v[242:243], v[242:243], s[72:73], s[72:73] op_sel:[0,0,1] op_sel_hi:[1,0,1]
v_pk_fma_f32 v[244:245], v[244:245], s[74:75], s[74:75] op_sel:[0,0,1] op_sel_hi:[1,0,1]
v_pk_fma_f32 v[246:247], v[246:247], s[74:75], s[74:75] op_sel:[0,0,1] op_sel_hi:[1,0,1]
v_pk_fma_f32 v[248:249], v[248:249], s[74:75], s[74:75] op_sel:[0,0,1] op_sel_hi:[1,0,1]
v_pk_fma_f32 v[250:251], v[250:251], s[74:75], s[74:75] op_sel:[0,0,1] op_sel_hi:[1,0,1]
s_mov_b32 s99, 0
Lgath_11_%=:
s_waitcnt vmcnt(23)
v_xor_b32 v10, s18, v60
v_xor_b32 v11, s18, v62
v_or_b32 v10, v10, v11
v_and_b32 v10, 0xff, v10
)ASM"
R"ASM(v_xor_b32 v11, s19, v61
v_xor_b32 v12, s19, v63
v_or3_b32 v13, v10, v11, v12
s_waitcnt vmcnt(22)
v_xor_b32 v10, s18, v64
v_xor_b32 v11, s18, v66
v_or_b32 v10, v10, v11
v_and_b32 v10, 0xff, v10
v_xor_b32 v11, s19, v65
v_xor_b32 v12, s19, v67
v_or3_b32 v10, v10, v11, v12
v_or_b32 v13, v13, v10
s_waitcnt vmcnt(21)
v_xor_b32 v10, s18, v68
v_xor_b32 v11, s18, v70
v_or_b32 v10, v10, v11
v_and_b32 v10, 0xff, v10
v_xor_b32 v11, s19, v69
v_xor_b32 v12, s19, v71
v_or3_b32 v10, v10, v11, v12
v_or_b32 v13, v13, v10
s_waitcnt vmcnt(20)
v_xor_b32 v10, s18, v72
v_xor_b32 v11, s18, v74
v_or_b32 v10, v10, v11
v_and_b32 v10, 0xff, v10
v_xor_b32 v11, s19, v73
v_xor_b32 v12, s19, v75
v_or3_b32 v10, v10, v11, v12
v_or_b32 v13, v13, v10
s_waitcnt vmcnt(19)
v_xor_b32 v10, s18, v76
)ASM"
R"ASM(v_xor_b32 v11, s18, v78
v_or_b32 v10, v10, v11
v_and_b32 v10, 0xff, v10
v_xor_b32 v11, s19, v77
v_xor_b32 v12, s19, v79
v_or3_b32 v10, v10, v11, v12
v_or_b32 v13, v13, v10
s_waitcnt vmcnt(18)
v_xor_b32 v10, s18, v80
v_xor_b32 v11, s18, v82
v_or_b32 v10, v10, v11
v_and_b32 v10, 0xff, v10
v_xor_b32 v11, s19, v81
v_xor_b32 v12, s19, v83
v_or3_b32 v10, v10, v11, v12
v_or_b32 v13, v13, v10
s_waitcnt vmcnt(17)
v_xor_b32 v10, s18, v84
v_xor_b32 v11, s18, v86
v_or_b32 v10, v10, v11
v_and_b32 v10, 0xff, v10
v_xor_b32 v11, s19, v85
v_xor_b32 v12, s19, v87
v_or3_b32 v10, v10, v11, v12
v_or_b32 v13, v13, v10
s_waitcnt vmcnt(16)
v_xor_b32 v10, s18, v88
v_xor_b32 v11, s18, v90
v_or_b32 v10, v10, v11
v_and_b32 v10, 0xff, v10
v_xor_b32 v11, s19, v89
v_xor_b32 v12, s19, v91
)ASM"
R"ASM(v_or3_b32 v10, v10, v11, v12
v_or_b32 v13, v13, v10
v_cmp_eq_u32 vcc, 0, v13
s_cmp_eq_u64 vcc, -1
s_cbranch_scc1 Lgout_12_%=
s_add_u32 s99, s99, 1
s_cmp_ge_u32 s99, 0x8000
s_cbranch_scc1 Lgout_12_%=
s_mov_b32 s98, 0x1280000
buffer_load_dwordx4 v[60:63], v9, s[20:23], s98 offen sc1
s_mov_b32 s98, 0x1282000
buffer_load_dwordx4 v[64:67], v9, s[20:23], s98 offen sc1
s_mov_b32 s98, 0x1284000
buffer_load_dwordx4 v[68:71], v9, s[20:23], s98 offen sc1
s_mov_b32 s98, 0x1286000
buffer_load_dwordx4 v[72:75], v9, s[20:23], s98 offen sc1
s_mov_b32 s98, 0x1281000
buffer_load_dwordx4 v[76:79], v9, s[20:23], s98 offen sc1
s_mov_b32 s98, 0x1283000
buffer_load_dwordx4 v[80:83], v9, s[20:23], s98 offen sc1
s_mov_b32 s98, 0x1285000
buffer_load_dwordx4 v[84:87], v9, s[20:23], s98 offen sc1
s_mov_b32 s98, 0x1287000
buffer_load_dwordx4 v[88:91], v9, s[20:23], s98 offen sc1
s_waitcnt vmcnt(0)
s_branch Lgath_11_%=
Lgout_12_%=:
v_lshlrev_b32 v10, 3, v0
v_and_b32 v60, 0xffffff00, v60
v_and_b32 v61, 0xffffff00, v62
v_and_b32 v64, 0xffffff00, v64
v_and_b32 v65, 0xffffff00, v66
)ASM"
R"ASM(v_and_b32 v68, 0xffffff00, v68
v_and_b32 v69, 0xffffff00, v70
v_and_b32 v72, 0xffffff00, v72
v_and_b32 v73, 0xffffff00, v74
v_pk_add_f32 v[60:61], v[60:61], v[64:65]
v_pk_add_f32 v[68:69], v[68:69], v[72:73]
v_pk_add_f32 v[60:61], v[60:61], v[68:69]
v_add_u32 v11, 133120, v10
ds_write_b64 v11, v[60:61]
v_and_b32 v76, 0xffffff00, v76
v_and_b32 v77, 0xffffff00, v78
v_and_b32 v80, 0xffffff00, v80
v_and_b32 v81, 0xffffff00, v82
v_and_b32 v84, 0xffffff00, v84
v_and_b32 v85, 0xffffff00, v86
v_and_b32 v88, 0xffffff00, v88
v_and_b32 v89, 0xffffff00, v90
v_pk_add_f32 v[76:77], v[76:77], v[80:81]
v_pk_add_f32 v[84:85], v[84:85], v[88:89]
v_pk_add_f32 v[76:77], v[76:77], v[84:85]
v_add_f32 v76, 1.0, v76
v_add_f32 v77, 1.0, v77
v_add_u32 v11, 135168, v10
ds_write_b64 v11, v[76:77]
s_waitcnt lgkmcnt(0)
s_barrier
v_lshlrev_b32 v26, 5, v1
v_add_u32 v26, 133120, v26
ds_read_b128 v[18:21], v26
ds_read_b128 v[22:25], v26 offset:16
ds_read_b128 v[10:13], v26 offset:2048
ds_read_b128 v[14:17], v26 offset:2064
)ASM"
R"ASM(v_mul_u32_u24 v27, 1040, v1
s_lshl_b32 s80, s13, 8
v_add_u32 v27, s80, v27
s_waitcnt lgkmcnt(1)
v_pk_fma_f32 v[124:125], v[124:125], v[10:11], v[18:19]
v_pk_fma_f32 v[126:127], v[126:127], v[12:13], v[20:21]
s_waitcnt lgkmcnt(0)
v_pk_fma_f32 v[128:129], v[128:129], v[14:15], v[22:23]
v_pk_fma_f32 v[130:131], v[130:131], v[16:17], v[24:25]
v_cvt_pk_bf16_f32 v60, v124, v125
v_cvt_pk_bf16_f32 v61, v126, v127
v_cvt_pk_bf16_f32 v62, v128, v129
v_cvt_pk_bf16_f32 v63, v130, v131
ds_write_b128 v27, v[60:63] offset:0
v_pk_fma_f32 v[132:133], v[132:133], v[10:11], v[18:19]
v_pk_fma_f32 v[134:135], v[134:135], v[12:13], v[20:21]
v_pk_fma_f32 v[136:137], v[136:137], v[14:15], v[22:23]
v_pk_fma_f32 v[138:139], v[138:139], v[16:17], v[24:25]
v_cvt_pk_bf16_f32 v64, v132, v133
v_cvt_pk_bf16_f32 v65, v134, v135
v_cvt_pk_bf16_f32 v66, v136, v137
v_cvt_pk_bf16_f32 v67, v138, v139
ds_write_b128 v27, v[64:67] offset:16
v_pk_fma_f32 v[140:141], v[140:141], v[10:11], v[18:19]
v_pk_fma_f32 v[142:143], v[142:143], v[12:13], v[20:21]
v_pk_fma_f32 v[144:145], v[144:145], v[14:15], v[22:23]
v_pk_fma_f32 v[146:147], v[146:147], v[16:17], v[24:25]
v_cvt_pk_bf16_f32 v68, v140, v141
v_cvt_pk_bf16_f32 v69, v142, v143
v_cvt_pk_bf16_f32 v70, v144, v145
v_cvt_pk_bf16_f32 v71, v146, v147
ds_write_b128 v27, v[68:71] offset:32
)ASM"
R"ASM(v_pk_fma_f32 v[148:149], v[148:149], v[10:11], v[18:19]
v_pk_fma_f32 v[150:151], v[150:151], v[12:13], v[20:21]
v_pk_fma_f32 v[152:153], v[152:153], v[14:15], v[22:23]
v_pk_fma_f32 v[154:155], v[154:155], v[16:17], v[24:25]
v_cvt_pk_bf16_f32 v72, v148, v149
v_cvt_pk_bf16_f32 v73, v150, v151
v_cvt_pk_bf16_f32 v74, v152, v153
v_cvt_pk_bf16_f32 v75, v154, v155
ds_write_b128 v27, v[72:75] offset:48
v_pk_fma_f32 v[156:157], v[156:157], v[10:11], v[18:19]
v_pk_fma_f32 v[158:159], v[158:159], v[12:13], v[20:21]
v_pk_fma_f32 v[160:161], v[160:161], v[14:15], v[22:23]
v_pk_fma_f32 v[162:163], v[162:163], v[16:17], v[24:25]
v_cvt_pk_bf16_f32 v76, v156, v157
v_cvt_pk_bf16_f32 v77, v158, v159
v_cvt_pk_bf16_f32 v78, v160, v161
v_cvt_pk_bf16_f32 v79, v162, v163
ds_write_b128 v27, v[76:79] offset:64
v_pk_fma_f32 v[164:165], v[164:165], v[10:11], v[18:19]
v_pk_fma_f32 v[166:167], v[166:167], v[12:13], v[20:21]
v_pk_fma_f32 v[168:169], v[168:169], v[14:15], v[22:23]
v_pk_fma_f32 v[170:171], v[170:171], v[16:17], v[24:25]
v_cvt_pk_bf16_f32 v80, v164, v165
v_cvt_pk_bf16_f32 v81, v166, v167
v_cvt_pk_bf16_f32 v82, v168, v169
v_cvt_pk_bf16_f32 v83, v170, v171
ds_write_b128 v27, v[80:83] offset:80
v_pk_fma_f32 v[172:173], v[172:173], v[10:11], v[18:19]
v_pk_fma_f32 v[174:175], v[174:175], v[12:13], v[20:21]
v_pk_fma_f32 v[176:177], v[176:177], v[14:15], v[22:23]
v_pk_fma_f32 v[178:179], v[178:179], v[16:17], v[24:25]
v_cvt_pk_bf16_f32 v84, v172, v173
)ASM"
R"ASM(v_cvt_pk_bf16_f32 v85, v174, v175
v_cvt_pk_bf16_f32 v86, v176, v177
v_cvt_pk_bf16_f32 v87, v178, v179
ds_write_b128 v27, v[84:87] offset:96
v_pk_fma_f32 v[180:181], v[180:181], v[10:11], v[18:19]
v_pk_fma_f32 v[182:183], v[182:183], v[12:13], v[20:21]
v_pk_fma_f32 v[184:185], v[184:185], v[14:15], v[22:23]
v_pk_fma_f32 v[186:187], v[186:187], v[16:17], v[24:25]
v_cvt_pk_bf16_f32 v88, v180, v181
v_cvt_pk_bf16_f32 v89, v182, v183
v_cvt_pk_bf16_f32 v90, v184, v185
v_cvt_pk_bf16_f32 v91, v186, v187
ds_write_b128 v27, v[88:91] offset:112
v_pk_fma_f32 v[188:189], v[188:189], v[10:11], v[18:19]
v_pk_fma_f32 v[190:191], v[190:191], v[12:13], v[20:21]
v_pk_fma_f32 v[192:193], v[192:193], v[14:15], v[22:23]
v_pk_fma_f32 v[194:195], v[194:195], v[16:17], v[24:25]
v_cvt_pk_bf16_f32 v92, v188, v189
v_cvt_pk_bf16_f32 v93, v190, v191
v_cvt_pk_bf16_f32 v94, v192, v193
v_cvt_pk_bf16_f32 v95, v194, v195
ds_write_b128 v27, v[92:95] offset:128
v_pk_fma_f32 v[196:197], v[196:197], v[10:11], v[18:19]
v_pk_fma_f32 v[198:199], v[198:199], v[12:13], v[20:21]
v_pk_fma_f32 v[200:201], v[200:201], v[14:15], v[22:23]
v_pk_fma_f32 v[202:203], v[202:203], v[16:17], v[24:25]
v_cvt_pk_bf16_f32 v96, v196, v197
v_cvt_pk_bf16_f32 v97, v198, v199
v_cvt_pk_bf16_f32 v98, v200, v201
v_cvt_pk_bf16_f32 v99, v202, v203
ds_write_b128 v27, v[96:99] offset:144
v_pk_fma_f32 v[204:205], v[204:205], v[10:11], v[18:19]
)ASM"
R"ASM(v_pk_fma_f32 v[206:207], v[206:207], v[12:13], v[20:21]
v_pk_fma_f32 v[208:209], v[208:209], v[14:15], v[22:23]
v_pk_fma_f32 v[210:211], v[210:211], v[16:17], v[24:25]
v_cvt_pk_bf16_f32 v100, v204, v205
v_cvt_pk_bf16_f32 v101, v206, v207
v_cvt_pk_bf16_f32 v102, v208, v209
v_cvt_pk_bf16_f32 v103, v210, v211
ds_write_b128 v27, v[100:103] offset:160
v_pk_fma_f32 v[212:213], v[212:213], v[10:11], v[18:19]
v_pk_fma_f32 v[214:215], v[214:215], v[12:13], v[20:21]
v_pk_fma_f32 v[216:217], v[216:217], v[14:15], v[22:23]
v_pk_fma_f32 v[218:219], v[218:219], v[16:17], v[24:25]
v_cvt_pk_bf16_f32 v104, v212, v213
v_cvt_pk_bf16_f32 v105, v214, v215
v_cvt_pk_bf16_f32 v106, v216, v217
v_cvt_pk_bf16_f32 v107, v218, v219
ds_write_b128 v27, v[104:107] offset:176
v_pk_fma_f32 v[220:221], v[220:221], v[10:11], v[18:19]
v_pk_fma_f32 v[222:223], v[222:223], v[12:13], v[20:21]
v_pk_fma_f32 v[224:225], v[224:225], v[14:15], v[22:23]
v_pk_fma_f32 v[226:227], v[226:227], v[16:17], v[24:25]
v_cvt_pk_bf16_f32 v108, v220, v221
v_cvt_pk_bf16_f32 v109, v222, v223
v_cvt_pk_bf16_f32 v110, v224, v225
v_cvt_pk_bf16_f32 v111, v226, v227
ds_write_b128 v27, v[108:111] offset:192
v_pk_fma_f32 v[228:229], v[228:229], v[10:11], v[18:19]
v_pk_fma_f32 v[230:231], v[230:231], v[12:13], v[20:21]
v_pk_fma_f32 v[232:233], v[232:233], v[14:15], v[22:23]
v_pk_fma_f32 v[234:235], v[234:235], v[16:17], v[24:25]
v_cvt_pk_bf16_f32 v112, v228, v229
v_cvt_pk_bf16_f32 v113, v230, v231
)ASM"
R"ASM(v_cvt_pk_bf16_f32 v114, v232, v233
v_cvt_pk_bf16_f32 v115, v234, v235
ds_write_b128 v27, v[112:115] offset:208
v_pk_fma_f32 v[236:237], v[236:237], v[10:11], v[18:19]
v_pk_fma_f32 v[238:239], v[238:239], v[12:13], v[20:21]
v_pk_fma_f32 v[240:241], v[240:241], v[14:15], v[22:23]
v_pk_fma_f32 v[242:243], v[242:243], v[16:17], v[24:25]
v_cvt_pk_bf16_f32 v116, v236, v237
v_cvt_pk_bf16_f32 v117, v238, v239
v_cvt_pk_bf16_f32 v118, v240, v241
v_cvt_pk_bf16_f32 v119, v242, v243
ds_write_b128 v27, v[116:119] offset:224
v_pk_fma_f32 v[244:245], v[244:245], v[10:11], v[18:19]
v_pk_fma_f32 v[246:247], v[246:247], v[12:13], v[20:21]
v_pk_fma_f32 v[248:249], v[248:249], v[14:15], v[22:23]
v_pk_fma_f32 v[250:251], v[250:251], v[16:17], v[24:25]
v_cvt_pk_bf16_f32 v120, v244, v245
v_cvt_pk_bf16_f32 v121, v246, v247
v_cvt_pk_bf16_f32 v122, v248, v249
v_cvt_pk_bf16_f32 v123, v250, v251
ds_write_b128 v27, v[120:123] offset:240
s_lshr_b32 s80, s15, 1
s_cmp_lg_u32 s13, s80
s_cbranch_scc1 Lnokv_13_%=
s_waitcnt lgkmcnt(0)
v_lshrrev_b32 v11, 4, v1
v_mul_u32_u24 v8, 1040, v11
v_and_b32 v26, 15, v1
v_lshlrev_b32 v26, 4, v26
v_add_u32 v8, v8, v26
s_lshl_b32 s80, s13, 8
v_add_u32 v8, s80, v8
)ASM"
R"ASM(v_lshlrev_b32 v10, 8, v11
v_and_b32 v26, 12, v1
v_lshlrev_b32 v26, 3, v26
v_add_u32 v10, v10, v26
v_and_b32 v26, 3, v1
v_lshlrev_b32 v26, 3, v26
v_add_u32 v10, v10, v26
v_add_u32 v10, 145408, v10
s_bitcmp1_b32 s15, 0
s_cbranch_scc1 Lisv_14_%=
ds_read_b128 v[12:15], v8 offset:0
ds_read_b64_tr_b16 v[40:41], v10 offset:0
ds_read_b64_tr_b16 v[42:43], v10 offset:128
ds_read_b128 v[16:19], v8 offset:4160
ds_read_b64_tr_b16 v[44:45], v10 offset:1024
ds_read_b64_tr_b16 v[46:47], v10 offset:1152
ds_read_b128 v[20:23], v8 offset:8320
ds_read_b64_tr_b16 v[48:49], v10 offset:2048
ds_read_b64_tr_b16 v[50:51], v10 offset:2176
ds_read_b128 v[28:31], v8 offset:12480
ds_read_b64_tr_b16 v[52:53], v10 offset:3072
ds_read_b64_tr_b16 v[54:55], v10 offset:3200
ds_read_b128 v[32:35], v8 offset:16640
ds_read_b64_tr_b16 v[56:57], v10 offset:4096
ds_read_b64_tr_b16 v[58:59], v10 offset:4224
s_waitcnt lgkmcnt(12)
v_mfma_f32_16x16x32_bf16 v[64:67], v[40:43], v[12:15], 0
ds_read_b128 v[36:39], v8 offset:20800
ds_read_b64_tr_b16 v[60:61], v10 offset:5120
ds_read_b64_tr_b16 v[62:63], v10 offset:5248
s_waitcnt lgkmcnt(12)
v_mfma_f32_16x16x32_bf16 v[64:67], v[44:47], v[16:19], v[64:67]
)ASM"
R"ASM(ds_read_b128 v[12:15], v8 offset:24960
ds_read_b64_tr_b16 v[40:41], v10 offset:6144
ds_read_b64_tr_b16 v[42:43], v10 offset:6272
s_waitcnt lgkmcnt(12)
v_mfma_f32_16x16x32_bf16 v[64:67], v[48:51], v[20:23], v[64:67]
ds_read_b128 v[16:19], v8 offset:29120
ds_read_b64_tr_b16 v[44:45], v10 offset:7168
ds_read_b64_tr_b16 v[46:47], v10 offset:7296
s_waitcnt lgkmcnt(12)
v_mfma_f32_16x16x32_bf16 v[64:67], v[52:55], v[28:31], v[64:67]
ds_read_b128 v[20:23], v8 offset:33280
ds_read_b64_tr_b16 v[48:49], v10 offset:8192
ds_read_b64_tr_b16 v[50:51], v10 offset:8320
s_waitcnt lgkmcnt(12)
v_mfma_f32_16x16x32_bf16 v[64:67], v[56:59], v[32:35], v[64:67]
ds_read_b128 v[28:31], v8 offset:37440
ds_read_b64_tr_b16 v[52:53], v10 offset:9216
ds_read_b64_tr_b16 v[54:55], v10 offset:9344
s_waitcnt lgkmcnt(12)
v_mfma_f32_16x16x32_bf16 v[64:67], v[60:63], v[36:39], v[64:67]
ds_read_b128 v[32:35], v8 offset:41600
ds_read_b64_tr_b16 v[56:57], v10 offset:10240
ds_read_b64_tr_b16 v[58:59], v10 offset:10368
s_waitcnt lgkmcnt(12)
v_mfma_f32_16x16x32_bf16 v[64:67], v[40:43], v[12:15], v[64:67]
ds_read_b128 v[36:39], v8 offset:45760
ds_read_b64_tr_b16 v[60:61], v10 offset:11264
ds_read_b64_tr_b16 v[62:63], v10 offset:11392
s_waitcnt lgkmcnt(12)
v_mfma_f32_16x16x32_bf16 v[64:67], v[44:47], v[16:19], v[64:67]
ds_read_b128 v[12:15], v8 offset:49920
ds_read_b64_tr_b16 v[40:41], v10 offset:12288
)ASM"
R"ASM(ds_read_b64_tr_b16 v[42:43], v10 offset:12416
s_waitcnt lgkmcnt(12)
v_mfma_f32_16x16x32_bf16 v[64:67], v[48:51], v[20:23], v[64:67]
ds_read_b128 v[16:19], v8 offset:54080
ds_read_b64_tr_b16 v[44:45], v10 offset:13312
ds_read_b64_tr_b16 v[46:47], v10 offset:13440
s_waitcnt lgkmcnt(12)
v_mfma_f32_16x16x32_bf16 v[64:67], v[52:55], v[28:31], v[64:67]
ds_read_b128 v[20:23], v8 offset:58240
ds_read_b64_tr_b16 v[48:49], v10 offset:14336
ds_read_b64_tr_b16 v[50:51], v10 offset:14464
s_waitcnt lgkmcnt(12)
v_mfma_f32_16x16x32_bf16 v[64:67], v[56:59], v[32:35], v[64:67]
ds_read_b128 v[28:31], v8 offset:62400
ds_read_b64_tr_b16 v[52:53], v10 offset:15360
ds_read_b64_tr_b16 v[54:55], v10 offset:15488
s_waitcnt lgkmcnt(12)
v_mfma_f32_16x16x32_bf16 v[64:67], v[60:63], v[36:39], v[64:67]
s_waitcnt lgkmcnt(9)
v_mfma_f32_16x16x32_bf16 v[64:67], v[40:43], v[12:15], v[64:67]
s_waitcnt lgkmcnt(6)
v_mfma_f32_16x16x32_bf16 v[64:67], v[44:47], v[16:19], v[64:67]
s_waitcnt lgkmcnt(3)
v_mfma_f32_16x16x32_bf16 v[64:67], v[48:51], v[20:23], v[64:67]
s_waitcnt lgkmcnt(0)
v_mfma_f32_16x16x32_bf16 v[64:67], v[52:55], v[28:31], v[64:67]
s_lshl_b32 s80, s14, 1
s_lshr_b32 s81, s15, 2
s_add_u32 s80, s80, s81
s_lshl_b32 s80, s80, 10
s_lshl_b32 s81, s15, 7
s_and_b32 s81, s81, 0x100
)ASM"
R"ASM(s_add_u32 s80, s80, s81
s_add_u32 s80, s80, 262144
v_and_b32 v26, 1, v11
v_lshlrev_b32 v26, 9, v26
v_and_b32 v27, 15, v1
v_lshlrev_b32 v27, 4, v27
v_add_u32 v26, v26, v27
v_lshrrev_b32 v27, 1, v11
v_lshlrev_b32 v27, 3, v27
v_add_u32 v26, v26, v27
v_cvt_pk_bf16_f32 v12, v64, v65
v_cvt_pk_bf16_f32 v13, v66, v67
buffer_store_dwordx2 v[12:13], v26, s[20:23], s80 offen sc1
s_branch Lkvdone_15_%=
Lisv_14_%=:
ds_read_b128 v[12:15], v8 offset:0
ds_read_b64_tr_b16 v[40:41], v10 offset:0
ds_read_b64_tr_b16 v[42:43], v10 offset:128
ds_read_b128 v[16:19], v8 offset:4160
ds_read_b64_tr_b16 v[44:45], v10 offset:1024
ds_read_b64_tr_b16 v[46:47], v10 offset:1152
ds_read_b128 v[20:23], v8 offset:8320
ds_read_b64_tr_b16 v[48:49], v10 offset:2048
ds_read_b64_tr_b16 v[50:51], v10 offset:2176
ds_read_b128 v[28:31], v8 offset:12480
ds_read_b64_tr_b16 v[52:53], v10 offset:3072
ds_read_b64_tr_b16 v[54:55], v10 offset:3200
ds_read_b128 v[32:35], v8 offset:16640
ds_read_b64_tr_b16 v[56:57], v10 offset:4096
ds_read_b64_tr_b16 v[58:59], v10 offset:4224
s_waitcnt lgkmcnt(12)
v_mfma_f32_16x16x32_bf16 v[64:67], v[12:15], v[40:43], 0
)ASM"
R"ASM(ds_read_b128 v[36:39], v8 offset:20800
ds_read_b64_tr_b16 v[60:61], v10 offset:5120
ds_read_b64_tr_b16 v[62:63], v10 offset:5248
s_waitcnt lgkmcnt(12)
v_mfma_f32_16x16x32_bf16 v[64:67], v[16:19], v[44:47], v[64:67]
ds_read_b128 v[12:15], v8 offset:24960
ds_read_b64_tr_b16 v[40:41], v10 offset:6144
ds_read_b64_tr_b16 v[42:43], v10 offset:6272
s_waitcnt lgkmcnt(12)
v_mfma_f32_16x16x32_bf16 v[64:67], v[20:23], v[48:51], v[64:67]
ds_read_b128 v[16:19], v8 offset:29120
ds_read_b64_tr_b16 v[44:45], v10 offset:7168
ds_read_b64_tr_b16 v[46:47], v10 offset:7296
s_waitcnt lgkmcnt(12)
v_mfma_f32_16x16x32_bf16 v[64:67], v[28:31], v[52:55], v[64:67]
ds_read_b128 v[20:23], v8 offset:33280
ds_read_b64_tr_b16 v[48:49], v10 offset:8192
ds_read_b64_tr_b16 v[50:51], v10 offset:8320
s_waitcnt lgkmcnt(12)
v_mfma_f32_16x16x32_bf16 v[64:67], v[32:35], v[56:59], v[64:67]
ds_read_b128 v[28:31], v8 offset:37440
ds_read_b64_tr_b16 v[52:53], v10 offset:9216
ds_read_b64_tr_b16 v[54:55], v10 offset:9344
s_waitcnt lgkmcnt(12)
v_mfma_f32_16x16x32_bf16 v[64:67], v[36:39], v[60:63], v[64:67]
ds_read_b128 v[32:35], v8 offset:41600
ds_read_b64_tr_b16 v[56:57], v10 offset:10240
ds_read_b64_tr_b16 v[58:59], v10 offset:10368
s_waitcnt lgkmcnt(12)
v_mfma_f32_16x16x32_bf16 v[64:67], v[12:15], v[40:43], v[64:67]
ds_read_b128 v[36:39], v8 offset:45760
ds_read_b64_tr_b16 v[60:61], v10 offset:11264
)ASM"
R"ASM(ds_read_b64_tr_b16 v[62:63], v10 offset:11392
s_waitcnt lgkmcnt(12)
v_mfma_f32_16x16x32_bf16 v[64:67], v[16:19], v[44:47], v[64:67]
ds_read_b128 v[12:15], v8 offset:49920
ds_read_b64_tr_b16 v[40:41], v10 offset:12288
ds_read_b64_tr_b16 v[42:43], v10 offset:12416
s_waitcnt lgkmcnt(12)
v_mfma_f32_16x16x32_bf16 v[64:67], v[20:23], v[48:51], v[64:67]
ds_read_b128 v[16:19], v8 offset:54080
ds_read_b64_tr_b16 v[44:45], v10 offset:13312
ds_read_b64_tr_b16 v[46:47], v10 offset:13440
s_waitcnt lgkmcnt(12)
v_mfma_f32_16x16x32_bf16 v[64:67], v[28:31], v[52:55], v[64:67]
ds_read_b128 v[20:23], v8 offset:58240
ds_read_b64_tr_b16 v[48:49], v10 offset:14336
ds_read_b64_tr_b16 v[50:51], v10 offset:14464
s_waitcnt lgkmcnt(12)
v_mfma_f32_16x16x32_bf16 v[64:67], v[32:35], v[56:59], v[64:67]
ds_read_b128 v[28:31], v8 offset:62400
ds_read_b64_tr_b16 v[52:53], v10 offset:15360
ds_read_b64_tr_b16 v[54:55], v10 offset:15488
s_waitcnt lgkmcnt(12)
v_mfma_f32_16x16x32_bf16 v[64:67], v[36:39], v[60:63], v[64:67]
s_waitcnt lgkmcnt(9)
v_mfma_f32_16x16x32_bf16 v[64:67], v[12:15], v[40:43], v[64:67]
s_waitcnt lgkmcnt(6)
v_mfma_f32_16x16x32_bf16 v[64:67], v[16:19], v[44:47], v[64:67]
s_waitcnt lgkmcnt(3)
v_mfma_f32_16x16x32_bf16 v[64:67], v[20:23], v[48:51], v[64:67]
s_waitcnt lgkmcnt(0)
v_mfma_f32_16x16x32_bf16 v[64:67], v[28:31], v[52:55], v[64:67]
s_lshl_b32 s80, s14, 1
)ASM"
R"ASM(s_lshr_b32 s81, s15, 2
s_add_u32 s80, s80, s81
s_mul_i32 s80, s80, 1056
s_bfe_u32 s81, s15, 0x10001
s_lshl_b32 s81, s81, 9
s_add_u32 s80, s80, s81
s_add_u32 s80, s80, 131072
v_and_b32 v26, 1, v11
v_lshlrev_b32 v26, 8, v26
v_and_b32 v27, 15, v1
v_lshlrev_b32 v27, 4, v27
v_add_u32 v26, v26, v27
v_lshrrev_b32 v27, 1, v11
v_lshlrev_b32 v27, 3, v27
v_add_u32 v26, v26, v27
v_cvt_pk_bf16_f32 v12, v64, v65
v_cvt_pk_bf16_f32 v13, v66, v67
buffer_store_dwordx2 v[12:13], v26, s[20:23], s80 offen sc1
Lkvdone_15_%=:
Lnokv_13_%=:
s_mov_b32 s98, 0x1288000
buffer_load_dwordx4 v[28:31], v9, s[20:23], s98 offen sc1
s_mov_b32 s98, 0x128c000
buffer_load_dwordx4 v[32:35], v9, s[20:23], s98 offen sc1
s_mov_b32 s98, 0x1289000
buffer_load_dwordx4 v[36:39], v9, s[20:23], s98 offen sc1
s_mov_b32 s98, 0x128d000
buffer_load_dwordx4 v[40:43], v9, s[20:23], s98 offen sc1
s_mov_b32 s98, 0x128a000
buffer_load_dwordx4 v[44:47], v9, s[20:23], s98 offen sc1
s_mov_b32 s98, 0x128e000
buffer_load_dwordx4 v[48:51], v9, s[20:23], s98 offen sc1
)ASM"
R"ASM(s_mov_b32 s98, 0x128b000
buffer_load_dwordx4 v[52:55], v9, s[20:23], s98 offen sc1
s_mov_b32 s98, 0x128f000
buffer_load_dwordx4 v[56:59], v9, s[20:23], s98 offen sc1
v_lshrrev_b32 v8, 3, v1
v_mul_lo_u32 v10, v8, s91
v_and_b32 v8, 7, v1
v_lshl_add_u32 v10, v8, 4, v10
s_lshl_b32 s80, s91, 3
v_add_u32 v11, s80, v10
s_lshr_b32 s80, s15, 1
s_cmp_lg_u32 s13, s80
s_cbranch_scc1 Lkvw_16_%=
s_waitcnt vmcnt(9)
s_branch Lkvj_17_%=
Lkvw_16_%=:
s_waitcnt vmcnt(8)
Lkvj_17_%=:
s_waitcnt lgkmcnt(0)
s_barrier
v_mul_u32_u24 v8, 1040, v2
v_lshlrev_b32 v26, 4, v4
v_add_u32 v8, v8, v26
s_lshl_b32 s80, s100, 9
v_add_u32 v8, s80, v8
v_bfe_u32 v13, v1, 2, 2
v_lshlrev_b32 v12, 3, v2
v_add_u32 v12, v12, v13
v_lshlrev_b32 v12, 7, v12
v_bfe_u32 v14, v1, 4, 1
v_lshlrev_b32 v14, 1, v14
v_bfe_u32 v26, v1, 1, 1
)ASM"
R"ASM(v_or_b32 v14, v14, v26
s_lshl_b32 s80, s101, 2
v_or_b32 v14, s80, v14
v_lshrrev_b32 v26, 1, v13
v_lshlrev_b32 v26, 2, v26
v_xor_b32 v14, v14, v26
v_lshlrev_b32 v14, 4, v14
v_add_u32 v12, v12, v14
v_and_b32 v26, 1, v1
v_lshlrev_b32 v26, 3, v26
v_add_u32 v12, v12, v26
v_add_u32 v12, 66560, v12
ds_read_b128 v[14:17], v8 offset:0
ds_read_b64_tr_b16 v[72:73], v12 offset:0
ds_read_b64_tr_b16 v[74:75], v12 offset:512
ds_read_b128 v[18:21], v8 offset:2080
ds_read_b64_tr_b16 v[76:77], v12 offset:2048
ds_read_b64_tr_b16 v[78:79], v12 offset:2560
ds_read_b128 v[22:25], v8 offset:4160
ds_read_b64_tr_b16 v[80:81], v12 offset:4096
ds_read_b64_tr_b16 v[82:83], v12 offset:4608
ds_read_b128 v[60:63], v8 offset:6240
ds_read_b64_tr_b16 v[84:85], v12 offset:6144
ds_read_b64_tr_b16 v[86:87], v12 offset:6656
ds_read_b128 v[64:67], v8 offset:8320
ds_read_b64_tr_b16 v[88:89], v12 offset:8192
ds_read_b64_tr_b16 v[90:91], v12 offset:8704
s_waitcnt lgkmcnt(12)
v_mfma_f32_32x32x16_bf16 v[96:111], v[72:75], v[14:17], 0
ds_read_b128 v[68:71], v8 offset:10400
ds_read_b64_tr_b16 v[92:93], v12 offset:10240
ds_read_b64_tr_b16 v[94:95], v12 offset:10752
)ASM"
R"ASM(s_waitcnt lgkmcnt(12)
v_mfma_f32_32x32x16_bf16 v[96:111], v[76:79], v[18:21], v[96:111]
buffer_load_dwordx4 a[0:3], v10, s[32:35], s92 offen
ds_read_b128 v[14:17], v8 offset:12480
ds_read_b64_tr_b16 v[72:73], v12 offset:12288
ds_read_b64_tr_b16 v[74:75], v12 offset:12800
s_waitcnt lgkmcnt(12)
v_mfma_f32_32x32x16_bf16 v[96:111], v[80:83], v[22:25], v[96:111]
ds_read_b128 v[18:21], v8 offset:14560
ds_read_b64_tr_b16 v[76:77], v12 offset:14336
ds_read_b64_tr_b16 v[78:79], v12 offset:14848
s_waitcnt lgkmcnt(12)
v_mfma_f32_32x32x16_bf16 v[96:111], v[84:87], v[60:63], v[96:111]
buffer_load_dwordx4 a[4:7], v11, s[32:35], s92 offen
ds_read_b128 v[22:25], v8 offset:16640
ds_read_b64_tr_b16 v[80:81], v12 offset:16384
ds_read_b64_tr_b16 v[82:83], v12 offset:16896
s_waitcnt lgkmcnt(12)
v_mfma_f32_32x32x16_bf16 v[96:111], v[88:91], v[64:67], v[96:111]
ds_read_b128 v[60:63], v8 offset:18720
ds_read_b64_tr_b16 v[84:85], v12 offset:18432
ds_read_b64_tr_b16 v[86:87], v12 offset:18944
s_waitcnt lgkmcnt(12)
v_mfma_f32_32x32x16_bf16 v[96:111], v[92:95], v[68:71], v[96:111]
s_add_u32 s93, s93, s92
buffer_load_dwordx4 a[8:11], v10, s[36:39], s93 offen
ds_read_b128 v[64:67], v8 offset:20800
ds_read_b64_tr_b16 v[88:89], v12 offset:20480
ds_read_b64_tr_b16 v[90:91], v12 offset:20992
s_waitcnt lgkmcnt(12)
v_mfma_f32_32x32x16_bf16 v[96:111], v[72:75], v[14:17], v[96:111]
ds_read_b128 v[68:71], v8 offset:22880
)ASM"
R"ASM(ds_read_b64_tr_b16 v[92:93], v12 offset:22528
ds_read_b64_tr_b16 v[94:95], v12 offset:23040
s_waitcnt lgkmcnt(12)
v_mfma_f32_32x32x16_bf16 v[96:111], v[76:79], v[18:21], v[96:111]
buffer_load_dwordx4 a[12:15], v11, s[36:39], s93 offen
ds_read_b128 v[14:17], v8 offset:24960
ds_read_b64_tr_b16 v[72:73], v12 offset:24576
ds_read_b64_tr_b16 v[74:75], v12 offset:25088
s_waitcnt lgkmcnt(12)
v_mfma_f32_32x32x16_bf16 v[96:111], v[80:83], v[22:25], v[96:111]
s_lshr_b32 s80, s15, 1
s_cmp_lg_u32 s13, s80
s_cbranch_scc1 Lnokvf_18_%=
s_waitcnt vmcnt(12)
v_mov_b32 v112, s18
v_mov_b32 v113, s19
v_mov_b32 v13, 0
s_lshl_b32 s98, s10, 7
s_add_u32 s98, s98, 0x8000
s_mov_b64 exec, 1
buffer_store_dwordx2 v[112:113], v13, s[20:23], s98 offen sc1
s_mov_b64 exec, -1
Lnokvf_18_%=:
ds_read_b128 v[18:21], v8 offset:27040
ds_read_b64_tr_b16 v[76:77], v12 offset:26624
ds_read_b64_tr_b16 v[78:79], v12 offset:27136
s_waitcnt lgkmcnt(12)
v_mfma_f32_32x32x16_bf16 v[96:111], v[84:87], v[60:63], v[96:111]
s_add_u32 s92, s92, s94
buffer_load_dwordx4 a[16:19], v10, s[32:35], s92 offen
ds_read_b128 v[22:25], v8 offset:29120
ds_read_b64_tr_b16 v[80:81], v12 offset:28672
)ASM"
R"ASM(ds_read_b64_tr_b16 v[82:83], v12 offset:29184
s_waitcnt lgkmcnt(12)
v_mfma_f32_32x32x16_bf16 v[96:111], v[88:91], v[64:67], v[96:111]
ds_read_b128 v[60:63], v8 offset:31200
ds_read_b64_tr_b16 v[84:85], v12 offset:30720
ds_read_b64_tr_b16 v[86:87], v12 offset:31232
s_waitcnt lgkmcnt(12)
v_mfma_f32_32x32x16_bf16 v[96:111], v[92:95], v[68:71], v[96:111]
buffer_load_dwordx4 a[20:23], v11, s[32:35], s92 offen
ds_read_b128 v[64:67], v8 offset:33280
ds_read_b64_tr_b16 v[88:89], v12 offset:32768
ds_read_b64_tr_b16 v[90:91], v12 offset:33280
s_waitcnt lgkmcnt(12)
v_mfma_f32_32x32x16_bf16 v[96:111], v[72:75], v[14:17], v[96:111]
ds_read_b128 v[68:71], v8 offset:35360
ds_read_b64_tr_b16 v[92:93], v12 offset:34816
ds_read_b64_tr_b16 v[94:95], v12 offset:35328
s_waitcnt lgkmcnt(12)
v_mfma_f32_32x32x16_bf16 v[96:111], v[76:79], v[18:21], v[96:111]
s_add_u32 s93, s93, s94
buffer_load_dwordx4 a[24:27], v10, s[36:39], s93 offen
ds_read_b128 v[14:17], v8 offset:37440
ds_read_b64_tr_b16 v[72:73], v12 offset:36864
ds_read_b64_tr_b16 v[74:75], v12 offset:37376
s_waitcnt lgkmcnt(12)
v_mfma_f32_32x32x16_bf16 v[96:111], v[80:83], v[22:25], v[96:111]
ds_read_b128 v[18:21], v8 offset:39520
ds_read_b64_tr_b16 v[76:77], v12 offset:38912
ds_read_b64_tr_b16 v[78:79], v12 offset:39424
s_waitcnt lgkmcnt(12)
v_mfma_f32_32x32x16_bf16 v[96:111], v[84:87], v[60:63], v[96:111]
buffer_load_dwordx4 a[28:31], v11, s[36:39], s93 offen
)ASM"
R"ASM(ds_read_b128 v[22:25], v8 offset:41600
ds_read_b64_tr_b16 v[80:81], v12 offset:40960
ds_read_b64_tr_b16 v[82:83], v12 offset:41472
s_waitcnt lgkmcnt(12)
v_mfma_f32_32x32x16_bf16 v[96:111], v[88:91], v[64:67], v[96:111]
ds_read_b128 v[60:63], v8 offset:43680
ds_read_b64_tr_b16 v[84:85], v12 offset:43008
ds_read_b64_tr_b16 v[86:87], v12 offset:43520
s_waitcnt lgkmcnt(12)
v_mfma_f32_32x32x16_bf16 v[96:111], v[92:95], v[68:71], v[96:111]
ds_read_b128 v[64:67], v8 offset:45760
ds_read_b64_tr_b16 v[88:89], v12 offset:45056
ds_read_b64_tr_b16 v[90:91], v12 offset:45568
s_waitcnt lgkmcnt(12)
v_mfma_f32_32x32x16_bf16 v[96:111], v[72:75], v[14:17], v[96:111]
ds_read_b128 v[68:71], v8 offset:47840
ds_read_b64_tr_b16 v[92:93], v12 offset:47104
ds_read_b64_tr_b16 v[94:95], v12 offset:47616
s_waitcnt lgkmcnt(12)
v_mfma_f32_32x32x16_bf16 v[96:111], v[76:79], v[18:21], v[96:111]
ds_read_b128 v[14:17], v8 offset:49920
ds_read_b64_tr_b16 v[72:73], v12 offset:49152
ds_read_b64_tr_b16 v[74:75], v12 offset:49664
s_waitcnt lgkmcnt(12)
v_mfma_f32_32x32x16_bf16 v[96:111], v[80:83], v[22:25], v[96:111]
ds_read_b128 v[18:21], v8 offset:52000
ds_read_b64_tr_b16 v[76:77], v12 offset:51200
ds_read_b64_tr_b16 v[78:79], v12 offset:51712
s_waitcnt lgkmcnt(12)
v_mfma_f32_32x32x16_bf16 v[96:111], v[84:87], v[60:63], v[96:111]
ds_read_b128 v[22:25], v8 offset:54080
ds_read_b64_tr_b16 v[80:81], v12 offset:53248
)ASM"
R"ASM(ds_read_b64_tr_b16 v[82:83], v12 offset:53760
s_waitcnt lgkmcnt(12)
v_mfma_f32_32x32x16_bf16 v[96:111], v[88:91], v[64:67], v[96:111]
ds_read_b128 v[60:63], v8 offset:56160
ds_read_b64_tr_b16 v[84:85], v12 offset:55296
ds_read_b64_tr_b16 v[86:87], v12 offset:55808
s_waitcnt lgkmcnt(12)
v_mfma_f32_32x32x16_bf16 v[96:111], v[92:95], v[68:71], v[96:111]
ds_read_b128 v[64:67], v8 offset:58240
ds_read_b64_tr_b16 v[88:89], v12 offset:57344
ds_read_b64_tr_b16 v[90:91], v12 offset:57856
s_waitcnt lgkmcnt(12)
v_mfma_f32_32x32x16_bf16 v[96:111], v[72:75], v[14:17], v[96:111]
ds_read_b128 v[68:71], v8 offset:60320
ds_read_b64_tr_b16 v[92:93], v12 offset:59392
ds_read_b64_tr_b16 v[94:95], v12 offset:59904
s_waitcnt lgkmcnt(12)
v_mfma_f32_32x32x16_bf16 v[96:111], v[76:79], v[18:21], v[96:111]
ds_read_b128 v[14:17], v8 offset:62400
ds_read_b64_tr_b16 v[72:73], v12 offset:61440
ds_read_b64_tr_b16 v[74:75], v12 offset:61952
s_waitcnt lgkmcnt(12)
v_mfma_f32_32x32x16_bf16 v[96:111], v[80:83], v[22:25], v[96:111]
ds_read_b128 v[18:21], v8 offset:64480
ds_read_b64_tr_b16 v[76:77], v12 offset:63488
ds_read_b64_tr_b16 v[78:79], v12 offset:64000
s_waitcnt lgkmcnt(12)
v_mfma_f32_32x32x16_bf16 v[96:111], v[84:87], v[60:63], v[96:111]
s_waitcnt lgkmcnt(9)
v_mfma_f32_32x32x16_bf16 v[96:111], v[88:91], v[64:67], v[96:111]
s_waitcnt lgkmcnt(6)
v_mfma_f32_32x32x16_bf16 v[96:111], v[92:95], v[68:71], v[96:111]
)ASM"
R"ASM(s_waitcnt lgkmcnt(3)
v_mfma_f32_32x32x16_bf16 v[96:111], v[72:75], v[14:17], v[96:111]
s_waitcnt lgkmcnt(0)
v_mfma_f32_32x32x16_bf16 v[96:111], v[76:79], v[18:21], v[96:111]
s_mov_b32 s80, 0x3eb8aa3b
s_nop 10
v_mul_f32 v96, s80, v96
v_mul_f32 v97, s80, v97
v_mul_f32 v98, s80, v98
v_mul_f32 v99, s80, v99
v_mul_f32 v100, s80, v100
v_mul_f32 v101, s80, v101
v_mul_f32 v102, s80, v102
v_mul_f32 v103, s80, v103
v_mul_f32 v104, s80, v104
v_mul_f32 v105, s80, v105
v_mul_f32 v106, s80, v106
v_mul_f32 v107, s80, v107
v_mul_f32 v108, s80, v108
v_mul_f32 v109, s80, v109
v_mul_f32 v110, s80, v110
v_mul_f32 v111, s80, v111
v_cvt_pk_bf16_f32 v10, v96, v97
v_cvt_pk_bf16_f32 v11, v98, v99
v_cvt_pk_bf16_f32 v12, v100, v101
v_cvt_pk_bf16_f32 v13, v102, v103
v_cvt_pk_bf16_f32 v14, v104, v105
v_cvt_pk_bf16_f32 v15, v106, v107
v_cvt_pk_bf16_f32 v16, v108, v109
v_cvt_pk_bf16_f32 v17, v110, v111
s_lshl_b32 s80, s13, 11
s_add_u32 s80, s80, 145408
)ASM"
R"ASM(v_add_u32 v26, s80, v3
ds_write_b128 v26, v[10:13]
ds_write_b128 v26, v[14:17] offset:1024
v_accvgpr_write_b32 a192, v10
v_accvgpr_write_b32 a193, v11
v_accvgpr_write_b32 a194, v12
v_accvgpr_write_b32 a195, v13
v_accvgpr_write_b32 a196, v14
v_accvgpr_write_b32 a197, v15
v_accvgpr_write_b32 a198, v16
v_accvgpr_write_b32 a199, v17
s_mov_b32 s99, 0
Lgath_19_%=:
s_waitcnt vmcnt(15)
v_xor_b32 v8, s18, v28
v_xor_b32 v10, s18, v30
v_or_b32 v8, v8, v10
v_and_b32 v8, 0xff, v8
v_xor_b32 v10, s19, v29
v_xor_b32 v11, s19, v31
v_or3_b32 v12, v8, v10, v11
s_waitcnt vmcnt(14)
v_xor_b32 v8, s18, v32
v_xor_b32 v10, s18, v34
v_or_b32 v8, v8, v10
v_and_b32 v8, 0xff, v8
v_xor_b32 v10, s19, v33
v_xor_b32 v11, s19, v35
v_or3_b32 v8, v8, v10, v11
v_or_b32 v12, v12, v8
s_waitcnt vmcnt(13)
v_xor_b32 v8, s18, v36
)ASM"
R"ASM(v_xor_b32 v10, s18, v38
v_or_b32 v8, v8, v10
v_and_b32 v8, 0xff, v8
v_xor_b32 v10, s19, v37
v_xor_b32 v11, s19, v39
v_or3_b32 v8, v8, v10, v11
v_or_b32 v12, v12, v8
s_waitcnt vmcnt(12)
v_xor_b32 v8, s18, v40
v_xor_b32 v10, s18, v42
v_or_b32 v8, v8, v10
v_and_b32 v8, 0xff, v8
v_xor_b32 v10, s19, v41
v_xor_b32 v11, s19, v43
v_or3_b32 v8, v8, v10, v11
v_or_b32 v12, v12, v8
s_waitcnt vmcnt(11)
v_xor_b32 v8, s18, v44
v_xor_b32 v10, s18, v46
v_or_b32 v8, v8, v10
v_and_b32 v8, 0xff, v8
v_xor_b32 v10, s19, v45
v_xor_b32 v11, s19, v47
v_or3_b32 v8, v8, v10, v11
v_or_b32 v12, v12, v8
s_waitcnt vmcnt(10)
v_xor_b32 v8, s18, v48
v_xor_b32 v10, s18, v50
v_or_b32 v8, v8, v10
v_and_b32 v8, 0xff, v8
v_xor_b32 v10, s19, v49
v_xor_b32 v11, s19, v51
)ASM"
R"ASM(v_or3_b32 v8, v8, v10, v11
v_or_b32 v12, v12, v8
s_waitcnt vmcnt(9)
v_xor_b32 v8, s18, v52
v_xor_b32 v10, s18, v54
v_or_b32 v8, v8, v10
v_and_b32 v8, 0xff, v8
v_xor_b32 v10, s19, v53
v_xor_b32 v11, s19, v55
v_or3_b32 v8, v8, v10, v11
v_or_b32 v12, v12, v8
s_waitcnt vmcnt(8)
v_xor_b32 v8, s18, v56
v_xor_b32 v10, s18, v58
v_or_b32 v8, v8, v10
v_and_b32 v8, 0xff, v8
v_xor_b32 v10, s19, v57
v_xor_b32 v11, s19, v59
v_or3_b32 v8, v8, v10, v11
v_or_b32 v12, v12, v8
v_cmp_eq_u32 vcc, 0, v12
s_cmp_eq_u64 vcc, -1
s_cbranch_scc1 Lgout_20_%=
s_add_u32 s99, s99, 1
s_cmp_ge_u32 s99, 0x8000
s_cbranch_scc1 Lgout_20_%=
s_mov_b32 s98, 0x1288000
buffer_load_dwordx4 v[28:31], v9, s[20:23], s98 offen sc1
s_mov_b32 s98, 0x128c000
buffer_load_dwordx4 v[32:35], v9, s[20:23], s98 offen sc1
s_mov_b32 s98, 0x1289000
buffer_load_dwordx4 v[36:39], v9, s[20:23], s98 offen sc1
)ASM"
R"ASM(s_mov_b32 s98, 0x128d000
buffer_load_dwordx4 v[40:43], v9, s[20:23], s98 offen sc1
s_mov_b32 s98, 0x128a000
buffer_load_dwordx4 v[44:47], v9, s[20:23], s98 offen sc1
s_mov_b32 s98, 0x128e000
buffer_load_dwordx4 v[48:51], v9, s[20:23], s98 offen sc1
s_mov_b32 s98, 0x128b000
buffer_load_dwordx4 v[52:55], v9, s[20:23], s98 offen sc1
s_mov_b32 s98, 0x128f000
buffer_load_dwordx4 v[56:59], v9, s[20:23], s98 offen sc1
s_waitcnt vmcnt(0)
s_branch Lgath_19_%=
Lgout_20_%=:
v_lshlrev_b32 v8, 3, v0
v_and_b32 v28, 0xffffff00, v28
v_and_b32 v29, 0xffffff00, v30
v_and_b32 v32, 0xffffff00, v32
v_and_b32 v33, 0xffffff00, v34
v_pk_add_f32 v[28:29], v[28:29], v[32:33]
v_add_u32 v10, 137216, v8
ds_write_b64 v10, v[28:29]
v_and_b32 v36, 0xffffff00, v36
v_and_b32 v37, 0xffffff00, v38
v_and_b32 v40, 0xffffff00, v40
v_and_b32 v41, 0xffffff00, v42
v_pk_add_f32 v[36:37], v[36:37], v[40:41]
v_add_u32 v10, 139264, v8
ds_write_b64 v10, v[36:37]
v_and_b32 v44, 0xffffff00, v44
v_and_b32 v45, 0xffffff00, v46
v_and_b32 v48, 0xffffff00, v48
v_and_b32 v49, 0xffffff00, v50
)ASM"
R"ASM(v_pk_add_f32 v[44:45], v[44:45], v[48:49]
v_add_f32 v44, 1.0, v44
v_add_f32 v45, 1.0, v45
v_add_u32 v10, 141312, v8
ds_write_b64 v10, v[44:45]
v_and_b32 v52, 0xffffff00, v52
v_and_b32 v53, 0xffffff00, v54
v_and_b32 v56, 0xffffff00, v56
v_and_b32 v57, 0xffffff00, v58
v_pk_add_f32 v[52:53], v[52:53], v[56:57]
v_add_u32 v10, 143360, v8
ds_write_b64 v10, v[52:53]
s_waitcnt lgkmcnt(0)
s_barrier
s_xor_b32 s80, s13, 2
s_lshl_b32 s80, s80, 11
s_add_u32 s80, s80, 145408
v_add_u32 v26, s80, v3
ds_read_b128 a[200:203], v26
ds_read_b128 a[204:207], v26 offset:1024
v_lshlrev_b32 v13, 7, v0
s_mov_b32 s98, 0x8000
buffer_load_dwordx2 v[14:15], v13, s[20:23], s98 offen sc1
v_lshrrev_b32 v16, 2, v0
v_and_b32 v16, 31, v16
v_lshlrev_b32 v16, 6, v16
v_and_b32 v17, 1, v0
v_lshlrev_b32 v17, 5, v17
v_add_u32 v16, v16, v17
v_and_b32 v17, 2, v0
v_lshlrev_b32 v17, 3, v17
v_add_u32 v16, v16, v17
)ASM"
R"ASM(v_lshrrev_b32 v17, 7, v0
v_mul_u32_u24 v30, 2048, v17
v_sub_u32 v16, v16, v30
v_add_u32 v16, 141312, v16
ds_read_b128 v[18:21], v16
v_lshlrev_b32 v30, 11, v17
v_lshrrev_b32 v16, 2, v0
v_and_b32 v16, 31, v16
v_lshlrev_b32 v16, 5, v16
v_add_u32 v30, v30, v16
v_and_b32 v16, 2, v0
v_lshlrev_b32 v16, 3, v16
v_add_u32 v30, v30, v16
v_and_b32 v16, 1, v0
v_lshlrev_b32 v16, 3, v16
v_add_u32 v30, v30, v16
v_add_u32 v30, 153600, v30
s_waitcnt lgkmcnt(0)
v_cvt_pk_bf16_f32 v22, v18, v19
v_cvt_pk_bf16_f32 v23, v20, v21
v_lshlrev_b32 v26, 16, v22
v_and_b32 v27, 0xffff0000, v22
v_lshlrev_b32 v28, 16, v23
v_and_b32 v29, 0xffff0000, v23
v_sub_f32 v18, v18, v26
v_sub_f32 v19, v19, v27
v_sub_f32 v20, v20, v28
v_sub_f32 v21, v21, v29
v_cvt_pk_bf16_f32 v24, v18, v19
v_cvt_pk_bf16_f32 v25, v20, v21
ds_write_b64 v30, v[22:23]
ds_write_b64 v30, v[24:25] offset:1024
)ASM"
R"ASM(s_mov_b32 s32, s20
s_mov_b32 s33, s21
s_mov_b32 s35, s23
s_cmp_ge_u32 s12, 23
s_cselect_b32 s34, 0, s22
s_lshl_b32 s59, s14, 1
s_add_u32 s59, s59, s100
s_lshl_b32 s47, s59, 4
s_lshl_b32 s80, s15, 1
s_add_u32 s47, s47, s80
s_add_u32 s47, s47, s101
s_lshl_b32 s47, s47, 11
s_lshl_b32 s54, s59, 15
s_lshl_b32 s80, s101, 14
s_add_u32 s54, s54, s80
s_add_u32 s54, s54, 0x100000
s_lshl_b32 s72, s16, 2
s_add_u32 s72, s72, s13
s_lshl_b32 s55, s72, 15
s_add_u32 s55, s55, 0x300000
s_lshl_b32 s56, s72, 12
s_add_u32 s56, s56, 0x60000
s_mul_i32 s73, s72, 114688
s_lshl_b32 s80, s17, 10
s_add_u32 s73, s73, s80
s_add_u32 s73, s73, 0x500000
s_lshl_b32 s80, s14, 1
s_add_u32 s80, s80, s100
s_mul_i32 s48, s80, 114688
s_lshl_b32 s80, s13, 12
s_add_u32 s48, s48, s80
s_add_u32 s48, s48, 0x500000
)ASM"
R"ASM(s_lshl_b32 s80, s14, 1
s_add_u32 s80, s80, 1
s_sub_u32 s80, s80, s100
s_mul_i32 s49, s80, 114688
s_lshl_b32 s80, s13, 12
s_add_u32 s49, s49, s80
s_add_u32 s49, s49, 0x500000
s_lshl_b32 s57, s13, 13
s_mul_i32 s58, s17, 229376
s_add_u32 s58, s58, s57
s_add_u32 s58, s58, 0xc00000
s_mul_i32 s74, s15, 229376
s_add_u32 s74, s74, s57
s_add_u32 s74, s74, 0x1000000
s_lshl_b32 s80, s101, 10
s_add_u32 s75, s74, 1024
s_sub_u32 s75, s75, s80
s_add_u32 s74, s74, s80
s_mov_b32 s46, 0
s_lshl_b32 s80, s15, 1
s_add_u32 s80, s80, 168
v_mov_b32 v16, s80
v_add_u32 v17, 1, v16
v_cmp_eq_u32 vcc, 25, v1
s_nop 1
v_cndmask_b32 v16, v16, v17, vcc
s_mul_i32 s80, s17, 7
s_andn2_b32 s80, s80, 3
v_add_u32 v17, s80, v1
v_min_u32 v17, 111, v17
v_cmp_gt_u32 vcc, 12, v1
s_nop 1
)ASM"
R"ASM(v_cndmask_b32 v16, v16, v17, vcc
s_mul_i32 s80, s15, 7
s_add_u32 s80, s80, 96
v_add_u32 v17, s80, v1
v_subrev_u32 v20, 16, v1
v_cmp_gt_u32 vcc, 7, v20
s_nop 1
v_cndmask_b32 v16, v16, v17, vcc
s_lshl_b32 s80, s16, 1
s_add_u32 s80, s80, s101
s_lshr_b32 s81, s80, 2
s_and_b32 s80, s80, 3
s_lshl_b32 s80, s80, 6
s_add_u32 s80, s80, s81
s_add_u32 s80, s80, 256
s_lshr_b32 s81, s12, 3
s_lshl_b32 s81, s81, 6
s_add_u32 s81, s81, s11
s_lshl_b32 s82, s101, 5
s_add_u32 s81, s81, s82
s_add_u32 s81, s81, 256
s_lshr_b32 s82, s14, 1
s_lshr_b32 s83, s82, 2
s_lshl_b32 s83, s83, 1
s_add_u32 s83, s83, s101
s_and_b32 s82, s82, 3
s_lshl_b32 s82, s82, 6
s_add_u32 s82, s82, s83
s_lshl_b32 s83, s100, 5
s_add_u32 s82, s82, s83
s_add_u32 s82, s82, 256
v_mov_b32 v17, s81
)ASM"
R"ASM(v_mov_b32 v20, s82
v_cmp_gt_u32 vcc, 44, v1
s_nop 1
v_cndmask_b32 v17, v20, v17, vcc
v_and_b32 v20, 3, v1
v_lshl_add_u32 v17, v20, 3, v17
v_and_b32 v20, 7, v1
v_lshlrev_b32 v20, 3, v20
v_add_u32 v20, s80, v20
v_cmp_gt_u32 vcc, 40, v1
s_nop 1
v_cndmask_b32 v17, v17, v20, vcc
v_cmp_gt_u32 vcc, 32, v1
s_nop 1
v_cndmask_b32 v16, v17, v16, vcc
v_lshlrev_b32 v16, 7, v16
v_mov_b32 v20, -1
v_mov_b32 v21, -1
v_mov_b32 v22, -1
v_mov_b32 v23, -1
v_lshlrev_b32 v17, 2, v1
v_mov_b32 v24, s18
v_mov_b32 v25, s19
v_mov_b32 v26, 0
s_lshl_b32 s80, s101, 15
v_add_u32 v27, s80, v3
v_lshlrev_b32 v28, 8, v2
v_lshlrev_b32 v30, 4, v4
v_add_u32 v28, v28, v30
v_add_u32 v29, 512, v28
v_and_b32 v30, 27, v4
v_cmp_eq_u32 vcc, 16, v30
)ASM"
R"ASM(v_mov_b32 v30, 1040
v_mov_b32 v31, 1024
v_cndmask_b32 v30, v30, v31, vcc
v_cmp_gt_u32 vcc, 16, v4
s_nop 1
v_cndmask_b32 v28, v30, v28, vcc
v_cndmask_b32 v29, v30, v29, vcc
s_mul_i32 s80, s101, 33792
s_add_u32 s80, s80, 65536
v_add_u32 v28, s80, v28
v_add_u32 v29, s80, v29
v_lshrrev_b32 v12, 1, v0
v_mul_u32_u24 v12, 1056, v12
v_and_b32 v8, 1, v0
v_cmp_eq_u32 vcc, 0, v8
v_lshlrev_b32 v8, 4, v8
v_add_u32 v12, v12, v8
v_add_u32 v12, 66560, v12
v_mov_b32 v9, 0x3f803f80
v_mov_b32 v10, 0
v_cndmask_b32 v8, v10, v9, vcc
v_mov_b32 v9, v8
v_mov_b32 v10, v8
v_mov_b32 v11, v8
s_cmp_lt_u32 s12, 14
s_cselect_b32 s80, 1, 2
s_cselect_b32 s81, 1, 3
s_cselect_b32 s82, 32, 2112
s_mul_i32 s83, s100, 8448
s_lshl_b32 s87, s101, 15
s_add_u32 s83, s83, s87
s_add_u32 s83, s83, 8192
)ASM"
R"ASM(v_and_b32 v34, 7, v1
v_lshrrev_b32 v33, s80, v34
v_mul_u32_u24 v30, 1056, v33
v_and_b32 v34, s81, v34
v_lshl_add_u32 v30, v34, 4, v30
v_lshrrev_b32 v33, 3, v1
v_lshl_add_u32 v30, v33, 6, v30
v_add_u32 v30, s83, v30
v_add_u32 v31, s82, v30
v_bfe_u32 v33, v1, 2, 2
v_lshl_add_u32 v32, v2, 2, v33
v_lshlrev_b32 v32, 6, v32
v_and_b32 v33, 3, v1
v_lshl_add_u32 v32, v33, 3, v32
v_bfe_u32 v33, v1, 4, 1
v_lshl_add_u32 v32, v33, 5, v32
v_add_u32 v32, s83, v32
v_and_b32 v33, 3, v1
v_lshlrev_b32 v33, 10, v33
v_lshl_add_u32 v33, v2, 4, v33
s_and_b32 s80, s96, 3
s_lshl_b32 s80, s80, 2
s_add_u32 s80, s80, s13
s_lshl_b32 s80, s80, 6
s_add_u32 s80, s80, 153600
v_add_u32 v33, s80, v33
s_lshl_b32 s80, s13, 10
s_add_u32 s80, s80, 157696
v_lshlrev_b32 v34, 3, v4
v_add_u32 v34, s80, v34
s_lshl_b32 s80, s101, 3
s_add_u32 s80, s80, s100
)ASM"
R"ASM(s_lshl_b32 s84, s80, 12
s_mul_i32 s85, s80, 4224
s_waitcnt vmcnt(0)
v_cmp_eq_u64 vcc, v[14:15], s[18:19]
s_cmp_eq_u64 vcc, -1
s_cbranch_scc1 Lkvwok_21_%=
s_mov_b32 s99, 0
s_mov_b32 s98, 0x8000
Lkvwsp_22_%=:
buffer_load_dwordx2 v[14:15], v13, s[20:23], s98 offen sc1
s_waitcnt vmcnt(0)
v_cmp_eq_u64 vcc, v[14:15], s[18:19]
s_cmp_eq_u64 vcc, -1
s_cbranch_scc1 Lkvwok_21_%=
s_add_u32 s99, s99, 1
s_cmp_lt_u32 s99, 0x8000
s_cbranch_scc1 Lkvwsp_22_%=
Lkvwok_21_%=:
s_waitcnt lgkmcnt(0)
s_barrier
s_add_u32 m0, s84, 0
s_add_u32 s86, s84, 0x40000
buffer_load_dwordx4 v3, s[20:23], s86 offen offset:0 sc1 lds
buffer_load_dwordx4 v3, s[20:23], s86 offen offset:1024 sc1 lds
buffer_load_dwordx4 v3, s[20:23], s86 offen offset:2048 sc1 lds
buffer_load_dwordx4 v3, s[20:23], s86 offen offset:3072 sc1 lds
s_add_u32 m0, s85, 65536
s_add_u32 s86, s85, 0x20000
buffer_load_dwordx4 v3, s[20:23], s86 offen offset:0 sc1 lds
buffer_load_dwordx4 v3, s[20:23], s86 offen offset:1056 sc1 lds
buffer_load_dwordx4 v3, s[20:23], s86 offen offset:2112 sc1 lds
buffer_load_dwordx4 v3, s[20:23], s86 offen offset:3168 sc1 lds
)ASM"
R"ASM(s_or_b32 s80, s10, s13
s_cmp_lg_u32 s80, 0
s_cbranch_scc1 Lnoe_23_%=
v_mov_b32 v14, s18
v_mov_b32 v15, s19
v_mov_b32 v13, 0
s_mov_b64 exec, 1
buffer_store_dwordx2 v[14:15], v13, s[20:23], 0 offen sc1
s_mov_b64 exec, -1
Lnoe_23_%=:
s_cmp_ge_u32 s13, 2
s_cbranch_scc1 Lnoones_24_%=
ds_write_b128 v12, v[8:11]
Lnoones_24_%=:
ds_write_b128 v30, a[0:3] offset:0
ds_write_b128 v30, a[4:7] offset:512
ds_write_b128 v31, a[8:11] offset:0
ds_write_b128 v31, a[12:15] offset:512
ds_write_b128 v30, a[16:19] offset:4224
ds_write_b128 v30, a[20:23] offset:4736
ds_write_b128 v31, a[24:27] offset:4224
ds_write_b128 v31, a[28:31] offset:4736
ds_read_b64_tr_b16 v[36:37], v32 offset:0
ds_read_b64_tr_b16 v[38:39], v32 offset:512
ds_read_b64_tr_b16 v[40:41], v32 offset:1056
ds_read_b64_tr_b16 v[42:43], v32 offset:1568
ds_read_b64_tr_b16 v[44:45], v32 offset:2112
ds_read_b64_tr_b16 v[46:47], v32 offset:2624
ds_read_b64_tr_b16 v[48:49], v32 offset:3168
ds_read_b64_tr_b16 v[50:51], v32 offset:3680
ds_read_b64_tr_b16 v[52:53], v32 offset:4224
ds_read_b64_tr_b16 v[54:55], v32 offset:4736
)ASM"
R"ASM(ds_read_b64_tr_b16 v[56:57], v32 offset:5280
ds_read_b64_tr_b16 v[58:59], v32 offset:5792
ds_read_b64_tr_b16 v[60:61], v32 offset:6336
ds_read_b64_tr_b16 v[62:63], v32 offset:6848
ds_read_b64_tr_b16 v[64:65], v32 offset:7392
ds_read_b64_tr_b16 v[66:67], v32 offset:7904
ds_read_b128 v[8:11], v33 offset:0
ds_read_b128 v[12:15], v33 offset:32
s_cmp_lt_u32 s12, 14
s_cselect_b32 s81, 64, 2
s_cselect_b32 s82, 1, 4
s_cselect_b32 s80, 32, 1
s_cmp_ge_u32 s12, 21
s_cselect_b32 s80, 32, s80
s_cselect_b32 s81, 1, s81
s_cselect_b32 s82, 2, s82
s_lshl_b32 s80, s80, 10
s_lshl_b32 s81, s81, 10
s_lshl_b32 s82, s82, 10
s_mov_b32 s83, s95
s_waitcnt lgkmcnt(14)
buffer_store_dwordx4 v[36:39], v3, s[32:35], s83 offen sc1
s_add_u32 s83, s83, s80
buffer_store_dwordx4 v[40:43], v3, s[32:35], s83 offen sc1
s_add_u32 s83, s83, s81
s_waitcnt lgkmcnt(10)
buffer_store_dwordx4 v[48:51], v3, s[32:35], s83 offen sc1
s_sub_u32 s83, s83, s80
buffer_store_dwordx4 v[44:47], v3, s[32:35], s83 offen sc1
s_add_u32 s83, s83, s82
s_waitcnt lgkmcnt(4)
buffer_store_dwordx4 v[60:63], v3, s[32:35], s83 offen sc1
)ASM"
R"ASM(s_add_u32 s83, s83, s80
s_waitcnt lgkmcnt(2)
buffer_store_dwordx4 v[64:67], v3, s[32:35], s83 offen sc1
s_sub_u32 s83, s83, s81
buffer_store_dwordx4 v[56:59], v3, s[32:35], s83 offen sc1
s_sub_u32 s83, s83, s80
buffer_store_dwordx4 v[52:55], v3, s[32:35], s83 offen sc1
s_waitcnt lgkmcnt(1)
v_mfma_f32_32x32x16_bf16 v[172:187], v[8:11], v[36:39], 0
v_mfma_f32_32x32x16_bf16 v[188:203], v[8:11], v[40:43], 0
v_mfma_f32_32x32x16_bf16 v[204:219], v[8:11], v[44:47], 0
v_mfma_f32_32x32x16_bf16 v[220:235], v[8:11], v[48:51], 0
s_waitcnt lgkmcnt(0)
v_mfma_f32_32x32x16_bf16 v[172:187], v[12:15], v[52:55], v[172:187]
v_mfma_f32_32x32x16_bf16 v[188:203], v[12:15], v[56:59], v[188:203]
v_mfma_f32_32x32x16_bf16 v[204:219], v[12:15], v[60:63], v[204:219]
v_mfma_f32_32x32x16_bf16 v[220:235], v[12:15], v[64:67], v[220:235]
s_nop 8
v_add_f32 v172, v172, v173
v_add_f32 v173, v174, v175
ds_write_b64 v34, v[172:173] offset:0
v_add_f32 v188, v188, v189
v_add_f32 v189, v190, v191
ds_write_b64 v34, v[188:189] offset:256
v_add_f32 v204, v204, v205
v_add_f32 v205, v206, v207
ds_write_b64 v34, v[204:205] offset:512
v_add_f32 v220, v220, v221
v_add_f32 v221, v222, v223
ds_write_b64 v34, v[220:221] offset:768
s_waitcnt vmcnt(8) lgkmcnt(0)
s_barrier
)ASM"
R"ASM(s_cmp_ge_u32 s12, 14
s_cbranch_scc1 Lnred_25_%=
s_lshl_b32 s80, s13, 8
s_add_u32 s80, s80, 157696
v_lshlrev_b32 v32, 3, v4
v_add_u32 v8, s80, v32
ds_read_b64 v[14:15], v8 offset:3072
ds_read_b64 v[12:13], v8 offset:2048
ds_read_b64 v[10:11], v8 offset:1024
ds_read_b64 v[8:9], v8 offset:0
s_and_b32 s80, s96, 3
s_lshl_b32 s80, s80, 8
s_andn2_b32 s81, s96, 3
s_add_u32 s81, s81, s13
s_lshl_b32 s81, s81, 10
s_add_u32 s80, s80, s81
s_add_u32 s80, s80, 0x12c0000
s_waitcnt lgkmcnt(2)
v_add_f32 v12, v12, v14
v_add_f32 v13, v13, v15
s_waitcnt lgkmcnt(0)
v_add_f32 v8, v8, v10
v_add_f32 v9, v9, v11
v_add_f32 v8, v8, v12
v_add_f32 v9, v9, v13
buffer_store_dwordx2 v[8:9], v32, s[20:23], s80 offen sc1
Lnred_25_%=:
ds_read_b128 v[184:187], v29 offset:0
ds_read_b128 v[176:179], v28 offset:0
ds_read_b128 v[64:67], v27 offset:0
s_waitcnt lgkmcnt(0)
v_mfma_f32_32x32x16_bf16 v[32:47], v[64:67], a[192:195], 0
)ASM"
R"ASM(s_nop 11
v_exp_f32 v32, v32
v_nop
v_exp_f32 v33, v33
v_exp_f32 v34, v34
v_exp_f32 v35, v35
ds_read_b128 v[188:191], v29 offset:1056
v_cvt_pk_bf16_f32 v8, v32, v33
s_add_u32 m0, s84, 8192
s_add_u32 s86, s84, 0x42000
buffer_load_dwordx4 v3, s[20:23], s86 offen offset:0 sc1 lds
v_exp_f32 v36, v36
v_exp_f32 v37, v37
ds_read_b128 v[180:183], v28 offset:1056
v_cvt_pk_bf16_f32 v9, v34, v35
v_exp_f32 v38, v38
v_mfma_f32_32x32x16_bf16 v[48:63], v[64:67], a[196:199], 0
v_exp_f32 v39, v39
v_cvt_pk_bf16_f32 v10, v36, v37
ds_read_b128 v[172:175], v27 offset:1024
v_exp_f32 v40, v40
buffer_load_dwordx4 v3, s[20:23], s86 offen offset:1024 sc1 lds
v_exp_f32 v41, v41
v_cvt_pk_bf16_f32 v11, v38, v39
v_exp_f32 v42, v42
v_exp_f32 v43, v43
v_mfma_f32_32x32x16_bf16 a[128:143], v[176:179], v[8:11], 0
v_cvt_pk_bf16_f32 v12, v40, v41
v_exp_f32 v44, v44
v_exp_f32 v45, v45
v_cvt_pk_bf16_f32 v13, v42, v43
v_exp_f32 v46, v46
)ASM"
R"ASM(v_exp_f32 v47, v47
v_cvt_pk_bf16_f32 v14, v44, v45
v_exp_f32 v48, v48
v_cvt_pk_bf16_f32 v15, v46, v47
v_exp_f32 v49, v49
v_exp_f32 v50, v50
v_mfma_f32_32x32x16_bf16 a[128:143], v[184:187], v[12:15], a[128:143]
v_exp_f32 v51, v51
v_cvt_pk_bf16_f32 v8, v48, v49
buffer_load_dwordx4 v3, s[20:23], s86 offen offset:2048 sc1 lds
v_exp_f32 v52, v52
v_exp_f32 v53, v53
v_cvt_pk_bf16_f32 v9, v50, v51
v_exp_f32 v54, v54
v_mfma_f32_32x32x16_bf16 v[32:47], v[64:67], a[200:203], 0
v_exp_f32 v55, v55
v_cvt_pk_bf16_f32 v10, v52, v53
v_exp_f32 v56, v56
buffer_load_dwordx4 v3, s[20:23], s86 offen offset:3072 sc1 lds
v_exp_f32 v57, v57
v_cvt_pk_bf16_f32 v11, v54, v55
v_exp_f32 v58, v58
v_exp_f32 v59, v59
v_mfma_f32_32x32x16_bf16 a[144:159], v[176:179], v[8:11], 0
v_cvt_pk_bf16_f32 v12, v56, v57
v_exp_f32 v60, v60
v_exp_f32 v61, v61
v_cvt_pk_bf16_f32 v13, v58, v59
v_exp_f32 v62, v62
v_exp_f32 v63, v63
v_cvt_pk_bf16_f32 v14, v60, v61
v_exp_f32 v32, v32
)ASM"
R"ASM(v_cvt_pk_bf16_f32 v15, v62, v63
v_exp_f32 v33, v33
v_exp_f32 v34, v34
v_mfma_f32_32x32x16_bf16 a[144:159], v[184:187], v[12:15], a[144:159]
v_exp_f32 v35, v35
v_cvt_pk_bf16_f32 v8, v32, v33
s_add_u32 m0, s85, 73984
s_add_u32 s86, s85, 0x22100
buffer_load_dwordx4 v3, s[20:23], s86 offen offset:0 sc1 lds
v_exp_f32 v36, v36
v_exp_f32 v37, v37
v_cvt_pk_bf16_f32 v9, v34, v35
v_exp_f32 v38, v38
v_mfma_f32_32x32x16_bf16 v[48:63], v[64:67], a[204:207], 0
v_exp_f32 v39, v39
v_cvt_pk_bf16_f32 v10, v36, v37
v_exp_f32 v40, v40
buffer_load_dwordx4 v3, s[20:23], s86 offen offset:1056 sc1 lds
v_exp_f32 v41, v41
v_cvt_pk_bf16_f32 v11, v38, v39
v_exp_f32 v42, v42
v_exp_f32 v43, v43
v_mfma_f32_32x32x16_bf16 a[160:175], v[176:179], v[8:11], 0
v_cvt_pk_bf16_f32 v12, v40, v41
v_exp_f32 v44, v44
v_exp_f32 v45, v45
v_cvt_pk_bf16_f32 v13, v42, v43
v_exp_f32 v46, v46
v_exp_f32 v47, v47
v_cvt_pk_bf16_f32 v14, v44, v45
v_exp_f32 v48, v48
v_cvt_pk_bf16_f32 v15, v46, v47
)ASM"
R"ASM(v_exp_f32 v49, v49
v_exp_f32 v50, v50
v_mfma_f32_32x32x16_bf16 a[160:175], v[184:187], v[12:15], a[160:175]
v_exp_f32 v51, v51
v_cvt_pk_bf16_f32 v8, v48, v49
buffer_load_dwordx4 v3, s[20:23], s86 offen offset:2112 sc1 lds
v_exp_f32 v52, v52
v_exp_f32 v53, v53
v_cvt_pk_bf16_f32 v9, v50, v51
v_exp_f32 v54, v54
s_waitcnt lgkmcnt(0)
v_mfma_f32_32x32x16_bf16 v[32:47], v[172:175], a[192:195], 0
v_exp_f32 v55, v55
v_cvt_pk_bf16_f32 v10, v52, v53
v_exp_f32 v56, v56
buffer_load_dwordx4 v3, s[20:23], s86 offen offset:3168 sc1 lds
v_exp_f32 v57, v57
v_cvt_pk_bf16_f32 v11, v54, v55
v_exp_f32 v58, v58
v_exp_f32 v59, v59
v_mfma_f32_32x32x16_bf16 a[176:191], v[176:179], v[8:11], 0
v_cvt_pk_bf16_f32 v12, v56, v57
v_exp_f32 v60, v60
v_exp_f32 v61, v61
v_cvt_pk_bf16_f32 v13, v58, v59
v_exp_f32 v62, v62
v_exp_f32 v63, v63
v_cvt_pk_bf16_f32 v14, v60, v61
v_exp_f32 v32, v32
v_cvt_pk_bf16_f32 v15, v62, v63
v_exp_f32 v33, v33
v_exp_f32 v34, v34
)ASM"
R"ASM(v_mfma_f32_32x32x16_bf16 a[176:191], v[184:187], v[12:15], a[176:191]
v_exp_f32 v35, v35
ds_read_b128 v[184:187], v29 offset:2112
v_cvt_pk_bf16_f32 v8, v32, v33
s_add_u32 m0, s84, 16384
s_add_u32 s86, s84, 0x44000
buffer_load_dwordx4 v3, s[20:23], s86 offen offset:0 sc1 lds
v_exp_f32 v36, v36
v_exp_f32 v37, v37
ds_read_b128 v[176:179], v28 offset:2112
v_cvt_pk_bf16_f32 v9, v34, v35
v_exp_f32 v38, v38
v_mfma_f32_32x32x16_bf16 v[48:63], v[172:175], a[196:199], 0
v_exp_f32 v39, v39
v_cvt_pk_bf16_f32 v10, v36, v37
ds_read_b128 v[64:67], v27 offset:2048
v_exp_f32 v40, v40
buffer_load_dwordx4 v3, s[20:23], s86 offen offset:1024 sc1 lds
v_exp_f32 v41, v41
v_cvt_pk_bf16_f32 v11, v38, v39
v_exp_f32 v42, v42
v_exp_f32 v43, v43
v_mfma_f32_32x32x16_bf16 a[128:143], v[180:183], v[8:11], a[128:143]
v_cvt_pk_bf16_f32 v12, v40, v41
v_exp_f32 v44, v44
v_exp_f32 v45, v45
v_cvt_pk_bf16_f32 v13, v42, v43
v_exp_f32 v46, v46
v_exp_f32 v47, v47
v_cvt_pk_bf16_f32 v14, v44, v45
v_exp_f32 v48, v48
v_cvt_pk_bf16_f32 v15, v46, v47
)ASM"
R"ASM(v_exp_f32 v49, v49
v_exp_f32 v50, v50
v_mfma_f32_32x32x16_bf16 a[128:143], v[188:191], v[12:15], a[128:143]
v_exp_f32 v51, v51
v_cvt_pk_bf16_f32 v8, v48, v49
buffer_load_dwordx4 v3, s[20:23], s86 offen offset:2048 sc1 lds
v_exp_f32 v52, v52
v_exp_f32 v53, v53
v_cvt_pk_bf16_f32 v9, v50, v51
v_exp_f32 v54, v54
v_mfma_f32_32x32x16_bf16 v[32:47], v[172:175], a[200:203], 0
v_exp_f32 v55, v55
v_cvt_pk_bf16_f32 v10, v52, v53
v_exp_f32 v56, v56
buffer_load_dwordx4 v3, s[20:23], s86 offen offset:3072 sc1 lds
v_exp_f32 v57, v57
v_cvt_pk_bf16_f32 v11, v54, v55
v_exp_f32 v58, v58
v_exp_f32 v59, v59
v_mfma_f32_32x32x16_bf16 a[144:159], v[180:183], v[8:11], a[144:159]
v_cvt_pk_bf16_f32 v12, v56, v57
v_exp_f32 v60, v60
v_exp_f32 v61, v61
v_cvt_pk_bf16_f32 v13, v58, v59
v_exp_f32 v62, v62
v_exp_f32 v63, v63
v_cvt_pk_bf16_f32 v14, v60, v61
v_exp_f32 v32, v32
v_cvt_pk_bf16_f32 v15, v62, v63
v_exp_f32 v33, v33
v_exp_f32 v34, v34
v_mfma_f32_32x32x16_bf16 a[144:159], v[188:191], v[12:15], a[144:159]
)ASM"
R"ASM(v_exp_f32 v35, v35
v_cvt_pk_bf16_f32 v8, v32, v33
s_add_u32 m0, s85, 82432
s_add_u32 s86, s85, 0x24200
buffer_load_dwordx4 v3, s[20:23], s86 offen offset:0 sc1 lds
v_exp_f32 v36, v36
v_exp_f32 v37, v37
v_cvt_pk_bf16_f32 v9, v34, v35
v_exp_f32 v38, v38
v_mfma_f32_32x32x16_bf16 v[48:63], v[172:175], a[204:207], 0
v_exp_f32 v39, v39
v_cvt_pk_bf16_f32 v10, v36, v37
v_exp_f32 v40, v40
buffer_load_dwordx4 v3, s[20:23], s86 offen offset:1056 sc1 lds
v_exp_f32 v41, v41
v_cvt_pk_bf16_f32 v11, v38, v39
v_exp_f32 v42, v42
v_exp_f32 v43, v43
v_mfma_f32_32x32x16_bf16 a[160:175], v[180:183], v[8:11], a[160:175]
v_cvt_pk_bf16_f32 v12, v40, v41
v_exp_f32 v44, v44
v_exp_f32 v45, v45
v_cvt_pk_bf16_f32 v13, v42, v43
v_exp_f32 v46, v46
v_exp_f32 v47, v47
v_cvt_pk_bf16_f32 v14, v44, v45
v_exp_f32 v48, v48
v_cvt_pk_bf16_f32 v15, v46, v47
v_exp_f32 v49, v49
v_exp_f32 v50, v50
v_mfma_f32_32x32x16_bf16 a[160:175], v[188:191], v[12:15], a[160:175]
v_exp_f32 v51, v51
)ASM"
R"ASM(v_cvt_pk_bf16_f32 v8, v48, v49
buffer_load_dwordx4 v3, s[20:23], s86 offen offset:2112 sc1 lds
v_exp_f32 v52, v52
v_exp_f32 v53, v53
v_cvt_pk_bf16_f32 v9, v50, v51
v_exp_f32 v54, v54
s_waitcnt lgkmcnt(0)
v_mfma_f32_32x32x16_bf16 v[32:47], v[64:67], a[192:195], 0
v_exp_f32 v55, v55
v_cvt_pk_bf16_f32 v10, v52, v53
v_exp_f32 v56, v56
buffer_load_dwordx4 v3, s[20:23], s86 offen offset:3168 sc1 lds
v_exp_f32 v57, v57
v_cvt_pk_bf16_f32 v11, v54, v55
v_exp_f32 v58, v58
v_exp_f32 v59, v59
v_mfma_f32_32x32x16_bf16 a[176:191], v[180:183], v[8:11], a[176:191]
v_cvt_pk_bf16_f32 v12, v56, v57
v_exp_f32 v60, v60
v_exp_f32 v61, v61
v_cvt_pk_bf16_f32 v13, v58, v59
v_exp_f32 v62, v62
v_exp_f32 v63, v63
v_cvt_pk_bf16_f32 v14, v60, v61
v_exp_f32 v32, v32
v_cvt_pk_bf16_f32 v15, v62, v63
v_exp_f32 v33, v33
v_exp_f32 v34, v34
v_mfma_f32_32x32x16_bf16 a[176:191], v[188:191], v[12:15], a[176:191]
v_exp_f32 v35, v35
ds_read_b128 v[188:191], v29 offset:3168
v_cvt_pk_bf16_f32 v8, v32, v33
)ASM"
R"ASM(s_add_u32 m0, s84, 24576
s_add_u32 s86, s84, 0x46000
buffer_load_dwordx4 v3, s[20:23], s86 offen offset:0 sc1 lds
v_exp_f32 v36, v36
v_exp_f32 v37, v37
ds_read_b128 v[180:183], v28 offset:3168
v_cvt_pk_bf16_f32 v9, v34, v35
v_exp_f32 v38, v38
v_mfma_f32_32x32x16_bf16 v[48:63], v[64:67], a[196:199], 0
v_exp_f32 v39, v39
v_cvt_pk_bf16_f32 v10, v36, v37
ds_read_b128 v[172:175], v27 offset:3072
v_exp_f32 v40, v40
buffer_load_dwordx4 v3, s[20:23], s86 offen offset:1024 sc1 lds
v_exp_f32 v41, v41
v_cvt_pk_bf16_f32 v11, v38, v39
v_exp_f32 v42, v42
v_exp_f32 v43, v43
v_mfma_f32_32x32x16_bf16 a[128:143], v[176:179], v[8:11], a[128:143]
v_cvt_pk_bf16_f32 v12, v40, v41
v_exp_f32 v44, v44
v_exp_f32 v45, v45
v_cvt_pk_bf16_f32 v13, v42, v43
v_exp_f32 v46, v46
v_exp_f32 v47, v47
v_cvt_pk_bf16_f32 v14, v44, v45
v_exp_f32 v48, v48
v_cvt_pk_bf16_f32 v15, v46, v47
v_exp_f32 v49, v49
v_exp_f32 v50, v50
v_mfma_f32_32x32x16_bf16 a[128:143], v[184:187], v[12:15], a[128:143]
v_exp_f32 v51, v51
)ASM"
R"ASM(v_cvt_pk_bf16_f32 v8, v48, v49
buffer_load_dwordx4 v3, s[20:23], s86 offen offset:2048 sc1 lds
v_exp_f32 v52, v52
v_exp_f32 v53, v53
v_cvt_pk_bf16_f32 v9, v50, v51
v_exp_f32 v54, v54
v_mfma_f32_32x32x16_bf16 v[32:47], v[64:67], a[200:203], 0
v_exp_f32 v55, v55
v_cvt_pk_bf16_f32 v10, v52, v53
v_exp_f32 v56, v56
buffer_load_dwordx4 v3, s[20:23], s86 offen offset:3072 sc1 lds
v_exp_f32 v57, v57
v_cvt_pk_bf16_f32 v11, v54, v55
v_exp_f32 v58, v58
v_exp_f32 v59, v59
v_mfma_f32_32x32x16_bf16 a[144:159], v[176:179], v[8:11], a[144:159]
v_cvt_pk_bf16_f32 v12, v56, v57
v_exp_f32 v60, v60
v_exp_f32 v61, v61
v_cvt_pk_bf16_f32 v13, v58, v59
v_exp_f32 v62, v62
v_exp_f32 v63, v63
v_cvt_pk_bf16_f32 v14, v60, v61
v_exp_f32 v32, v32
v_cvt_pk_bf16_f32 v15, v62, v63
v_exp_f32 v33, v33
v_exp_f32 v34, v34
v_mfma_f32_32x32x16_bf16 a[144:159], v[184:187], v[12:15], a[144:159]
v_exp_f32 v35, v35
v_cvt_pk_bf16_f32 v8, v32, v33
s_add_u32 m0, s85, 90880
s_add_u32 s86, s85, 0x26300
)ASM"
R"ASM(buffer_load_dwordx4 v3, s[20:23], s86 offen offset:0 sc1 lds
v_exp_f32 v36, v36
v_exp_f32 v37, v37
v_cvt_pk_bf16_f32 v9, v34, v35
v_exp_f32 v38, v38
v_mfma_f32_32x32x16_bf16 v[48:63], v[64:67], a[204:207], 0
v_exp_f32 v39, v39
v_cvt_pk_bf16_f32 v10, v36, v37
v_exp_f32 v40, v40
buffer_load_dwordx4 v3, s[20:23], s86 offen offset:1056 sc1 lds
v_exp_f32 v41, v41
v_cvt_pk_bf16_f32 v11, v38, v39
v_exp_f32 v42, v42
v_exp_f32 v43, v43
v_mfma_f32_32x32x16_bf16 a[160:175], v[176:179], v[8:11], a[160:175]
v_cvt_pk_bf16_f32 v12, v40, v41
v_exp_f32 v44, v44
v_exp_f32 v45, v45
v_cvt_pk_bf16_f32 v13, v42, v43
v_exp_f32 v46, v46
v_exp_f32 v47, v47
v_cvt_pk_bf16_f32 v14, v44, v45
v_exp_f32 v48, v48
v_cvt_pk_bf16_f32 v15, v46, v47
v_exp_f32 v49, v49
v_exp_f32 v50, v50
v_mfma_f32_32x32x16_bf16 a[160:175], v[184:187], v[12:15], a[160:175]
v_exp_f32 v51, v51
v_cvt_pk_bf16_f32 v8, v48, v49
buffer_load_dwordx4 v3, s[20:23], s86 offen offset:2112 sc1 lds
v_exp_f32 v52, v52
v_exp_f32 v53, v53
)ASM"
R"ASM(v_cvt_pk_bf16_f32 v9, v50, v51
v_exp_f32 v54, v54
s_waitcnt lgkmcnt(0)
v_mfma_f32_32x32x16_bf16 v[32:47], v[172:175], a[192:195], 0
v_exp_f32 v55, v55
v_cvt_pk_bf16_f32 v10, v52, v53
v_exp_f32 v56, v56
buffer_load_dwordx4 v3, s[20:23], s86 offen offset:3168 sc1 lds
v_exp_f32 v57, v57
v_cvt_pk_bf16_f32 v11, v54, v55
v_exp_f32 v58, v58
v_exp_f32 v59, v59
v_mfma_f32_32x32x16_bf16 a[176:191], v[176:179], v[8:11], a[176:191]
v_cvt_pk_bf16_f32 v12, v56, v57
v_exp_f32 v60, v60
v_exp_f32 v61, v61
v_cvt_pk_bf16_f32 v13, v58, v59
v_exp_f32 v62, v62
v_exp_f32 v63, v63
v_cvt_pk_bf16_f32 v14, v60, v61
v_exp_f32 v32, v32
v_cvt_pk_bf16_f32 v15, v62, v63
v_exp_f32 v33, v33
v_exp_f32 v34, v34
v_mfma_f32_32x32x16_bf16 a[176:191], v[184:187], v[12:15], a[176:191]
v_exp_f32 v35, v35
ds_read_b128 v[184:187], v29 offset:4224
v_cvt_pk_bf16_f32 v8, v32, v33
s_add_u32 s87, s47, 0x100000
buffer_store_dwordx4 v[20:23], v3, s[20:23], s87 offen sc1
v_exp_f32 v36, v36
v_exp_f32 v37, v37
)ASM"
R"ASM(ds_read_b128 v[176:179], v28 offset:4224
v_cvt_pk_bf16_f32 v9, v34, v35
v_exp_f32 v38, v38
v_mfma_f32_32x32x16_bf16 v[48:63], v[172:175], a[196:199], 0
v_exp_f32 v39, v39
v_cvt_pk_bf16_f32 v10, v36, v37
ds_read_b128 v[64:67], v27 offset:4096
v_exp_f32 v40, v40
v_exp_f32 v41, v41
v_cvt_pk_bf16_f32 v11, v38, v39
v_exp_f32 v42, v42
v_exp_f32 v43, v43
v_mfma_f32_32x32x16_bf16 a[128:143], v[180:183], v[8:11], a[128:143]
v_cvt_pk_bf16_f32 v12, v40, v41
v_exp_f32 v44, v44
v_exp_f32 v45, v45
v_cvt_pk_bf16_f32 v13, v42, v43
v_exp_f32 v46, v46
v_exp_f32 v47, v47
v_cvt_pk_bf16_f32 v14, v44, v45
v_exp_f32 v48, v48
v_cvt_pk_bf16_f32 v15, v46, v47
v_exp_f32 v49, v49
v_exp_f32 v50, v50
v_mfma_f32_32x32x16_bf16 a[128:143], v[188:191], v[12:15], a[128:143]
v_exp_f32 v51, v51
v_cvt_pk_bf16_f32 v8, v48, v49
buffer_store_dwordx4 v[20:23], v3, s[20:23], s87 offen offset:1024 sc1
v_exp_f32 v52, v52
v_exp_f32 v53, v53
v_cvt_pk_bf16_f32 v9, v50, v51
v_exp_f32 v54, v54
)ASM"
R"ASM(v_mfma_f32_32x32x16_bf16 v[32:47], v[172:175], a[200:203], 0
v_exp_f32 v55, v55
v_cvt_pk_bf16_f32 v10, v52, v53
v_exp_f32 v56, v56
v_exp_f32 v57, v57
v_cvt_pk_bf16_f32 v11, v54, v55
v_exp_f32 v58, v58
v_exp_f32 v59, v59
v_mfma_f32_32x32x16_bf16 a[144:159], v[180:183], v[8:11], a[144:159]
v_cvt_pk_bf16_f32 v12, v56, v57
v_exp_f32 v60, v60
v_exp_f32 v61, v61
v_cvt_pk_bf16_f32 v13, v58, v59
v_exp_f32 v62, v62
v_exp_f32 v63, v63
v_cvt_pk_bf16_f32 v14, v60, v61
v_exp_f32 v32, v32
v_cvt_pk_bf16_f32 v15, v62, v63
v_exp_f32 v33, v33
v_exp_f32 v34, v34
v_mfma_f32_32x32x16_bf16 a[144:159], v[188:191], v[12:15], a[144:159]
v_exp_f32 v35, v35
v_cvt_pk_bf16_f32 v8, v32, v33
s_add_u32 s87, s47, 0x300000
buffer_store_dwordx4 v[20:23], v3, s[20:23], s87 offen sc1
v_exp_f32 v36, v36
v_exp_f32 v37, v37
v_cvt_pk_bf16_f32 v9, v34, v35
v_exp_f32 v38, v38
v_mfma_f32_32x32x16_bf16 v[48:63], v[172:175], a[204:207], 0
v_exp_f32 v39, v39
v_cvt_pk_bf16_f32 v10, v36, v37
)ASM"
R"ASM(v_exp_f32 v40, v40
v_exp_f32 v41, v41
v_cvt_pk_bf16_f32 v11, v38, v39
v_exp_f32 v42, v42
v_exp_f32 v43, v43
v_mfma_f32_32x32x16_bf16 a[160:175], v[180:183], v[8:11], a[160:175]
v_cvt_pk_bf16_f32 v12, v40, v41
v_exp_f32 v44, v44
v_exp_f32 v45, v45
v_cvt_pk_bf16_f32 v13, v42, v43
v_exp_f32 v46, v46
v_exp_f32 v47, v47
v_cvt_pk_bf16_f32 v14, v44, v45
v_exp_f32 v48, v48
v_cvt_pk_bf16_f32 v15, v46, v47
v_exp_f32 v49, v49
v_exp_f32 v50, v50
v_mfma_f32_32x32x16_bf16 a[160:175], v[188:191], v[12:15], a[160:175]
v_exp_f32 v51, v51
v_cvt_pk_bf16_f32 v8, v48, v49
buffer_store_dwordx4 v[20:23], v3, s[20:23], s87 offen offset:1024 sc1
v_exp_f32 v52, v52
v_exp_f32 v53, v53
v_cvt_pk_bf16_f32 v9, v50, v51
v_exp_f32 v54, v54
s_waitcnt lgkmcnt(0)
v_mfma_f32_32x32x16_bf16 v[32:47], v[64:67], a[192:195], 0
v_exp_f32 v55, v55
v_cvt_pk_bf16_f32 v10, v52, v53
v_exp_f32 v56, v56
v_exp_f32 v57, v57
v_cvt_pk_bf16_f32 v11, v54, v55
)ASM"
R"ASM(v_exp_f32 v58, v58
v_exp_f32 v59, v59
v_mfma_f32_32x32x16_bf16 a[176:191], v[180:183], v[8:11], a[176:191]
v_cvt_pk_bf16_f32 v12, v56, v57
v_exp_f32 v60, v60
v_exp_f32 v61, v61
v_cvt_pk_bf16_f32 v13, v58, v59
v_exp_f32 v62, v62
v_exp_f32 v63, v63
v_cvt_pk_bf16_f32 v14, v60, v61
v_exp_f32 v32, v32
v_cvt_pk_bf16_f32 v15, v62, v63
v_exp_f32 v33, v33
v_exp_f32 v34, v34
v_mfma_f32_32x32x16_bf16 a[176:191], v[188:191], v[12:15], a[176:191]
v_exp_f32 v35, v35
ds_read_b128 v[188:191], v29 offset:5280
v_cvt_pk_bf16_f32 v8, v32, v33
s_lshr_b32 s87, s47, 3
s_add_u32 s87, s87, 0x60000
buffer_store_dword v20, v17, s[20:23], s87 offen sc1
v_exp_f32 v36, v36
v_exp_f32 v37, v37
ds_read_b128 v[180:183], v28 offset:5280
v_cvt_pk_bf16_f32 v9, v34, v35
v_exp_f32 v38, v38
v_mfma_f32_32x32x16_bf16 v[48:63], v[64:67], a[196:199], 0
v_exp_f32 v39, v39
v_cvt_pk_bf16_f32 v10, v36, v37
ds_read_b128 v[172:175], v27 offset:5120
v_exp_f32 v40, v40
v_exp_f32 v41, v41
)ASM"
R"ASM(v_cvt_pk_bf16_f32 v11, v38, v39
v_exp_f32 v42, v42
v_exp_f32 v43, v43
v_mfma_f32_32x32x16_bf16 a[128:143], v[176:179], v[8:11], a[128:143]
v_cvt_pk_bf16_f32 v12, v40, v41
v_exp_f32 v44, v44
v_exp_f32 v45, v45
v_cvt_pk_bf16_f32 v13, v42, v43
v_exp_f32 v46, v46
v_exp_f32 v47, v47
v_cvt_pk_bf16_f32 v14, v44, v45
v_exp_f32 v48, v48
v_cvt_pk_bf16_f32 v15, v46, v47
v_exp_f32 v49, v49
v_exp_f32 v50, v50
v_mfma_f32_32x32x16_bf16 a[128:143], v[184:187], v[12:15], a[128:143]
v_exp_f32 v51, v51
v_cvt_pk_bf16_f32 v8, v48, v49
s_add_u32 s87, s73, 0
buffer_store_dwordx4 v[20:23], v3, s[20:23], s87 offen sc1
v_exp_f32 v52, v52
v_exp_f32 v53, v53
v_cvt_pk_bf16_f32 v9, v50, v51
v_exp_f32 v54, v54
v_mfma_f32_32x32x16_bf16 v[32:47], v[64:67], a[200:203], 0
v_exp_f32 v55, v55
v_cvt_pk_bf16_f32 v10, v52, v53
v_exp_f32 v56, v56
v_exp_f32 v57, v57
v_cvt_pk_bf16_f32 v11, v54, v55
v_exp_f32 v58, v58
v_exp_f32 v59, v59
)ASM"
R"ASM(v_mfma_f32_32x32x16_bf16 a[144:159], v[176:179], v[8:11], a[144:159]
v_cvt_pk_bf16_f32 v12, v56, v57
v_exp_f32 v60, v60
v_exp_f32 v61, v61
v_cvt_pk_bf16_f32 v13, v58, v59
v_exp_f32 v62, v62
v_exp_f32 v63, v63
v_cvt_pk_bf16_f32 v14, v60, v61
v_exp_f32 v32, v32
v_cvt_pk_bf16_f32 v15, v62, v63
v_exp_f32 v33, v33
v_exp_f32 v34, v34
v_mfma_f32_32x32x16_bf16 a[144:159], v[184:187], v[12:15], a[144:159]
v_exp_f32 v35, v35
v_cvt_pk_bf16_f32 v8, v32, v33
s_add_u32 s87, s73, 16384
buffer_store_dwordx4 v[20:23], v3, s[20:23], s87 offen sc1
v_exp_f32 v36, v36
v_exp_f32 v37, v37
v_cvt_pk_bf16_f32 v9, v34, v35
v_exp_f32 v38, v38
v_mfma_f32_32x32x16_bf16 v[48:63], v[64:67], a[204:207], 0
v_exp_f32 v39, v39
v_cvt_pk_bf16_f32 v10, v36, v37
v_exp_f32 v40, v40
v_exp_f32 v41, v41
v_cvt_pk_bf16_f32 v11, v38, v39
v_exp_f32 v42, v42
v_exp_f32 v43, v43
v_mfma_f32_32x32x16_bf16 a[160:175], v[176:179], v[8:11], a[160:175]
v_cvt_pk_bf16_f32 v12, v40, v41
v_exp_f32 v44, v44
)ASM"
R"ASM(v_exp_f32 v45, v45
v_cvt_pk_bf16_f32 v13, v42, v43
v_exp_f32 v46, v46
v_exp_f32 v47, v47
v_cvt_pk_bf16_f32 v14, v44, v45
v_exp_f32 v48, v48
v_cvt_pk_bf16_f32 v15, v46, v47
v_exp_f32 v49, v49
v_exp_f32 v50, v50
v_mfma_f32_32x32x16_bf16 a[160:175], v[184:187], v[12:15], a[160:175]
v_exp_f32 v51, v51
v_cvt_pk_bf16_f32 v8, v48, v49
s_add_u32 s87, s73, 32768
buffer_store_dwordx4 v[20:23], v3, s[20:23], s87 offen sc1
v_exp_f32 v52, v52
v_exp_f32 v53, v53
v_cvt_pk_bf16_f32 v9, v50, v51
v_exp_f32 v54, v54
s_waitcnt lgkmcnt(0)
v_mfma_f32_32x32x16_bf16 v[32:47], v[172:175], a[192:195], 0
v_exp_f32 v55, v55
v_cvt_pk_bf16_f32 v10, v52, v53
v_exp_f32 v56, v56
v_exp_f32 v57, v57
v_cvt_pk_bf16_f32 v11, v54, v55
v_exp_f32 v58, v58
v_exp_f32 v59, v59
v_mfma_f32_32x32x16_bf16 a[176:191], v[176:179], v[8:11], a[176:191]
v_cvt_pk_bf16_f32 v12, v56, v57
v_exp_f32 v60, v60
v_exp_f32 v61, v61
v_cvt_pk_bf16_f32 v13, v58, v59
)ASM"
R"ASM(v_exp_f32 v62, v62
v_exp_f32 v63, v63
v_cvt_pk_bf16_f32 v14, v60, v61
v_exp_f32 v32, v32
v_cvt_pk_bf16_f32 v15, v62, v63
v_exp_f32 v33, v33
v_exp_f32 v34, v34
v_mfma_f32_32x32x16_bf16 a[176:191], v[184:187], v[12:15], a[176:191]
v_exp_f32 v35, v35
ds_read_b128 v[184:187], v29 offset:6336
v_cvt_pk_bf16_f32 v8, v32, v33
s_add_u32 s87, s73, 49152
buffer_store_dwordx4 v[20:23], v3, s[20:23], s87 offen sc1
v_exp_f32 v36, v36
v_exp_f32 v37, v37
ds_read_b128 v[176:179], v28 offset:6336
v_cvt_pk_bf16_f32 v9, v34, v35
v_exp_f32 v38, v38
v_mfma_f32_32x32x16_bf16 v[48:63], v[172:175], a[196:199], 0
v_exp_f32 v39, v39
v_cvt_pk_bf16_f32 v10, v36, v37
ds_read_b128 v[64:67], v27 offset:6144
v_exp_f32 v40, v40
v_exp_f32 v41, v41
v_cvt_pk_bf16_f32 v11, v38, v39
v_exp_f32 v42, v42
v_exp_f32 v43, v43
v_mfma_f32_32x32x16_bf16 a[128:143], v[180:183], v[8:11], a[128:143]
v_cvt_pk_bf16_f32 v12, v40, v41
v_exp_f32 v44, v44
v_exp_f32 v45, v45
v_cvt_pk_bf16_f32 v13, v42, v43
)ASM"
R"ASM(v_exp_f32 v46, v46
v_exp_f32 v47, v47
v_cvt_pk_bf16_f32 v14, v44, v45
v_exp_f32 v48, v48
v_cvt_pk_bf16_f32 v15, v46, v47
v_exp_f32 v49, v49
v_exp_f32 v50, v50
v_mfma_f32_32x32x16_bf16 a[128:143], v[188:191], v[12:15], a[128:143]
v_exp_f32 v51, v51
v_cvt_pk_bf16_f32 v8, v48, v49
s_add_u32 s87, s73, 65536
buffer_store_dwordx4 v[20:23], v3, s[20:23], s87 offen sc1
v_exp_f32 v52, v52
v_exp_f32 v53, v53
v_cvt_pk_bf16_f32 v9, v50, v51
v_exp_f32 v54, v54
v_mfma_f32_32x32x16_bf16 v[32:47], v[172:175], a[200:203], 0
v_exp_f32 v55, v55
v_cvt_pk_bf16_f32 v10, v52, v53
v_exp_f32 v56, v56
v_exp_f32 v57, v57
v_cvt_pk_bf16_f32 v11, v54, v55
v_exp_f32 v58, v58
v_exp_f32 v59, v59
v_mfma_f32_32x32x16_bf16 a[144:159], v[180:183], v[8:11], a[144:159]
v_cvt_pk_bf16_f32 v12, v56, v57
v_exp_f32 v60, v60
v_exp_f32 v61, v61
v_cvt_pk_bf16_f32 v13, v58, v59
v_exp_f32 v62, v62
v_exp_f32 v63, v63
v_cvt_pk_bf16_f32 v14, v60, v61
)ASM"
R"ASM(v_exp_f32 v32, v32
v_cvt_pk_bf16_f32 v15, v62, v63
v_exp_f32 v33, v33
v_exp_f32 v34, v34
v_mfma_f32_32x32x16_bf16 a[144:159], v[188:191], v[12:15], a[144:159]
v_exp_f32 v35, v35
v_cvt_pk_bf16_f32 v8, v32, v33
s_add_u32 s87, s73, 81920
buffer_store_dwordx4 v[20:23], v3, s[20:23], s87 offen sc1
v_exp_f32 v36, v36
v_exp_f32 v37, v37
v_cvt_pk_bf16_f32 v9, v34, v35
v_exp_f32 v38, v38
v_mfma_f32_32x32x16_bf16 v[48:63], v[172:175], a[204:207], 0
v_exp_f32 v39, v39
v_cvt_pk_bf16_f32 v10, v36, v37
v_exp_f32 v40, v40
v_exp_f32 v41, v41
v_cvt_pk_bf16_f32 v11, v38, v39
v_exp_f32 v42, v42
v_exp_f32 v43, v43
v_mfma_f32_32x32x16_bf16 a[160:175], v[180:183], v[8:11], a[160:175]
v_cvt_pk_bf16_f32 v12, v40, v41
v_exp_f32 v44, v44
v_exp_f32 v45, v45
v_cvt_pk_bf16_f32 v13, v42, v43
v_exp_f32 v46, v46
v_exp_f32 v47, v47
v_cvt_pk_bf16_f32 v14, v44, v45
v_exp_f32 v48, v48
v_cvt_pk_bf16_f32 v15, v46, v47
v_exp_f32 v49, v49
)ASM"
R"ASM(v_exp_f32 v50, v50
v_mfma_f32_32x32x16_bf16 a[160:175], v[188:191], v[12:15], a[160:175]
v_exp_f32 v51, v51
v_cvt_pk_bf16_f32 v8, v48, v49
s_add_u32 s87, s73, 98304
buffer_store_dwordx4 v[20:23], v3, s[20:23], s87 offen sc1
v_exp_f32 v52, v52
v_exp_f32 v53, v53
v_cvt_pk_bf16_f32 v9, v50, v51
v_exp_f32 v54, v54
s_waitcnt lgkmcnt(0)
v_mfma_f32_32x32x16_bf16 v[32:47], v[64:67], a[192:195], 0
v_exp_f32 v55, v55
v_cvt_pk_bf16_f32 v10, v52, v53
v_exp_f32 v56, v56
v_exp_f32 v57, v57
v_cvt_pk_bf16_f32 v11, v54, v55
v_exp_f32 v58, v58
v_exp_f32 v59, v59
v_mfma_f32_32x32x16_bf16 a[176:191], v[180:183], v[8:11], a[176:191]
v_cvt_pk_bf16_f32 v12, v56, v57
v_exp_f32 v60, v60
v_exp_f32 v61, v61
v_cvt_pk_bf16_f32 v13, v58, v59
v_exp_f32 v62, v62
v_exp_f32 v63, v63
v_cvt_pk_bf16_f32 v14, v60, v61
v_exp_f32 v32, v32
v_cvt_pk_bf16_f32 v15, v62, v63
v_exp_f32 v33, v33
v_exp_f32 v34, v34
v_mfma_f32_32x32x16_bf16 a[176:191], v[188:191], v[12:15], a[176:191]
)ASM"
R"ASM(v_exp_f32 v35, v35
ds_read_b128 v[188:191], v29 offset:7392
v_cvt_pk_bf16_f32 v8, v32, v33
v_exp_f32 v36, v36
v_exp_f32 v37, v37
ds_read_b128 v[180:183], v28 offset:7392
v_cvt_pk_bf16_f32 v9, v34, v35
v_exp_f32 v38, v38
v_mfma_f32_32x32x16_bf16 v[48:63], v[64:67], a[196:199], 0
v_exp_f32 v39, v39
v_cvt_pk_bf16_f32 v10, v36, v37
ds_read_b128 v[172:175], v27 offset:7168
v_exp_f32 v40, v40
v_exp_f32 v41, v41
v_cvt_pk_bf16_f32 v11, v38, v39
v_exp_f32 v42, v42
v_exp_f32 v43, v43
v_mfma_f32_32x32x16_bf16 a[128:143], v[176:179], v[8:11], a[128:143]
v_cvt_pk_bf16_f32 v12, v40, v41
v_exp_f32 v44, v44
v_exp_f32 v45, v45
v_cvt_pk_bf16_f32 v13, v42, v43
v_exp_f32 v46, v46
v_exp_f32 v47, v47
v_cvt_pk_bf16_f32 v14, v44, v45
v_exp_f32 v48, v48
v_cvt_pk_bf16_f32 v15, v46, v47
v_exp_f32 v49, v49
v_exp_f32 v50, v50
v_mfma_f32_32x32x16_bf16 a[128:143], v[184:187], v[12:15], a[128:143]
v_exp_f32 v51, v51
v_cvt_pk_bf16_f32 v8, v48, v49
)ASM"
R"ASM(v_exp_f32 v52, v52
v_exp_f32 v53, v53
v_cvt_pk_bf16_f32 v9, v50, v51
v_exp_f32 v54, v54
v_mfma_f32_32x32x16_bf16 v[32:47], v[64:67], a[200:203], 0
v_exp_f32 v55, v55
v_cvt_pk_bf16_f32 v10, v52, v53
v_exp_f32 v56, v56
v_exp_f32 v57, v57
v_cvt_pk_bf16_f32 v11, v54, v55
v_exp_f32 v58, v58
v_exp_f32 v59, v59
v_mfma_f32_32x32x16_bf16 a[144:159], v[176:179], v[8:11], a[144:159]
v_cvt_pk_bf16_f32 v12, v56, v57
v_exp_f32 v60, v60
v_exp_f32 v61, v61
v_cvt_pk_bf16_f32 v13, v58, v59
v_exp_f32 v62, v62
v_exp_f32 v63, v63
v_cvt_pk_bf16_f32 v14, v60, v61
v_exp_f32 v32, v32
v_cvt_pk_bf16_f32 v15, v62, v63
v_exp_f32 v33, v33
v_exp_f32 v34, v34
v_mfma_f32_32x32x16_bf16 a[144:159], v[184:187], v[12:15], a[144:159]
v_exp_f32 v35, v35
v_cvt_pk_bf16_f32 v8, v32, v33
s_waitcnt vmcnt(12)
s_barrier
v_exp_f32 v36, v36
v_exp_f32 v37, v37
v_cvt_pk_bf16_f32 v9, v34, v35
)ASM"
R"ASM(v_exp_f32 v38, v38
v_mfma_f32_32x32x16_bf16 v[48:63], v[64:67], a[204:207], 0
v_exp_f32 v39, v39
v_cvt_pk_bf16_f32 v10, v36, v37
v_exp_f32 v40, v40
v_exp_f32 v41, v41
v_cvt_pk_bf16_f32 v11, v38, v39
v_exp_f32 v42, v42
v_exp_f32 v43, v43
v_mfma_f32_32x32x16_bf16 a[160:175], v[176:179], v[8:11], a[160:175]
v_cvt_pk_bf16_f32 v12, v40, v41
v_exp_f32 v44, v44
v_exp_f32 v45, v45
v_cvt_pk_bf16_f32 v13, v42, v43
v_exp_f32 v46, v46
v_exp_f32 v47, v47
v_cvt_pk_bf16_f32 v14, v44, v45
v_exp_f32 v48, v48
v_cvt_pk_bf16_f32 v15, v46, v47
v_exp_f32 v49, v49
v_exp_f32 v50, v50
v_mfma_f32_32x32x16_bf16 a[160:175], v[184:187], v[12:15], a[160:175]
v_exp_f32 v51, v51
v_cvt_pk_bf16_f32 v8, v48, v49
v_exp_f32 v52, v52
v_exp_f32 v53, v53
v_cvt_pk_bf16_f32 v9, v50, v51
v_exp_f32 v54, v54
s_waitcnt lgkmcnt(0)
v_mfma_f32_32x32x16_bf16 v[32:47], v[172:175], a[192:195], 0
v_exp_f32 v55, v55
v_cvt_pk_bf16_f32 v10, v52, v53
)ASM"
R"ASM(v_exp_f32 v56, v56
v_exp_f32 v57, v57
v_cvt_pk_bf16_f32 v11, v54, v55
v_exp_f32 v58, v58
v_exp_f32 v59, v59
v_mfma_f32_32x32x16_bf16 a[176:191], v[176:179], v[8:11], a[176:191]
v_cvt_pk_bf16_f32 v12, v56, v57
v_exp_f32 v60, v60
v_exp_f32 v61, v61
v_cvt_pk_bf16_f32 v13, v58, v59
v_exp_f32 v62, v62
v_exp_f32 v63, v63
v_cvt_pk_bf16_f32 v14, v60, v61
v_exp_f32 v32, v32
v_cvt_pk_bf16_f32 v15, v62, v63
v_exp_f32 v33, v33
v_exp_f32 v34, v34
v_mfma_f32_32x32x16_bf16 a[176:191], v[184:187], v[12:15], a[176:191]
v_exp_f32 v35, v35
ds_read_b128 v[184:187], v29 offset:8448
v_cvt_pk_bf16_f32 v8, v32, v33
v_exp_f32 v36, v36
v_exp_f32 v37, v37
ds_read_b128 v[176:179], v28 offset:8448
v_cvt_pk_bf16_f32 v9, v34, v35
v_exp_f32 v38, v38
v_mfma_f32_32x32x16_bf16 v[48:63], v[172:175], a[196:199], 0
v_exp_f32 v39, v39
v_cvt_pk_bf16_f32 v10, v36, v37
ds_read_b128 v[64:67], v27 offset:8192
v_exp_f32 v40, v40
v_exp_f32 v41, v41
)ASM"
R"ASM(v_cvt_pk_bf16_f32 v11, v38, v39
v_exp_f32 v42, v42
v_exp_f32 v43, v43
v_mfma_f32_32x32x16_bf16 a[128:143], v[180:183], v[8:11], a[128:143]
v_cvt_pk_bf16_f32 v12, v40, v41
v_exp_f32 v44, v44
v_exp_f32 v45, v45
v_cvt_pk_bf16_f32 v13, v42, v43
v_exp_f32 v46, v46
v_exp_f32 v47, v47
v_cvt_pk_bf16_f32 v14, v44, v45
v_exp_f32 v48, v48
v_cvt_pk_bf16_f32 v15, v46, v47
v_exp_f32 v49, v49
v_exp_f32 v50, v50
v_mfma_f32_32x32x16_bf16 a[128:143], v[188:191], v[12:15], a[128:143]
v_exp_f32 v51, v51
v_cvt_pk_bf16_f32 v8, v48, v49
v_exp_f32 v52, v52
v_exp_f32 v53, v53
v_cvt_pk_bf16_f32 v9, v50, v51
v_exp_f32 v54, v54
v_mfma_f32_32x32x16_bf16 v[32:47], v[172:175], a[200:203], 0
v_exp_f32 v55, v55
v_cvt_pk_bf16_f32 v10, v52, v53
v_exp_f32 v56, v56
v_exp_f32 v57, v57
v_cvt_pk_bf16_f32 v11, v54, v55
v_exp_f32 v58, v58
v_exp_f32 v59, v59
v_mfma_f32_32x32x16_bf16 a[144:159], v[180:183], v[8:11], a[144:159]
v_cvt_pk_bf16_f32 v12, v56, v57
)ASM"
R"ASM(v_exp_f32 v60, v60
v_exp_f32 v61, v61
v_cvt_pk_bf16_f32 v13, v58, v59
v_exp_f32 v62, v62
v_exp_f32 v63, v63
v_cvt_pk_bf16_f32 v14, v60, v61
v_exp_f32 v32, v32
v_cvt_pk_bf16_f32 v15, v62, v63
v_exp_f32 v33, v33
v_exp_f32 v34, v34
v_mfma_f32_32x32x16_bf16 a[144:159], v[188:191], v[12:15], a[144:159]
v_exp_f32 v35, v35
v_cvt_pk_bf16_f32 v8, v32, v33
v_exp_f32 v36, v36
v_exp_f32 v37, v37
v_cvt_pk_bf16_f32 v9, v34, v35
v_exp_f32 v38, v38
v_mfma_f32_32x32x16_bf16 v[48:63], v[172:175], a[204:207], 0
v_exp_f32 v39, v39
v_cvt_pk_bf16_f32 v10, v36, v37
v_exp_f32 v40, v40
v_exp_f32 v41, v41
v_cvt_pk_bf16_f32 v11, v38, v39
v_exp_f32 v42, v42
v_exp_f32 v43, v43
v_mfma_f32_32x32x16_bf16 a[160:175], v[180:183], v[8:11], a[160:175]
v_cvt_pk_bf16_f32 v12, v40, v41
v_exp_f32 v44, v44
v_exp_f32 v45, v45
v_cvt_pk_bf16_f32 v13, v42, v43
v_exp_f32 v46, v46
v_exp_f32 v47, v47
)ASM"
R"ASM(v_cvt_pk_bf16_f32 v14, v44, v45
v_exp_f32 v48, v48
v_cvt_pk_bf16_f32 v15, v46, v47
v_exp_f32 v49, v49
v_exp_f32 v50, v50
v_mfma_f32_32x32x16_bf16 a[160:175], v[188:191], v[12:15], a[160:175]
v_exp_f32 v51, v51
v_cvt_pk_bf16_f32 v8, v48, v49
v_exp_f32 v52, v52
v_exp_f32 v53, v53
v_cvt_pk_bf16_f32 v9, v50, v51
v_exp_f32 v54, v54
s_waitcnt lgkmcnt(0)
v_mfma_f32_32x32x16_bf16 v[32:47], v[64:67], a[192:195], 0
v_exp_f32 v55, v55
v_cvt_pk_bf16_f32 v10, v52, v53
v_exp_f32 v56, v56
v_exp_f32 v57, v57
v_cvt_pk_bf16_f32 v11, v54, v55
v_exp_f32 v58, v58
v_exp_f32 v59, v59
v_mfma_f32_32x32x16_bf16 a[176:191], v[180:183], v[8:11], a[176:191]
v_cvt_pk_bf16_f32 v12, v56, v57
v_exp_f32 v60, v60
v_exp_f32 v61, v61
v_cvt_pk_bf16_f32 v13, v58, v59
v_exp_f32 v62, v62
v_exp_f32 v63, v63
v_cvt_pk_bf16_f32 v14, v60, v61
v_exp_f32 v32, v32
v_cvt_pk_bf16_f32 v15, v62, v63
v_exp_f32 v33, v33
)ASM"
R"ASM(v_exp_f32 v34, v34
v_mfma_f32_32x32x16_bf16 a[176:191], v[188:191], v[12:15], a[176:191]
v_exp_f32 v35, v35
ds_read_b128 v[188:191], v29 offset:9504
v_cvt_pk_bf16_f32 v8, v32, v33
v_exp_f32 v36, v36
v_exp_f32 v37, v37
ds_read_b128 v[180:183], v28 offset:9504
v_cvt_pk_bf16_f32 v9, v34, v35
v_exp_f32 v38, v38
v_mfma_f32_32x32x16_bf16 v[48:63], v[64:67], a[196:199], 0
v_exp_f32 v39, v39
v_cvt_pk_bf16_f32 v10, v36, v37
ds_read_b128 v[172:175], v27 offset:9216
v_exp_f32 v40, v40
v_exp_f32 v41, v41
v_cvt_pk_bf16_f32 v11, v38, v39
v_exp_f32 v42, v42
v_exp_f32 v43, v43
v_mfma_f32_32x32x16_bf16 a[128:143], v[176:179], v[8:11], a[128:143]
v_cvt_pk_bf16_f32 v12, v40, v41
v_exp_f32 v44, v44
v_exp_f32 v45, v45
v_cvt_pk_bf16_f32 v13, v42, v43
v_exp_f32 v46, v46
v_exp_f32 v47, v47
v_cvt_pk_bf16_f32 v14, v44, v45
v_exp_f32 v48, v48
v_cvt_pk_bf16_f32 v15, v46, v47
v_exp_f32 v49, v49
v_exp_f32 v50, v50
v_mfma_f32_32x32x16_bf16 a[128:143], v[184:187], v[12:15], a[128:143]
)ASM"
R"ASM(v_exp_f32 v51, v51
v_cvt_pk_bf16_f32 v8, v48, v49
v_exp_f32 v52, v52
v_exp_f32 v53, v53
v_cvt_pk_bf16_f32 v9, v50, v51
v_exp_f32 v54, v54
v_mfma_f32_32x32x16_bf16 v[32:47], v[64:67], a[200:203], 0
v_exp_f32 v55, v55
v_cvt_pk_bf16_f32 v10, v52, v53
v_exp_f32 v56, v56
v_exp_f32 v57, v57
v_cvt_pk_bf16_f32 v11, v54, v55
v_exp_f32 v58, v58
v_exp_f32 v59, v59
v_mfma_f32_32x32x16_bf16 a[144:159], v[176:179], v[8:11], a[144:159]
v_cvt_pk_bf16_f32 v12, v56, v57
v_exp_f32 v60, v60
v_exp_f32 v61, v61
v_cvt_pk_bf16_f32 v13, v58, v59
v_exp_f32 v62, v62
v_exp_f32 v63, v63
v_cvt_pk_bf16_f32 v14, v60, v61
v_exp_f32 v32, v32
v_cvt_pk_bf16_f32 v15, v62, v63
v_exp_f32 v33, v33
v_exp_f32 v34, v34
v_mfma_f32_32x32x16_bf16 a[144:159], v[184:187], v[12:15], a[144:159]
v_exp_f32 v35, v35
v_cvt_pk_bf16_f32 v8, v32, v33
s_waitcnt vmcnt(0)
s_barrier
s_lshl_b32 s96, s96, 7
)ASM"
R"ASM(s_add_u32 s96, s96, 0x10000
buffer_store_dwordx2 v[24:25], v26, s[32:35], s96 offen sc1
s_lshl_b32 s96, s10, 7
s_add_u32 s96, s96, 0x18000
buffer_store_dwordx2 v[24:25], v26, s[20:23], s96 offen sc1
v_exp_f32 v36, v36
v_exp_f32 v37, v37
v_cvt_pk_bf16_f32 v9, v34, v35
v_exp_f32 v38, v38
v_mfma_f32_32x32x16_bf16 v[48:63], v[64:67], a[204:207], 0
v_exp_f32 v39, v39
v_cvt_pk_bf16_f32 v10, v36, v37
v_exp_f32 v40, v40
v_exp_f32 v41, v41
v_cvt_pk_bf16_f32 v11, v38, v39
v_exp_f32 v42, v42
v_exp_f32 v43, v43
v_mfma_f32_32x32x16_bf16 a[160:175], v[176:179], v[8:11], a[160:175]
v_cvt_pk_bf16_f32 v12, v40, v41
v_exp_f32 v44, v44
v_exp_f32 v45, v45
v_cvt_pk_bf16_f32 v13, v42, v43
v_exp_f32 v46, v46
v_exp_f32 v47, v47
v_cvt_pk_bf16_f32 v14, v44, v45
v_exp_f32 v48, v48
v_cvt_pk_bf16_f32 v15, v46, v47
v_exp_f32 v49, v49
v_exp_f32 v50, v50
v_mfma_f32_32x32x16_bf16 a[160:175], v[184:187], v[12:15], a[160:175]
v_exp_f32 v51, v51
v_cvt_pk_bf16_f32 v8, v48, v49
)ASM"
R"ASM(v_exp_f32 v52, v52
v_exp_f32 v53, v53
v_cvt_pk_bf16_f32 v9, v50, v51
v_exp_f32 v54, v54
s_waitcnt lgkmcnt(0)
v_mfma_f32_32x32x16_bf16 v[32:47], v[172:175], a[192:195], 0
v_exp_f32 v55, v55
v_cvt_pk_bf16_f32 v10, v52, v53
v_exp_f32 v56, v56
v_exp_f32 v57, v57
v_cvt_pk_bf16_f32 v11, v54, v55
v_exp_f32 v58, v58
v_exp_f32 v59, v59
v_mfma_f32_32x32x16_bf16 a[176:191], v[176:179], v[8:11], a[176:191]
v_cvt_pk_bf16_f32 v12, v56, v57
v_exp_f32 v60, v60
v_exp_f32 v61, v61
v_cvt_pk_bf16_f32 v13, v58, v59
v_exp_f32 v62, v62
v_exp_f32 v63, v63
v_cvt_pk_bf16_f32 v14, v60, v61
v_exp_f32 v32, v32
v_cvt_pk_bf16_f32 v15, v62, v63
v_exp_f32 v33, v33
v_exp_f32 v34, v34
v_mfma_f32_32x32x16_bf16 a[176:191], v[184:187], v[12:15], a[176:191]
v_exp_f32 v35, v35
ds_read_b128 v[184:187], v29 offset:10560
v_cvt_pk_bf16_f32 v8, v32, v33
v_exp_f32 v36, v36
v_exp_f32 v37, v37
ds_read_b128 v[176:179], v28 offset:10560
)ASM"
R"ASM(v_cvt_pk_bf16_f32 v9, v34, v35
v_exp_f32 v38, v38
v_mfma_f32_32x32x16_bf16 v[48:63], v[172:175], a[196:199], 0
v_exp_f32 v39, v39
v_cvt_pk_bf16_f32 v10, v36, v37
ds_read_b128 v[64:67], v27 offset:10240
v_exp_f32 v40, v40
v_exp_f32 v41, v41
v_cvt_pk_bf16_f32 v11, v38, v39
v_exp_f32 v42, v42
v_exp_f32 v43, v43
v_mfma_f32_32x32x16_bf16 a[128:143], v[180:183], v[8:11], a[128:143]
v_cvt_pk_bf16_f32 v12, v40, v41
v_exp_f32 v44, v44
v_exp_f32 v45, v45
v_cvt_pk_bf16_f32 v13, v42, v43
v_exp_f32 v46, v46
v_exp_f32 v47, v47
v_cvt_pk_bf16_f32 v14, v44, v45
v_exp_f32 v48, v48
v_cvt_pk_bf16_f32 v15, v46, v47
v_exp_f32 v49, v49
v_exp_f32 v50, v50
v_mfma_f32_32x32x16_bf16 a[128:143], v[188:191], v[12:15], a[128:143]
v_exp_f32 v51, v51
v_cvt_pk_bf16_f32 v8, v48, v49
v_exp_f32 v52, v52
v_exp_f32 v53, v53
v_cvt_pk_bf16_f32 v9, v50, v51
v_exp_f32 v54, v54
v_mfma_f32_32x32x16_bf16 v[32:47], v[172:175], a[200:203], 0
v_exp_f32 v55, v55
)ASM"
R"ASM(v_cvt_pk_bf16_f32 v10, v52, v53
v_exp_f32 v56, v56
v_exp_f32 v57, v57
v_cvt_pk_bf16_f32 v11, v54, v55
v_exp_f32 v58, v58
v_exp_f32 v59, v59
v_mfma_f32_32x32x16_bf16 a[144:159], v[180:183], v[8:11], a[144:159]
v_cvt_pk_bf16_f32 v12, v56, v57
v_exp_f32 v60, v60
v_exp_f32 v61, v61
v_cvt_pk_bf16_f32 v13, v58, v59
v_exp_f32 v62, v62
v_exp_f32 v63, v63
v_cvt_pk_bf16_f32 v14, v60, v61
v_exp_f32 v32, v32
v_cvt_pk_bf16_f32 v15, v62, v63
v_exp_f32 v33, v33
v_exp_f32 v34, v34
v_mfma_f32_32x32x16_bf16 a[144:159], v[188:191], v[12:15], a[144:159]
v_exp_f32 v35, v35
v_cvt_pk_bf16_f32 v8, v32, v33
v_exp_f32 v36, v36
v_exp_f32 v37, v37
v_cvt_pk_bf16_f32 v9, v34, v35
v_exp_f32 v38, v38
v_mfma_f32_32x32x16_bf16 v[48:63], v[172:175], a[204:207], 0
v_exp_f32 v39, v39
v_cvt_pk_bf16_f32 v10, v36, v37
v_exp_f32 v40, v40
v_exp_f32 v41, v41
v_cvt_pk_bf16_f32 v11, v38, v39
v_exp_f32 v42, v42
)ASM"
R"ASM(v_exp_f32 v43, v43
v_mfma_f32_32x32x16_bf16 a[160:175], v[180:183], v[8:11], a[160:175]
v_cvt_pk_bf16_f32 v12, v40, v41
v_exp_f32 v44, v44
v_exp_f32 v45, v45
v_cvt_pk_bf16_f32 v13, v42, v43
v_exp_f32 v46, v46
v_exp_f32 v47, v47
v_cvt_pk_bf16_f32 v14, v44, v45
v_exp_f32 v48, v48
v_cvt_pk_bf16_f32 v15, v46, v47
v_exp_f32 v49, v49
v_exp_f32 v50, v50
v_mfma_f32_32x32x16_bf16 a[160:175], v[188:191], v[12:15], a[160:175]
v_exp_f32 v51, v51
v_cvt_pk_bf16_f32 v8, v48, v49
v_exp_f32 v52, v52
v_exp_f32 v53, v53
v_cvt_pk_bf16_f32 v9, v50, v51
v_exp_f32 v54, v54
s_waitcnt lgkmcnt(0)
v_mfma_f32_32x32x16_bf16 v[32:47], v[64:67], a[192:195], 0
v_exp_f32 v55, v55
v_cvt_pk_bf16_f32 v10, v52, v53
v_exp_f32 v56, v56
v_exp_f32 v57, v57
v_cvt_pk_bf16_f32 v11, v54, v55
v_exp_f32 v58, v58
v_exp_f32 v59, v59
v_mfma_f32_32x32x16_bf16 a[176:191], v[180:183], v[8:11], a[176:191]
v_cvt_pk_bf16_f32 v12, v56, v57
v_exp_f32 v60, v60
)ASM"
R"ASM(v_exp_f32 v61, v61
v_cvt_pk_bf16_f32 v13, v58, v59
v_exp_f32 v62, v62
v_exp_f32 v63, v63
v_cvt_pk_bf16_f32 v14, v60, v61
v_exp_f32 v32, v32
v_cvt_pk_bf16_f32 v15, v62, v63
v_exp_f32 v33, v33
v_exp_f32 v34, v34
v_mfma_f32_32x32x16_bf16 a[176:191], v[188:191], v[12:15], a[176:191]
v_exp_f32 v35, v35
ds_read_b128 v[188:191], v29 offset:11616
v_cvt_pk_bf16_f32 v8, v32, v33
v_exp_f32 v36, v36
v_exp_f32 v37, v37
ds_read_b128 v[180:183], v28 offset:11616
v_cvt_pk_bf16_f32 v9, v34, v35
v_exp_f32 v38, v38
v_mfma_f32_32x32x16_bf16 v[48:63], v[64:67], a[196:199], 0
v_exp_f32 v39, v39
v_cvt_pk_bf16_f32 v10, v36, v37
ds_read_b128 v[172:175], v27 offset:11264
v_exp_f32 v40, v40
v_exp_f32 v41, v41
v_cvt_pk_bf16_f32 v11, v38, v39
v_exp_f32 v42, v42
v_exp_f32 v43, v43
v_mfma_f32_32x32x16_bf16 a[128:143], v[176:179], v[8:11], a[128:143]
v_cvt_pk_bf16_f32 v12, v40, v41
v_exp_f32 v44, v44
v_exp_f32 v45, v45
v_cvt_pk_bf16_f32 v13, v42, v43
)ASM"
R"ASM(v_exp_f32 v46, v46
v_exp_f32 v47, v47
v_cvt_pk_bf16_f32 v14, v44, v45
v_exp_f32 v48, v48
v_cvt_pk_bf16_f32 v15, v46, v47
v_exp_f32 v49, v49
v_exp_f32 v50, v50
v_mfma_f32_32x32x16_bf16 a[128:143], v[184:187], v[12:15], a[128:143]
v_exp_f32 v51, v51
v_cvt_pk_bf16_f32 v8, v48, v49
v_exp_f32 v52, v52
v_exp_f32 v53, v53
v_cvt_pk_bf16_f32 v9, v50, v51
v_exp_f32 v54, v54
v_mfma_f32_32x32x16_bf16 v[32:47], v[64:67], a[200:203], 0
v_exp_f32 v55, v55
v_cvt_pk_bf16_f32 v10, v52, v53
v_exp_f32 v56, v56
v_exp_f32 v57, v57
v_cvt_pk_bf16_f32 v11, v54, v55
v_exp_f32 v58, v58
v_exp_f32 v59, v59
v_mfma_f32_32x32x16_bf16 a[144:159], v[176:179], v[8:11], a[144:159]
v_cvt_pk_bf16_f32 v12, v56, v57
v_exp_f32 v60, v60
v_exp_f32 v61, v61
v_cvt_pk_bf16_f32 v13, v58, v59
v_exp_f32 v62, v62
v_exp_f32 v63, v63
v_cvt_pk_bf16_f32 v14, v60, v61
v_exp_f32 v32, v32
v_cvt_pk_bf16_f32 v15, v62, v63
)ASM"
R"ASM(v_exp_f32 v33, v33
v_exp_f32 v34, v34
v_mfma_f32_32x32x16_bf16 a[144:159], v[184:187], v[12:15], a[144:159]
v_exp_f32 v35, v35
v_cvt_pk_bf16_f32 v8, v32, v33
s_mov_b32 s98, 0x10000
buffer_load_dwordx2 v[18:19], v16, s[20:23], s98 offen sc1
v_exp_f32 v36, v36
v_exp_f32 v37, v37
v_cvt_pk_bf16_f32 v9, v34, v35
v_exp_f32 v38, v38
v_mfma_f32_32x32x16_bf16 v[48:63], v[64:67], a[204:207], 0
v_exp_f32 v39, v39
v_cvt_pk_bf16_f32 v10, v36, v37
v_exp_f32 v40, v40
v_exp_f32 v41, v41
v_cvt_pk_bf16_f32 v11, v38, v39
v_exp_f32 v42, v42
v_exp_f32 v43, v43
v_mfma_f32_32x32x16_bf16 a[160:175], v[176:179], v[8:11], a[160:175]
v_cvt_pk_bf16_f32 v12, v40, v41
v_exp_f32 v44, v44
v_exp_f32 v45, v45
v_cvt_pk_bf16_f32 v13, v42, v43
v_exp_f32 v46, v46
v_exp_f32 v47, v47
v_cvt_pk_bf16_f32 v14, v44, v45
v_exp_f32 v48, v48
v_cvt_pk_bf16_f32 v15, v46, v47
v_exp_f32 v49, v49
v_exp_f32 v50, v50
v_mfma_f32_32x32x16_bf16 a[160:175], v[184:187], v[12:15], a[160:175]
)ASM"
R"ASM(v_exp_f32 v51, v51
v_cvt_pk_bf16_f32 v8, v48, v49
v_exp_f32 v52, v52
v_exp_f32 v53, v53
v_cvt_pk_bf16_f32 v9, v50, v51
v_exp_f32 v54, v54
s_waitcnt lgkmcnt(0)
v_mfma_f32_32x32x16_bf16 v[32:47], v[172:175], a[192:195], 0
v_exp_f32 v55, v55
v_cvt_pk_bf16_f32 v10, v52, v53
v_exp_f32 v56, v56
v_exp_f32 v57, v57
v_cvt_pk_bf16_f32 v11, v54, v55
v_exp_f32 v58, v58
v_exp_f32 v59, v59
v_mfma_f32_32x32x16_bf16 a[176:191], v[176:179], v[8:11], a[176:191]
v_cvt_pk_bf16_f32 v12, v56, v57
v_exp_f32 v60, v60
v_exp_f32 v61, v61
v_cvt_pk_bf16_f32 v13, v58, v59
v_exp_f32 v62, v62
v_exp_f32 v63, v63
v_cvt_pk_bf16_f32 v14, v60, v61
v_exp_f32 v32, v32
v_cvt_pk_bf16_f32 v15, v62, v63
v_exp_f32 v33, v33
v_exp_f32 v34, v34
v_mfma_f32_32x32x16_bf16 a[176:191], v[184:187], v[12:15], a[176:191]
v_exp_f32 v35, v35
ds_read_b128 v[184:187], v29 offset:12672
v_cvt_pk_bf16_f32 v8, v32, v33
v_exp_f32 v36, v36
)ASM"
R"ASM(v_exp_f32 v37, v37
ds_read_b128 v[176:179], v28 offset:12672
v_cvt_pk_bf16_f32 v9, v34, v35
v_exp_f32 v38, v38
v_mfma_f32_32x32x16_bf16 v[48:63], v[172:175], a[196:199], 0
v_exp_f32 v39, v39
v_cvt_pk_bf16_f32 v10, v36, v37
ds_read_b128 v[64:67], v27 offset:12288
v_exp_f32 v40, v40
v_exp_f32 v41, v41
v_cvt_pk_bf16_f32 v11, v38, v39
v_exp_f32 v42, v42
v_exp_f32 v43, v43
v_mfma_f32_32x32x16_bf16 a[128:143], v[180:183], v[8:11], a[128:143]
v_cvt_pk_bf16_f32 v12, v40, v41
v_exp_f32 v44, v44
v_exp_f32 v45, v45
v_cvt_pk_bf16_f32 v13, v42, v43
v_exp_f32 v46, v46
v_exp_f32 v47, v47
v_cvt_pk_bf16_f32 v14, v44, v45
v_exp_f32 v48, v48
v_cvt_pk_bf16_f32 v15, v46, v47
v_exp_f32 v49, v49
v_exp_f32 v50, v50
v_mfma_f32_32x32x16_bf16 a[128:143], v[188:191], v[12:15], a[128:143]
v_exp_f32 v51, v51
v_cvt_pk_bf16_f32 v8, v48, v49
v_exp_f32 v52, v52
v_exp_f32 v53, v53
v_cvt_pk_bf16_f32 v9, v50, v51
v_exp_f32 v54, v54
)ASM"
R"ASM(v_mfma_f32_32x32x16_bf16 v[32:47], v[172:175], a[200:203], 0
v_exp_f32 v55, v55
v_cvt_pk_bf16_f32 v10, v52, v53
v_exp_f32 v56, v56
v_exp_f32 v57, v57
v_cvt_pk_bf16_f32 v11, v54, v55
v_exp_f32 v58, v58
v_exp_f32 v59, v59
v_mfma_f32_32x32x16_bf16 a[144:159], v[180:183], v[8:11], a[144:159]
v_cvt_pk_bf16_f32 v12, v56, v57
v_exp_f32 v60, v60
v_exp_f32 v61, v61
v_cvt_pk_bf16_f32 v13, v58, v59
v_exp_f32 v62, v62
v_exp_f32 v63, v63
v_cvt_pk_bf16_f32 v14, v60, v61
v_exp_f32 v32, v32
v_cvt_pk_bf16_f32 v15, v62, v63
v_exp_f32 v33, v33
v_exp_f32 v34, v34
v_mfma_f32_32x32x16_bf16 a[144:159], v[188:191], v[12:15], a[144:159]
v_exp_f32 v35, v35
v_cvt_pk_bf16_f32 v8, v32, v33
v_exp_f32 v36, v36
v_exp_f32 v37, v37
v_cvt_pk_bf16_f32 v9, v34, v35
v_exp_f32 v38, v38
v_mfma_f32_32x32x16_bf16 v[48:63], v[172:175], a[204:207], 0
v_exp_f32 v39, v39
v_cvt_pk_bf16_f32 v10, v36, v37
v_exp_f32 v40, v40
v_exp_f32 v41, v41
)ASM"
R"ASM(v_cvt_pk_bf16_f32 v11, v38, v39
v_exp_f32 v42, v42
v_exp_f32 v43, v43
v_mfma_f32_32x32x16_bf16 a[160:175], v[180:183], v[8:11], a[160:175]
v_cvt_pk_bf16_f32 v12, v40, v41
v_exp_f32 v44, v44
v_exp_f32 v45, v45
v_cvt_pk_bf16_f32 v13, v42, v43
v_exp_f32 v46, v46
v_exp_f32 v47, v47
v_cvt_pk_bf16_f32 v14, v44, v45
v_exp_f32 v48, v48
v_cvt_pk_bf16_f32 v15, v46, v47
v_exp_f32 v49, v49
v_exp_f32 v50, v50
v_mfma_f32_32x32x16_bf16 a[160:175], v[188:191], v[12:15], a[160:175]
v_exp_f32 v51, v51
v_cvt_pk_bf16_f32 v8, v48, v49
v_exp_f32 v52, v52
v_exp_f32 v53, v53
v_cvt_pk_bf16_f32 v9, v50, v51
v_exp_f32 v54, v54
s_waitcnt lgkmcnt(0)
v_mfma_f32_32x32x16_bf16 v[32:47], v[64:67], a[192:195], 0
v_exp_f32 v55, v55
v_cvt_pk_bf16_f32 v10, v52, v53
v_exp_f32 v56, v56
v_exp_f32 v57, v57
v_cvt_pk_bf16_f32 v11, v54, v55
v_exp_f32 v58, v58
v_exp_f32 v59, v59
v_mfma_f32_32x32x16_bf16 a[176:191], v[180:183], v[8:11], a[176:191]
)ASM"
R"ASM(v_cvt_pk_bf16_f32 v12, v56, v57
v_exp_f32 v60, v60
v_exp_f32 v61, v61
v_cvt_pk_bf16_f32 v13, v58, v59
v_exp_f32 v62, v62
v_exp_f32 v63, v63
v_cvt_pk_bf16_f32 v14, v60, v61
v_exp_f32 v32, v32
v_cvt_pk_bf16_f32 v15, v62, v63
v_exp_f32 v33, v33
v_exp_f32 v34, v34
v_mfma_f32_32x32x16_bf16 a[176:191], v[188:191], v[12:15], a[176:191]
v_exp_f32 v35, v35
ds_read_b128 v[188:191], v29 offset:13728
v_cvt_pk_bf16_f32 v8, v32, v33
s_waitcnt vmcnt(0)
v_cmp_eq_u64 vcc, v[18:19], s[18:19]
s_cmp_eq_u64 vcc, -1
s_cbranch_scc1 Ltfok_26_%=
s_mov_b32 s99, 0
s_mov_b32 s98, 0x10000
Ltfsp_27_%=:
buffer_load_dwordx2 v[18:19], v16, s[20:23], s98 offen sc1
s_waitcnt vmcnt(0)
v_cmp_eq_u64 vcc, v[18:19], s[18:19]
s_cmp_eq_u64 vcc, -1
s_cbranch_scc1 Ltfok_26_%=
s_add_u32 s99, s99, 1
s_cmp_lt_u32 s99, 0x8000
s_cbranch_scc1 Ltfsp_27_%=
Ltfok_26_%=:
v_exp_f32 v36, v36
)ASM"
R"ASM(v_exp_f32 v37, v37
ds_read_b128 v[180:183], v28 offset:13728
v_cvt_pk_bf16_f32 v9, v34, v35
v_exp_f32 v38, v38
v_mfma_f32_32x32x16_bf16 v[48:63], v[64:67], a[196:199], 0
v_exp_f32 v39, v39
v_cvt_pk_bf16_f32 v10, v36, v37
ds_read_b128 v[172:175], v27 offset:13312
s_lshl_b32 s81, s15, 1
s_add_u32 s82, s81, 1
s_add_u32 s81, s81, s101
s_sub_u32 s82, s82, s101
s_lshl_b32 s81, s81, 15
s_lshl_b32 s82, s82, 15
s_lshl_b32 s83, s101, 14
s_add_u32 s83, s83, 0x1200000
s_add_u32 s81, s81, s83
s_add_u32 s82, s82, s83
v_exp_f32 v40, v40
v_exp_f32 v41, v41
v_cvt_pk_bf16_f32 v11, v38, v39
v_exp_f32 v42, v42
v_exp_f32 v43, v43
v_mfma_f32_32x32x16_bf16 a[128:143], v[176:179], v[8:11], a[128:143]
v_cvt_pk_bf16_f32 v12, v40, v41
v_exp_f32 v44, v44
v_exp_f32 v45, v45
v_cvt_pk_bf16_f32 v13, v42, v43
v_exp_f32 v46, v46
v_exp_f32 v47, v47
v_cvt_pk_bf16_f32 v14, v44, v45
v_exp_f32 v48, v48
)ASM"
R"ASM(v_cvt_pk_bf16_f32 v15, v46, v47
v_exp_f32 v49, v49
v_exp_f32 v50, v50
v_mfma_f32_32x32x16_bf16 a[128:143], v[184:187], v[12:15], a[128:143]
v_exp_f32 v51, v51
v_cvt_pk_bf16_f32 v8, v48, v49
v_exp_f32 v52, v52
v_exp_f32 v53, v53
v_cvt_pk_bf16_f32 v9, v50, v51
v_exp_f32 v54, v54
v_mfma_f32_32x32x16_bf16 v[32:47], v[64:67], a[200:203], 0
v_exp_f32 v55, v55
v_cvt_pk_bf16_f32 v10, v52, v53
v_exp_f32 v56, v56
v_exp_f32 v57, v57
v_cvt_pk_bf16_f32 v11, v54, v55
v_exp_f32 v58, v58
v_exp_f32 v59, v59
v_mfma_f32_32x32x16_bf16 a[144:159], v[176:179], v[8:11], a[144:159]
v_cvt_pk_bf16_f32 v12, v56, v57
v_exp_f32 v60, v60
v_exp_f32 v61, v61
v_cvt_pk_bf16_f32 v13, v58, v59
v_exp_f32 v62, v62
v_exp_f32 v63, v63
v_cvt_pk_bf16_f32 v14, v60, v61
v_exp_f32 v32, v32
v_cvt_pk_bf16_f32 v15, v62, v63
v_exp_f32 v33, v33
v_exp_f32 v34, v34
v_mfma_f32_32x32x16_bf16 a[144:159], v[184:187], v[12:15], a[144:159]
v_exp_f32 v35, v35
)ASM"
R"ASM(v_cvt_pk_bf16_f32 v8, v32, v33
buffer_load_dwordx4 a[0:3], v3, s[20:23], s81 offen offset:0 sc1
v_exp_f32 v36, v36
v_exp_f32 v37, v37
v_cvt_pk_bf16_f32 v9, v34, v35
v_exp_f32 v38, v38
v_mfma_f32_32x32x16_bf16 v[48:63], v[64:67], a[204:207], 0
v_exp_f32 v39, v39
v_cvt_pk_bf16_f32 v10, v36, v37
v_exp_f32 v40, v40
v_exp_f32 v41, v41
v_cvt_pk_bf16_f32 v11, v38, v39
v_exp_f32 v42, v42
v_exp_f32 v43, v43
v_mfma_f32_32x32x16_bf16 a[160:175], v[176:179], v[8:11], a[160:175]
v_cvt_pk_bf16_f32 v12, v40, v41
v_exp_f32 v44, v44
v_exp_f32 v45, v45
v_cvt_pk_bf16_f32 v13, v42, v43
v_exp_f32 v46, v46
v_exp_f32 v47, v47
v_cvt_pk_bf16_f32 v14, v44, v45
v_exp_f32 v48, v48
v_cvt_pk_bf16_f32 v15, v46, v47
v_exp_f32 v49, v49
v_exp_f32 v50, v50
v_mfma_f32_32x32x16_bf16 a[160:175], v[184:187], v[12:15], a[160:175]
v_exp_f32 v51, v51
v_cvt_pk_bf16_f32 v8, v48, v49
s_add_u32 s86, s74, 131072
buffer_load_dwordx4 v[68:71], v3, s[20:23], s86 offen offset:0 sc1
v_exp_f32 v52, v52
)ASM"
R"ASM(v_exp_f32 v53, v53
v_cvt_pk_bf16_f32 v9, v50, v51
v_exp_f32 v54, v54
s_waitcnt lgkmcnt(0)
v_mfma_f32_32x32x16_bf16 v[32:47], v[172:175], a[192:195], 0
v_exp_f32 v55, v55
v_cvt_pk_bf16_f32 v10, v52, v53
v_exp_f32 v56, v56
v_exp_f32 v57, v57
v_cvt_pk_bf16_f32 v11, v54, v55
v_exp_f32 v58, v58
v_exp_f32 v59, v59
v_mfma_f32_32x32x16_bf16 a[176:191], v[176:179], v[8:11], a[176:191]
v_cvt_pk_bf16_f32 v12, v56, v57
v_exp_f32 v60, v60
v_exp_f32 v61, v61
v_cvt_pk_bf16_f32 v13, v58, v59
v_exp_f32 v62, v62
v_exp_f32 v63, v63
v_cvt_pk_bf16_f32 v14, v60, v61
v_exp_f32 v32, v32
v_cvt_pk_bf16_f32 v15, v62, v63
v_exp_f32 v33, v33
v_exp_f32 v34, v34
v_mfma_f32_32x32x16_bf16 a[176:191], v[184:187], v[12:15], a[176:191]
v_exp_f32 v35, v35
ds_read_b128 v[184:187], v29 offset:14784
v_cvt_pk_bf16_f32 v8, v32, v33
buffer_load_dwordx4 a[64:67], v3, s[20:23], s82 offen offset:0 sc1
v_exp_f32 v36, v36
v_exp_f32 v37, v37
ds_read_b128 v[176:179], v28 offset:14784
)ASM"
R"ASM(v_cvt_pk_bf16_f32 v9, v34, v35
v_exp_f32 v38, v38
v_mfma_f32_32x32x16_bf16 v[48:63], v[172:175], a[196:199], 0
v_exp_f32 v39, v39
v_cvt_pk_bf16_f32 v10, v36, v37
ds_read_b128 v[64:67], v27 offset:14336
v_exp_f32 v40, v40
v_exp_f32 v41, v41
v_cvt_pk_bf16_f32 v11, v38, v39
v_exp_f32 v42, v42
v_exp_f32 v43, v43
v_mfma_f32_32x32x16_bf16 a[128:143], v[180:183], v[8:11], a[128:143]
v_cvt_pk_bf16_f32 v12, v40, v41
v_exp_f32 v44, v44
v_exp_f32 v45, v45
v_cvt_pk_bf16_f32 v13, v42, v43
v_exp_f32 v46, v46
v_exp_f32 v47, v47
v_cvt_pk_bf16_f32 v14, v44, v45
v_exp_f32 v48, v48
v_cvt_pk_bf16_f32 v15, v46, v47
v_exp_f32 v49, v49
v_exp_f32 v50, v50
v_mfma_f32_32x32x16_bf16 a[128:143], v[188:191], v[12:15], a[128:143]
v_exp_f32 v51, v51
v_cvt_pk_bf16_f32 v8, v48, v49
s_add_u32 s87, s75, 131072
buffer_load_dwordx4 v[72:75], v3, s[20:23], s87 offen offset:0 sc1
v_exp_f32 v52, v52
v_exp_f32 v53, v53
v_cvt_pk_bf16_f32 v9, v50, v51
v_exp_f32 v54, v54
)ASM"
R"ASM(v_mfma_f32_32x32x16_bf16 v[32:47], v[172:175], a[200:203], 0
v_exp_f32 v55, v55
v_cvt_pk_bf16_f32 v10, v52, v53
v_exp_f32 v56, v56
v_exp_f32 v57, v57
v_cvt_pk_bf16_f32 v11, v54, v55
v_exp_f32 v58, v58
v_exp_f32 v59, v59
v_mfma_f32_32x32x16_bf16 a[144:159], v[180:183], v[8:11], a[144:159]
v_cvt_pk_bf16_f32 v12, v56, v57
v_exp_f32 v60, v60
v_exp_f32 v61, v61
v_cvt_pk_bf16_f32 v13, v58, v59
v_exp_f32 v62, v62
v_exp_f32 v63, v63
v_cvt_pk_bf16_f32 v14, v60, v61
v_exp_f32 v32, v32
v_cvt_pk_bf16_f32 v15, v62, v63
v_exp_f32 v33, v33
v_exp_f32 v34, v34
v_mfma_f32_32x32x16_bf16 a[144:159], v[188:191], v[12:15], a[144:159]
v_exp_f32 v35, v35
v_cvt_pk_bf16_f32 v8, v32, v33
buffer_load_dwordx4 a[4:7], v3, s[20:23], s81 offen offset:1024 sc1
v_exp_f32 v36, v36
v_exp_f32 v37, v37
v_cvt_pk_bf16_f32 v9, v34, v35
v_exp_f32 v38, v38
v_mfma_f32_32x32x16_bf16 v[48:63], v[172:175], a[204:207], 0
v_exp_f32 v39, v39
v_cvt_pk_bf16_f32 v10, v36, v37
v_exp_f32 v40, v40
)ASM"
R"ASM(v_exp_f32 v41, v41
v_cvt_pk_bf16_f32 v11, v38, v39
v_exp_f32 v42, v42
v_exp_f32 v43, v43
v_mfma_f32_32x32x16_bf16 a[160:175], v[180:183], v[8:11], a[160:175]
v_cvt_pk_bf16_f32 v12, v40, v41
v_exp_f32 v44, v44
v_exp_f32 v45, v45
v_cvt_pk_bf16_f32 v13, v42, v43
v_exp_f32 v46, v46
v_exp_f32 v47, v47
v_cvt_pk_bf16_f32 v14, v44, v45
v_exp_f32 v48, v48
v_cvt_pk_bf16_f32 v15, v46, v47
v_exp_f32 v49, v49
v_exp_f32 v50, v50
v_mfma_f32_32x32x16_bf16 a[160:175], v[188:191], v[12:15], a[160:175]
v_exp_f32 v51, v51
v_cvt_pk_bf16_f32 v8, v48, v49
buffer_load_dwordx4 v[76:79], v3, s[20:23], s86 offen offset:2048 sc1
v_exp_f32 v52, v52
v_exp_f32 v53, v53
v_cvt_pk_bf16_f32 v9, v50, v51
v_exp_f32 v54, v54
s_waitcnt lgkmcnt(0)
v_mfma_f32_32x32x16_bf16 v[32:47], v[64:67], a[192:195], 0
v_exp_f32 v55, v55
v_cvt_pk_bf16_f32 v10, v52, v53
v_exp_f32 v56, v56
v_exp_f32 v57, v57
v_cvt_pk_bf16_f32 v11, v54, v55
v_exp_f32 v58, v58
)ASM"
R"ASM(v_exp_f32 v59, v59
v_mfma_f32_32x32x16_bf16 a[176:191], v[180:183], v[8:11], a[176:191]
v_cvt_pk_bf16_f32 v12, v56, v57
v_exp_f32 v60, v60
v_exp_f32 v61, v61
v_cvt_pk_bf16_f32 v13, v58, v59
v_exp_f32 v62, v62
v_exp_f32 v63, v63
v_cvt_pk_bf16_f32 v14, v60, v61
v_exp_f32 v32, v32
v_cvt_pk_bf16_f32 v15, v62, v63
v_exp_f32 v33, v33
v_exp_f32 v34, v34
v_mfma_f32_32x32x16_bf16 a[176:191], v[188:191], v[12:15], a[176:191]
v_exp_f32 v35, v35
ds_read_b128 v[188:191], v29 offset:15840
v_cvt_pk_bf16_f32 v8, v32, v33
buffer_load_dwordx4 a[68:71], v3, s[20:23], s82 offen offset:1024 sc1
v_exp_f32 v36, v36
v_exp_f32 v37, v37
ds_read_b128 v[180:183], v28 offset:15840
v_cvt_pk_bf16_f32 v9, v34, v35
v_exp_f32 v38, v38
v_mfma_f32_32x32x16_bf16 v[48:63], v[64:67], a[196:199], 0
v_exp_f32 v39, v39
v_cvt_pk_bf16_f32 v10, v36, v37
ds_read_b128 v[172:175], v27 offset:15360
v_exp_f32 v40, v40
v_exp_f32 v41, v41
v_cvt_pk_bf16_f32 v11, v38, v39
v_exp_f32 v42, v42
v_exp_f32 v43, v43
)ASM"
R"ASM(v_mfma_f32_32x32x16_bf16 a[128:143], v[176:179], v[8:11], a[128:143]
v_cvt_pk_bf16_f32 v12, v40, v41
v_exp_f32 v44, v44
v_exp_f32 v45, v45
v_cvt_pk_bf16_f32 v13, v42, v43
v_exp_f32 v46, v46
v_exp_f32 v47, v47
v_cvt_pk_bf16_f32 v14, v44, v45
v_exp_f32 v48, v48
v_cvt_pk_bf16_f32 v15, v46, v47
v_exp_f32 v49, v49
v_exp_f32 v50, v50
v_mfma_f32_32x32x16_bf16 a[128:143], v[184:187], v[12:15], a[128:143]
v_exp_f32 v51, v51
v_cvt_pk_bf16_f32 v8, v48, v49
buffer_load_dwordx4 v[80:83], v3, s[20:23], s87 offen offset:2048 sc1
v_exp_f32 v52, v52
v_exp_f32 v53, v53
v_cvt_pk_bf16_f32 v9, v50, v51
v_exp_f32 v54, v54
v_mfma_f32_32x32x16_bf16 v[32:47], v[64:67], a[200:203], 0
v_exp_f32 v55, v55
v_cvt_pk_bf16_f32 v10, v52, v53
v_exp_f32 v56, v56
v_exp_f32 v57, v57
v_cvt_pk_bf16_f32 v11, v54, v55
v_exp_f32 v58, v58
v_exp_f32 v59, v59
v_mfma_f32_32x32x16_bf16 a[144:159], v[176:179], v[8:11], a[144:159]
v_cvt_pk_bf16_f32 v12, v56, v57
v_exp_f32 v60, v60
v_exp_f32 v61, v61
)ASM"
R"ASM(v_cvt_pk_bf16_f32 v13, v58, v59
v_exp_f32 v62, v62
v_exp_f32 v63, v63
v_cvt_pk_bf16_f32 v14, v60, v61
v_exp_f32 v32, v32
v_cvt_pk_bf16_f32 v15, v62, v63
v_exp_f32 v33, v33
v_exp_f32 v34, v34
v_mfma_f32_32x32x16_bf16 a[144:159], v[184:187], v[12:15], a[144:159]
v_exp_f32 v35, v35
v_cvt_pk_bf16_f32 v8, v32, v33
buffer_load_dwordx4 a[8:11], v3, s[20:23], s81 offen offset:2048 sc1
v_exp_f32 v36, v36
v_exp_f32 v37, v37
v_cvt_pk_bf16_f32 v9, v34, v35
v_exp_f32 v38, v38
v_mfma_f32_32x32x16_bf16 v[48:63], v[64:67], a[204:207], 0
v_exp_f32 v39, v39
v_cvt_pk_bf16_f32 v10, v36, v37
v_exp_f32 v40, v40
v_exp_f32 v41, v41
v_cvt_pk_bf16_f32 v11, v38, v39
v_exp_f32 v42, v42
v_exp_f32 v43, v43
v_mfma_f32_32x32x16_bf16 a[160:175], v[176:179], v[8:11], a[160:175]
v_cvt_pk_bf16_f32 v12, v40, v41
v_exp_f32 v44, v44
v_exp_f32 v45, v45
v_cvt_pk_bf16_f32 v13, v42, v43
v_exp_f32 v46, v46
v_exp_f32 v47, v47
v_cvt_pk_bf16_f32 v14, v44, v45
)ASM"
R"ASM(v_exp_f32 v48, v48
v_cvt_pk_bf16_f32 v15, v46, v47
v_exp_f32 v49, v49
v_exp_f32 v50, v50
v_mfma_f32_32x32x16_bf16 a[160:175], v[184:187], v[12:15], a[160:175]
v_exp_f32 v51, v51
v_cvt_pk_bf16_f32 v8, v48, v49
s_add_u32 s86, s74, 135168
buffer_load_dwordx4 v[84:87], v3, s[20:23], s86 offen offset:0 sc1
v_exp_f32 v52, v52
v_exp_f32 v53, v53
v_cvt_pk_bf16_f32 v9, v50, v51
v_exp_f32 v54, v54
s_waitcnt lgkmcnt(0)
v_mfma_f32_32x32x16_bf16 v[32:47], v[172:175], a[192:195], 0
v_exp_f32 v55, v55
v_cvt_pk_bf16_f32 v10, v52, v53
v_exp_f32 v56, v56
v_exp_f32 v57, v57
v_cvt_pk_bf16_f32 v11, v54, v55
v_exp_f32 v58, v58
v_exp_f32 v59, v59
v_mfma_f32_32x32x16_bf16 a[176:191], v[176:179], v[8:11], a[176:191]
v_cvt_pk_bf16_f32 v12, v56, v57
v_exp_f32 v60, v60
v_exp_f32 v61, v61
v_cvt_pk_bf16_f32 v13, v58, v59
v_exp_f32 v62, v62
v_exp_f32 v63, v63
v_cvt_pk_bf16_f32 v14, v60, v61
v_exp_f32 v32, v32
v_cvt_pk_bf16_f32 v15, v62, v63
)ASM"
R"ASM(v_exp_f32 v33, v33
v_exp_f32 v34, v34
v_mfma_f32_32x32x16_bf16 a[176:191], v[184:187], v[12:15], a[176:191]
v_exp_f32 v35, v35
ds_read_b128 v[184:187], v29 offset:16896
v_cvt_pk_bf16_f32 v8, v32, v33
buffer_load_dwordx4 a[72:75], v3, s[20:23], s82 offen offset:2048 sc1
v_exp_f32 v36, v36
v_exp_f32 v37, v37
ds_read_b128 v[176:179], v28 offset:16896
v_cvt_pk_bf16_f32 v9, v34, v35
v_exp_f32 v38, v38
v_mfma_f32_32x32x16_bf16 v[48:63], v[172:175], a[196:199], 0
v_exp_f32 v39, v39
v_cvt_pk_bf16_f32 v10, v36, v37
ds_read_b128 v[64:67], v27 offset:16384
v_exp_f32 v40, v40
v_exp_f32 v41, v41
v_cvt_pk_bf16_f32 v11, v38, v39
v_exp_f32 v42, v42
v_exp_f32 v43, v43
v_mfma_f32_32x32x16_bf16 a[128:143], v[180:183], v[8:11], a[128:143]
v_cvt_pk_bf16_f32 v12, v40, v41
v_exp_f32 v44, v44
v_exp_f32 v45, v45
v_cvt_pk_bf16_f32 v13, v42, v43
v_exp_f32 v46, v46
v_exp_f32 v47, v47
v_cvt_pk_bf16_f32 v14, v44, v45
v_exp_f32 v48, v48
v_cvt_pk_bf16_f32 v15, v46, v47
v_exp_f32 v49, v49
)ASM"
R"ASM(v_exp_f32 v50, v50
v_mfma_f32_32x32x16_bf16 a[128:143], v[188:191], v[12:15], a[128:143]
v_exp_f32 v51, v51
v_cvt_pk_bf16_f32 v8, v48, v49
s_add_u32 s87, s75, 135168
buffer_load_dwordx4 v[88:91], v3, s[20:23], s87 offen offset:0 sc1
v_exp_f32 v52, v52
v_exp_f32 v53, v53
v_cvt_pk_bf16_f32 v9, v50, v51
v_exp_f32 v54, v54
v_mfma_f32_32x32x16_bf16 v[32:47], v[172:175], a[200:203], 0
v_exp_f32 v55, v55
v_cvt_pk_bf16_f32 v10, v52, v53
v_exp_f32 v56, v56
v_exp_f32 v57, v57
v_cvt_pk_bf16_f32 v11, v54, v55
v_exp_f32 v58, v58
v_exp_f32 v59, v59
v_mfma_f32_32x32x16_bf16 a[144:159], v[180:183], v[8:11], a[144:159]
v_cvt_pk_bf16_f32 v12, v56, v57
v_exp_f32 v60, v60
v_exp_f32 v61, v61
v_cvt_pk_bf16_f32 v13, v58, v59
v_exp_f32 v62, v62
v_exp_f32 v63, v63
v_cvt_pk_bf16_f32 v14, v60, v61
v_exp_f32 v32, v32
v_cvt_pk_bf16_f32 v15, v62, v63
v_exp_f32 v33, v33
v_exp_f32 v34, v34
v_mfma_f32_32x32x16_bf16 a[144:159], v[188:191], v[12:15], a[144:159]
v_exp_f32 v35, v35
)ASM"
R"ASM(v_cvt_pk_bf16_f32 v8, v32, v33
buffer_load_dwordx4 a[12:15], v3, s[20:23], s81 offen offset:3072 sc1
v_exp_f32 v36, v36
v_exp_f32 v37, v37
v_cvt_pk_bf16_f32 v9, v34, v35
v_exp_f32 v38, v38
v_mfma_f32_32x32x16_bf16 v[48:63], v[172:175], a[204:207], 0
v_exp_f32 v39, v39
v_cvt_pk_bf16_f32 v10, v36, v37
v_exp_f32 v40, v40
v_exp_f32 v41, v41
v_cvt_pk_bf16_f32 v11, v38, v39
v_exp_f32 v42, v42
v_exp_f32 v43, v43
v_mfma_f32_32x32x16_bf16 a[160:175], v[180:183], v[8:11], a[160:175]
v_cvt_pk_bf16_f32 v12, v40, v41
v_exp_f32 v44, v44
v_exp_f32 v45, v45
v_cvt_pk_bf16_f32 v13, v42, v43
v_exp_f32 v46, v46
v_exp_f32 v47, v47
v_cvt_pk_bf16_f32 v14, v44, v45
v_exp_f32 v48, v48
v_cvt_pk_bf16_f32 v15, v46, v47
v_exp_f32 v49, v49
v_exp_f32 v50, v50
v_mfma_f32_32x32x16_bf16 a[160:175], v[188:191], v[12:15], a[160:175]
v_exp_f32 v51, v51
v_cvt_pk_bf16_f32 v8, v48, v49
buffer_load_dwordx4 v[92:95], v3, s[20:23], s86 offen offset:2048 sc1
v_exp_f32 v52, v52
v_exp_f32 v53, v53
)ASM"
R"ASM(v_cvt_pk_bf16_f32 v9, v50, v51
v_exp_f32 v54, v54
s_waitcnt lgkmcnt(0)
v_mfma_f32_32x32x16_bf16 v[32:47], v[64:67], a[192:195], 0
v_exp_f32 v55, v55
v_cvt_pk_bf16_f32 v10, v52, v53
v_exp_f32 v56, v56
v_exp_f32 v57, v57
v_cvt_pk_bf16_f32 v11, v54, v55
v_exp_f32 v58, v58
v_exp_f32 v59, v59
v_mfma_f32_32x32x16_bf16 a[176:191], v[180:183], v[8:11], a[176:191]
v_cvt_pk_bf16_f32 v12, v56, v57
v_exp_f32 v60, v60
v_exp_f32 v61, v61
v_cvt_pk_bf16_f32 v13, v58, v59
v_exp_f32 v62, v62
v_exp_f32 v63, v63
v_cvt_pk_bf16_f32 v14, v60, v61
v_exp_f32 v32, v32
v_cvt_pk_bf16_f32 v15, v62, v63
v_exp_f32 v33, v33
v_exp_f32 v34, v34
v_mfma_f32_32x32x16_bf16 a[176:191], v[188:191], v[12:15], a[176:191]
v_exp_f32 v35, v35
ds_read_b128 v[188:191], v29 offset:17952
v_cvt_pk_bf16_f32 v8, v32, v33
buffer_load_dwordx4 a[76:79], v3, s[20:23], s82 offen offset:3072 sc1
v_exp_f32 v36, v36
v_exp_f32 v37, v37
ds_read_b128 v[180:183], v28 offset:17952
v_cvt_pk_bf16_f32 v9, v34, v35
)ASM"
R"ASM(v_exp_f32 v38, v38
v_mfma_f32_32x32x16_bf16 v[48:63], v[64:67], a[196:199], 0
v_exp_f32 v39, v39
v_cvt_pk_bf16_f32 v10, v36, v37
ds_read_b128 v[172:175], v27 offset:17408
v_exp_f32 v40, v40
v_exp_f32 v41, v41
v_cvt_pk_bf16_f32 v11, v38, v39
v_exp_f32 v42, v42
v_exp_f32 v43, v43
v_mfma_f32_32x32x16_bf16 a[128:143], v[176:179], v[8:11], a[128:143]
v_cvt_pk_bf16_f32 v12, v40, v41
v_exp_f32 v44, v44
v_exp_f32 v45, v45
v_cvt_pk_bf16_f32 v13, v42, v43
v_exp_f32 v46, v46
v_exp_f32 v47, v47
v_cvt_pk_bf16_f32 v14, v44, v45
v_exp_f32 v48, v48
v_cvt_pk_bf16_f32 v15, v46, v47
v_exp_f32 v49, v49
v_exp_f32 v50, v50
v_mfma_f32_32x32x16_bf16 a[128:143], v[184:187], v[12:15], a[128:143]
v_exp_f32 v51, v51
v_cvt_pk_bf16_f32 v8, v48, v49
buffer_load_dwordx4 v[96:99], v3, s[20:23], s87 offen offset:2048 sc1
v_exp_f32 v52, v52
v_exp_f32 v53, v53
v_cvt_pk_bf16_f32 v9, v50, v51
v_exp_f32 v54, v54
v_mfma_f32_32x32x16_bf16 v[32:47], v[64:67], a[200:203], 0
v_exp_f32 v55, v55
)ASM"
R"ASM(v_cvt_pk_bf16_f32 v10, v52, v53
v_exp_f32 v56, v56
v_exp_f32 v57, v57
v_cvt_pk_bf16_f32 v11, v54, v55
v_exp_f32 v58, v58
v_exp_f32 v59, v59
v_mfma_f32_32x32x16_bf16 a[144:159], v[176:179], v[8:11], a[144:159]
v_cvt_pk_bf16_f32 v12, v56, v57
v_exp_f32 v60, v60
v_exp_f32 v61, v61
v_cvt_pk_bf16_f32 v13, v58, v59
v_exp_f32 v62, v62
v_exp_f32 v63, v63
v_cvt_pk_bf16_f32 v14, v60, v61
v_exp_f32 v32, v32
v_cvt_pk_bf16_f32 v15, v62, v63
v_exp_f32 v33, v33
v_exp_f32 v34, v34
v_mfma_f32_32x32x16_bf16 a[144:159], v[184:187], v[12:15], a[144:159]
v_exp_f32 v35, v35
v_cvt_pk_bf16_f32 v8, v32, v33
s_add_u32 s81, s81, 4096
buffer_load_dwordx4 a[16:19], v3, s[20:23], s81 offen offset:0 sc1
v_exp_f32 v36, v36
v_exp_f32 v37, v37
v_cvt_pk_bf16_f32 v9, v34, v35
v_exp_f32 v38, v38
v_mfma_f32_32x32x16_bf16 v[48:63], v[64:67], a[204:207], 0
v_exp_f32 v39, v39
v_cvt_pk_bf16_f32 v10, v36, v37
v_exp_f32 v40, v40
v_exp_f32 v41, v41
)ASM"
R"ASM(v_cvt_pk_bf16_f32 v11, v38, v39
v_exp_f32 v42, v42
v_exp_f32 v43, v43
v_mfma_f32_32x32x16_bf16 a[160:175], v[176:179], v[8:11], a[160:175]
v_cvt_pk_bf16_f32 v12, v40, v41
v_exp_f32 v44, v44
v_exp_f32 v45, v45
v_cvt_pk_bf16_f32 v13, v42, v43
v_exp_f32 v46, v46
v_exp_f32 v47, v47
v_cvt_pk_bf16_f32 v14, v44, v45
v_exp_f32 v48, v48
v_cvt_pk_bf16_f32 v15, v46, v47
v_exp_f32 v49, v49
v_exp_f32 v50, v50
v_mfma_f32_32x32x16_bf16 a[160:175], v[184:187], v[12:15], a[160:175]
v_exp_f32 v51, v51
v_cvt_pk_bf16_f32 v8, v48, v49
s_add_u32 s86, s74, 163840
buffer_load_dwordx4 v[100:103], v3, s[20:23], s86 offen offset:0 sc1
v_exp_f32 v52, v52
v_exp_f32 v53, v53
v_cvt_pk_bf16_f32 v9, v50, v51
v_exp_f32 v54, v54
s_waitcnt lgkmcnt(0)
v_mfma_f32_32x32x16_bf16 v[32:47], v[172:175], a[192:195], 0
v_exp_f32 v55, v55
v_cvt_pk_bf16_f32 v10, v52, v53
v_exp_f32 v56, v56
v_exp_f32 v57, v57
v_cvt_pk_bf16_f32 v11, v54, v55
v_exp_f32 v58, v58
)ASM"
R"ASM(v_exp_f32 v59, v59
v_mfma_f32_32x32x16_bf16 a[176:191], v[176:179], v[8:11], a[176:191]
v_cvt_pk_bf16_f32 v12, v56, v57
v_exp_f32 v60, v60
v_exp_f32 v61, v61
v_cvt_pk_bf16_f32 v13, v58, v59
v_exp_f32 v62, v62
v_exp_f32 v63, v63
v_cvt_pk_bf16_f32 v14, v60, v61
v_exp_f32 v32, v32
v_cvt_pk_bf16_f32 v15, v62, v63
v_exp_f32 v33, v33
v_exp_f32 v34, v34
v_mfma_f32_32x32x16_bf16 a[176:191], v[184:187], v[12:15], a[176:191]
v_exp_f32 v35, v35
ds_read_b128 v[184:187], v29 offset:19008
v_cvt_pk_bf16_f32 v8, v32, v33
s_add_u32 s82, s82, 4096
buffer_load_dwordx4 a[80:83], v3, s[20:23], s82 offen offset:0 sc1
v_exp_f32 v36, v36
v_exp_f32 v37, v37
ds_read_b128 v[176:179], v28 offset:19008
v_cvt_pk_bf16_f32 v9, v34, v35
v_exp_f32 v38, v38
v_mfma_f32_32x32x16_bf16 v[48:63], v[172:175], a[196:199], 0
v_exp_f32 v39, v39
v_cvt_pk_bf16_f32 v10, v36, v37
ds_read_b128 v[64:67], v27 offset:18432
v_exp_f32 v40, v40
v_exp_f32 v41, v41
v_cvt_pk_bf16_f32 v11, v38, v39
v_exp_f32 v42, v42
)ASM"
R"ASM(v_exp_f32 v43, v43
v_mfma_f32_32x32x16_bf16 a[128:143], v[180:183], v[8:11], a[128:143]
v_cvt_pk_bf16_f32 v12, v40, v41
v_exp_f32 v44, v44
v_exp_f32 v45, v45
v_cvt_pk_bf16_f32 v13, v42, v43
v_exp_f32 v46, v46
v_exp_f32 v47, v47
v_cvt_pk_bf16_f32 v14, v44, v45
v_exp_f32 v48, v48
v_cvt_pk_bf16_f32 v15, v46, v47
v_exp_f32 v49, v49
v_exp_f32 v50, v50
v_mfma_f32_32x32x16_bf16 a[128:143], v[188:191], v[12:15], a[128:143]
v_exp_f32 v51, v51
v_cvt_pk_bf16_f32 v8, v48, v49
s_add_u32 s87, s75, 163840
buffer_load_dwordx4 v[104:107], v3, s[20:23], s87 offen offset:0 sc1
v_exp_f32 v52, v52
v_exp_f32 v53, v53
v_cvt_pk_bf16_f32 v9, v50, v51
v_exp_f32 v54, v54
v_mfma_f32_32x32x16_bf16 v[32:47], v[172:175], a[200:203], 0
v_exp_f32 v55, v55
v_cvt_pk_bf16_f32 v10, v52, v53
v_exp_f32 v56, v56
v_exp_f32 v57, v57
v_cvt_pk_bf16_f32 v11, v54, v55
v_exp_f32 v58, v58
v_exp_f32 v59, v59
v_mfma_f32_32x32x16_bf16 a[144:159], v[180:183], v[8:11], a[144:159]
v_cvt_pk_bf16_f32 v12, v56, v57
)ASM"
R"ASM(v_exp_f32 v60, v60
v_exp_f32 v61, v61
v_cvt_pk_bf16_f32 v13, v58, v59
v_exp_f32 v62, v62
v_exp_f32 v63, v63
v_cvt_pk_bf16_f32 v14, v60, v61
v_exp_f32 v32, v32
v_cvt_pk_bf16_f32 v15, v62, v63
v_exp_f32 v33, v33
v_exp_f32 v34, v34
v_mfma_f32_32x32x16_bf16 a[144:159], v[188:191], v[12:15], a[144:159]
v_exp_f32 v35, v35
v_cvt_pk_bf16_f32 v8, v32, v33
buffer_load_dwordx4 a[20:23], v3, s[20:23], s81 offen offset:1024 sc1
v_exp_f32 v36, v36
v_exp_f32 v37, v37
v_cvt_pk_bf16_f32 v9, v34, v35
v_exp_f32 v38, v38
v_mfma_f32_32x32x16_bf16 v[48:63], v[172:175], a[204:207], 0
v_exp_f32 v39, v39
v_cvt_pk_bf16_f32 v10, v36, v37
v_exp_f32 v40, v40
v_exp_f32 v41, v41
v_cvt_pk_bf16_f32 v11, v38, v39
v_exp_f32 v42, v42
v_exp_f32 v43, v43
v_mfma_f32_32x32x16_bf16 a[160:175], v[180:183], v[8:11], a[160:175]
v_cvt_pk_bf16_f32 v12, v40, v41
v_exp_f32 v44, v44
v_exp_f32 v45, v45
v_cvt_pk_bf16_f32 v13, v42, v43
v_exp_f32 v46, v46
)ASM"
R"ASM(v_exp_f32 v47, v47
v_cvt_pk_bf16_f32 v14, v44, v45
v_exp_f32 v48, v48
v_cvt_pk_bf16_f32 v15, v46, v47
v_exp_f32 v49, v49
v_exp_f32 v50, v50
v_mfma_f32_32x32x16_bf16 a[160:175], v[188:191], v[12:15], a[160:175]
v_exp_f32 v51, v51
v_cvt_pk_bf16_f32 v8, v48, v49
buffer_load_dwordx4 v[108:111], v3, s[20:23], s86 offen offset:2048 sc1
v_exp_f32 v52, v52
v_exp_f32 v53, v53
v_cvt_pk_bf16_f32 v9, v50, v51
v_exp_f32 v54, v54
s_waitcnt lgkmcnt(0)
v_mfma_f32_32x32x16_bf16 v[32:47], v[64:67], a[192:195], 0
v_exp_f32 v55, v55
v_cvt_pk_bf16_f32 v10, v52, v53
v_exp_f32 v56, v56
v_exp_f32 v57, v57
v_cvt_pk_bf16_f32 v11, v54, v55
v_exp_f32 v58, v58
v_exp_f32 v59, v59
v_mfma_f32_32x32x16_bf16 a[176:191], v[180:183], v[8:11], a[176:191]
v_cvt_pk_bf16_f32 v12, v56, v57
v_exp_f32 v60, v60
v_exp_f32 v61, v61
v_cvt_pk_bf16_f32 v13, v58, v59
v_exp_f32 v62, v62
v_exp_f32 v63, v63
v_cvt_pk_bf16_f32 v14, v60, v61
v_exp_f32 v32, v32
)ASM"
R"ASM(v_cvt_pk_bf16_f32 v15, v62, v63
v_exp_f32 v33, v33
v_exp_f32 v34, v34
v_mfma_f32_32x32x16_bf16 a[176:191], v[188:191], v[12:15], a[176:191]
v_exp_f32 v35, v35
ds_read_b128 v[188:191], v29 offset:20064
v_cvt_pk_bf16_f32 v8, v32, v33
buffer_load_dwordx4 a[84:87], v3, s[20:23], s82 offen offset:1024 sc1
v_exp_f32 v36, v36
v_exp_f32 v37, v37
ds_read_b128 v[180:183], v28 offset:20064
v_cvt_pk_bf16_f32 v9, v34, v35
v_exp_f32 v38, v38
v_mfma_f32_32x32x16_bf16 v[48:63], v[64:67], a[196:199], 0
v_exp_f32 v39, v39
v_cvt_pk_bf16_f32 v10, v36, v37
ds_read_b128 v[172:175], v27 offset:19456
v_exp_f32 v40, v40
v_exp_f32 v41, v41
v_cvt_pk_bf16_f32 v11, v38, v39
v_exp_f32 v42, v42
v_exp_f32 v43, v43
v_mfma_f32_32x32x16_bf16 a[128:143], v[176:179], v[8:11], a[128:143]
v_cvt_pk_bf16_f32 v12, v40, v41
v_exp_f32 v44, v44
v_exp_f32 v45, v45
v_cvt_pk_bf16_f32 v13, v42, v43
v_exp_f32 v46, v46
v_exp_f32 v47, v47
v_cvt_pk_bf16_f32 v14, v44, v45
v_exp_f32 v48, v48
v_cvt_pk_bf16_f32 v15, v46, v47
)ASM"
R"ASM(v_exp_f32 v49, v49
v_exp_f32 v50, v50
v_mfma_f32_32x32x16_bf16 a[128:143], v[184:187], v[12:15], a[128:143]
v_exp_f32 v51, v51
v_cvt_pk_bf16_f32 v8, v48, v49
buffer_load_dwordx4 v[112:115], v3, s[20:23], s87 offen offset:2048 sc1
v_exp_f32 v52, v52
v_exp_f32 v53, v53
v_cvt_pk_bf16_f32 v9, v50, v51
v_exp_f32 v54, v54
v_mfma_f32_32x32x16_bf16 v[32:47], v[64:67], a[200:203], 0
v_exp_f32 v55, v55
v_cvt_pk_bf16_f32 v10, v52, v53
v_exp_f32 v56, v56
v_exp_f32 v57, v57
v_cvt_pk_bf16_f32 v11, v54, v55
v_exp_f32 v58, v58
v_exp_f32 v59, v59
v_mfma_f32_32x32x16_bf16 a[144:159], v[176:179], v[8:11], a[144:159]
v_cvt_pk_bf16_f32 v12, v56, v57
v_exp_f32 v60, v60
v_exp_f32 v61, v61
v_cvt_pk_bf16_f32 v13, v58, v59
v_exp_f32 v62, v62
v_exp_f32 v63, v63
v_cvt_pk_bf16_f32 v14, v60, v61
v_exp_f32 v32, v32
v_cvt_pk_bf16_f32 v15, v62, v63
v_exp_f32 v33, v33
v_exp_f32 v34, v34
v_mfma_f32_32x32x16_bf16 a[144:159], v[184:187], v[12:15], a[144:159]
v_exp_f32 v35, v35
)ASM"
R"ASM(v_cvt_pk_bf16_f32 v8, v32, v33
buffer_load_dwordx4 a[24:27], v3, s[20:23], s81 offen offset:2048 sc1
v_exp_f32 v36, v36
v_exp_f32 v37, v37
v_cvt_pk_bf16_f32 v9, v34, v35
v_exp_f32 v38, v38
v_mfma_f32_32x32x16_bf16 v[48:63], v[64:67], a[204:207], 0
v_exp_f32 v39, v39
v_cvt_pk_bf16_f32 v10, v36, v37
v_exp_f32 v40, v40
v_exp_f32 v41, v41
v_cvt_pk_bf16_f32 v11, v38, v39
v_exp_f32 v42, v42
v_exp_f32 v43, v43
v_mfma_f32_32x32x16_bf16 a[160:175], v[176:179], v[8:11], a[160:175]
v_cvt_pk_bf16_f32 v12, v40, v41
v_exp_f32 v44, v44
v_exp_f32 v45, v45
v_cvt_pk_bf16_f32 v13, v42, v43
v_exp_f32 v46, v46
v_exp_f32 v47, v47
v_cvt_pk_bf16_f32 v14, v44, v45
v_exp_f32 v48, v48
v_cvt_pk_bf16_f32 v15, v46, v47
v_exp_f32 v49, v49
v_exp_f32 v50, v50
v_mfma_f32_32x32x16_bf16 a[160:175], v[184:187], v[12:15], a[160:175]
v_exp_f32 v51, v51
v_cvt_pk_bf16_f32 v8, v48, v49
s_add_u32 s86, s74, 167936
buffer_load_dwordx4 v[116:119], v3, s[20:23], s86 offen offset:0 sc1
v_exp_f32 v52, v52
)ASM"
R"ASM(v_exp_f32 v53, v53
v_cvt_pk_bf16_f32 v9, v50, v51
v_exp_f32 v54, v54
s_waitcnt lgkmcnt(0)
v_mfma_f32_32x32x16_bf16 v[32:47], v[172:175], a[192:195], 0
v_exp_f32 v55, v55
v_cvt_pk_bf16_f32 v10, v52, v53
v_exp_f32 v56, v56
v_exp_f32 v57, v57
v_cvt_pk_bf16_f32 v11, v54, v55
v_exp_f32 v58, v58
v_exp_f32 v59, v59
v_mfma_f32_32x32x16_bf16 a[176:191], v[176:179], v[8:11], a[176:191]
v_cvt_pk_bf16_f32 v12, v56, v57
v_exp_f32 v60, v60
v_exp_f32 v61, v61
v_cvt_pk_bf16_f32 v13, v58, v59
v_exp_f32 v62, v62
v_exp_f32 v63, v63
v_cvt_pk_bf16_f32 v14, v60, v61
v_exp_f32 v32, v32
v_cvt_pk_bf16_f32 v15, v62, v63
v_exp_f32 v33, v33
v_exp_f32 v34, v34
v_mfma_f32_32x32x16_bf16 a[176:191], v[184:187], v[12:15], a[176:191]
v_exp_f32 v35, v35
ds_read_b128 v[184:187], v29 offset:21120
v_cvt_pk_bf16_f32 v8, v32, v33
buffer_load_dwordx4 a[88:91], v3, s[20:23], s82 offen offset:2048 sc1
v_exp_f32 v36, v36
v_exp_f32 v37, v37
ds_read_b128 v[176:179], v28 offset:21120
)ASM"
R"ASM(v_cvt_pk_bf16_f32 v9, v34, v35
v_exp_f32 v38, v38
v_mfma_f32_32x32x16_bf16 v[48:63], v[172:175], a[196:199], 0
v_exp_f32 v39, v39
v_cvt_pk_bf16_f32 v10, v36, v37
ds_read_b128 v[64:67], v27 offset:20480
v_exp_f32 v40, v40
v_exp_f32 v41, v41
v_cvt_pk_bf16_f32 v11, v38, v39
v_exp_f32 v42, v42
v_exp_f32 v43, v43
v_mfma_f32_32x32x16_bf16 a[128:143], v[180:183], v[8:11], a[128:143]
v_cvt_pk_bf16_f32 v12, v40, v41
v_exp_f32 v44, v44
v_exp_f32 v45, v45
v_cvt_pk_bf16_f32 v13, v42, v43
v_exp_f32 v46, v46
v_exp_f32 v47, v47
v_cvt_pk_bf16_f32 v14, v44, v45
v_exp_f32 v48, v48
v_cvt_pk_bf16_f32 v15, v46, v47
v_exp_f32 v49, v49
v_exp_f32 v50, v50
v_mfma_f32_32x32x16_bf16 a[128:143], v[188:191], v[12:15], a[128:143]
v_exp_f32 v51, v51
v_cvt_pk_bf16_f32 v8, v48, v49
s_add_u32 s87, s75, 167936
buffer_load_dwordx4 v[120:123], v3, s[20:23], s87 offen offset:0 sc1
v_exp_f32 v52, v52
v_exp_f32 v53, v53
v_cvt_pk_bf16_f32 v9, v50, v51
v_exp_f32 v54, v54
)ASM"
R"ASM(v_mfma_f32_32x32x16_bf16 v[32:47], v[172:175], a[200:203], 0
v_exp_f32 v55, v55
v_cvt_pk_bf16_f32 v10, v52, v53
v_exp_f32 v56, v56
v_exp_f32 v57, v57
v_cvt_pk_bf16_f32 v11, v54, v55
v_exp_f32 v58, v58
v_exp_f32 v59, v59
v_mfma_f32_32x32x16_bf16 a[144:159], v[180:183], v[8:11], a[144:159]
v_cvt_pk_bf16_f32 v12, v56, v57
v_exp_f32 v60, v60
v_exp_f32 v61, v61
v_cvt_pk_bf16_f32 v13, v58, v59
v_exp_f32 v62, v62
v_exp_f32 v63, v63
v_cvt_pk_bf16_f32 v14, v60, v61
v_exp_f32 v32, v32
v_cvt_pk_bf16_f32 v15, v62, v63
v_exp_f32 v33, v33
v_exp_f32 v34, v34
v_mfma_f32_32x32x16_bf16 a[144:159], v[188:191], v[12:15], a[144:159]
v_exp_f32 v35, v35
v_cvt_pk_bf16_f32 v8, v32, v33
buffer_load_dwordx4 a[28:31], v3, s[20:23], s81 offen offset:3072 sc1
v_exp_f32 v36, v36
v_exp_f32 v37, v37
v_cvt_pk_bf16_f32 v9, v34, v35
v_exp_f32 v38, v38
v_mfma_f32_32x32x16_bf16 v[48:63], v[172:175], a[204:207], 0
v_exp_f32 v39, v39
v_cvt_pk_bf16_f32 v10, v36, v37
v_exp_f32 v40, v40
)ASM"
R"ASM(v_exp_f32 v41, v41
v_cvt_pk_bf16_f32 v11, v38, v39
v_exp_f32 v42, v42
v_exp_f32 v43, v43
v_mfma_f32_32x32x16_bf16 a[160:175], v[180:183], v[8:11], a[160:175]
v_cvt_pk_bf16_f32 v12, v40, v41
v_exp_f32 v44, v44
v_exp_f32 v45, v45
v_cvt_pk_bf16_f32 v13, v42, v43
v_exp_f32 v46, v46
v_exp_f32 v47, v47
v_cvt_pk_bf16_f32 v14, v44, v45
v_exp_f32 v48, v48
v_cvt_pk_bf16_f32 v15, v46, v47
v_exp_f32 v49, v49
v_exp_f32 v50, v50
v_mfma_f32_32x32x16_bf16 a[160:175], v[188:191], v[12:15], a[160:175]
v_exp_f32 v51, v51
v_cvt_pk_bf16_f32 v8, v48, v49
buffer_load_dwordx4 v[124:127], v3, s[20:23], s86 offen offset:2048 sc1
v_exp_f32 v52, v52
v_exp_f32 v53, v53
v_cvt_pk_bf16_f32 v9, v50, v51
v_exp_f32 v54, v54
s_waitcnt lgkmcnt(0)
v_mfma_f32_32x32x16_bf16 v[32:47], v[64:67], a[192:195], 0
v_exp_f32 v55, v55
v_cvt_pk_bf16_f32 v10, v52, v53
v_exp_f32 v56, v56
v_exp_f32 v57, v57
v_cvt_pk_bf16_f32 v11, v54, v55
v_exp_f32 v58, v58
)ASM"
R"ASM(v_exp_f32 v59, v59
v_mfma_f32_32x32x16_bf16 a[176:191], v[180:183], v[8:11], a[176:191]
v_cvt_pk_bf16_f32 v12, v56, v57
v_exp_f32 v60, v60
v_exp_f32 v61, v61
v_cvt_pk_bf16_f32 v13, v58, v59
v_exp_f32 v62, v62
v_exp_f32 v63, v63
v_cvt_pk_bf16_f32 v14, v60, v61
v_exp_f32 v32, v32
v_cvt_pk_bf16_f32 v15, v62, v63
v_exp_f32 v33, v33
v_exp_f32 v34, v34
v_mfma_f32_32x32x16_bf16 a[176:191], v[188:191], v[12:15], a[176:191]
v_exp_f32 v35, v35
ds_read_b128 v[188:191], v29 offset:22176
v_cvt_pk_bf16_f32 v8, v32, v33
buffer_load_dwordx4 a[92:95], v3, s[20:23], s82 offen offset:3072 sc1
v_exp_f32 v36, v36
v_exp_f32 v37, v37
ds_read_b128 v[180:183], v28 offset:22176
v_cvt_pk_bf16_f32 v9, v34, v35
v_exp_f32 v38, v38
v_mfma_f32_32x32x16_bf16 v[48:63], v[64:67], a[196:199], 0
v_exp_f32 v39, v39
v_cvt_pk_bf16_f32 v10, v36, v37
ds_read_b128 v[172:175], v27 offset:21504
v_exp_f32 v40, v40
v_exp_f32 v41, v41
v_cvt_pk_bf16_f32 v11, v38, v39
v_exp_f32 v42, v42
v_exp_f32 v43, v43
)ASM"
R"ASM(v_mfma_f32_32x32x16_bf16 a[128:143], v[176:179], v[8:11], a[128:143]
v_cvt_pk_bf16_f32 v12, v40, v41
v_exp_f32 v44, v44
v_exp_f32 v45, v45
v_cvt_pk_bf16_f32 v13, v42, v43
v_exp_f32 v46, v46
v_exp_f32 v47, v47
v_cvt_pk_bf16_f32 v14, v44, v45
v_exp_f32 v48, v48
v_cvt_pk_bf16_f32 v15, v46, v47
v_exp_f32 v49, v49
v_exp_f32 v50, v50
v_mfma_f32_32x32x16_bf16 a[128:143], v[184:187], v[12:15], a[128:143]
v_exp_f32 v51, v51
v_cvt_pk_bf16_f32 v8, v48, v49
buffer_load_dwordx4 v[128:131], v3, s[20:23], s87 offen offset:2048 sc1
v_exp_f32 v52, v52
v_exp_f32 v53, v53
v_cvt_pk_bf16_f32 v9, v50, v51
v_exp_f32 v54, v54
v_mfma_f32_32x32x16_bf16 v[32:47], v[64:67], a[200:203], 0
v_exp_f32 v55, v55
v_cvt_pk_bf16_f32 v10, v52, v53
v_exp_f32 v56, v56
v_exp_f32 v57, v57
v_cvt_pk_bf16_f32 v11, v54, v55
v_exp_f32 v58, v58
v_exp_f32 v59, v59
v_mfma_f32_32x32x16_bf16 a[144:159], v[176:179], v[8:11], a[144:159]
v_cvt_pk_bf16_f32 v12, v56, v57
v_exp_f32 v60, v60
v_exp_f32 v61, v61
)ASM"
R"ASM(v_cvt_pk_bf16_f32 v13, v58, v59
v_exp_f32 v62, v62
v_exp_f32 v63, v63
v_cvt_pk_bf16_f32 v14, v60, v61
v_exp_f32 v32, v32
v_cvt_pk_bf16_f32 v15, v62, v63
v_exp_f32 v33, v33
v_exp_f32 v34, v34
v_mfma_f32_32x32x16_bf16 a[144:159], v[184:187], v[12:15], a[144:159]
v_exp_f32 v35, v35
v_cvt_pk_bf16_f32 v8, v32, v33
s_add_u32 s81, s81, 4096
buffer_load_dwordx4 a[32:35], v3, s[20:23], s81 offen offset:0 sc1
v_exp_f32 v36, v36
v_exp_f32 v37, v37
v_cvt_pk_bf16_f32 v9, v34, v35
v_exp_f32 v38, v38
v_mfma_f32_32x32x16_bf16 v[48:63], v[64:67], a[204:207], 0
v_exp_f32 v39, v39
v_cvt_pk_bf16_f32 v10, v36, v37
v_exp_f32 v40, v40
v_exp_f32 v41, v41
v_cvt_pk_bf16_f32 v11, v38, v39
v_exp_f32 v42, v42
v_exp_f32 v43, v43
v_mfma_f32_32x32x16_bf16 a[160:175], v[176:179], v[8:11], a[160:175]
v_cvt_pk_bf16_f32 v12, v40, v41
v_exp_f32 v44, v44
v_exp_f32 v45, v45
v_cvt_pk_bf16_f32 v13, v42, v43
v_exp_f32 v46, v46
v_exp_f32 v47, v47
)ASM"
R"ASM(v_cvt_pk_bf16_f32 v14, v44, v45
v_exp_f32 v48, v48
v_cvt_pk_bf16_f32 v15, v46, v47
v_exp_f32 v49, v49
v_exp_f32 v50, v50
v_mfma_f32_32x32x16_bf16 a[160:175], v[184:187], v[12:15], a[160:175]
v_exp_f32 v51, v51
v_cvt_pk_bf16_f32 v8, v48, v49
s_add_u32 s86, s74, 196608
buffer_load_dwordx4 v[132:135], v3, s[20:23], s86 offen offset:0 sc1
v_exp_f32 v52, v52
v_exp_f32 v53, v53
v_cvt_pk_bf16_f32 v9, v50, v51
v_exp_f32 v54, v54
s_waitcnt lgkmcnt(0)
v_mfma_f32_32x32x16_bf16 v[32:47], v[172:175], a[192:195], 0
v_exp_f32 v55, v55
v_cvt_pk_bf16_f32 v10, v52, v53
v_exp_f32 v56, v56
v_exp_f32 v57, v57
v_cvt_pk_bf16_f32 v11, v54, v55
v_exp_f32 v58, v58
v_exp_f32 v59, v59
v_mfma_f32_32x32x16_bf16 a[176:191], v[176:179], v[8:11], a[176:191]
v_cvt_pk_bf16_f32 v12, v56, v57
v_exp_f32 v60, v60
v_exp_f32 v61, v61
v_cvt_pk_bf16_f32 v13, v58, v59
v_exp_f32 v62, v62
v_exp_f32 v63, v63
v_cvt_pk_bf16_f32 v14, v60, v61
v_exp_f32 v32, v32
)ASM"
R"ASM(v_cvt_pk_bf16_f32 v15, v62, v63
v_exp_f32 v33, v33
v_exp_f32 v34, v34
v_mfma_f32_32x32x16_bf16 a[176:191], v[184:187], v[12:15], a[176:191]
v_exp_f32 v35, v35
ds_read_b128 v[184:187], v29 offset:23232
v_cvt_pk_bf16_f32 v8, v32, v33
s_add_u32 s82, s82, 4096
buffer_load_dwordx4 a[96:99], v3, s[20:23], s82 offen offset:0 sc1
v_exp_f32 v36, v36
v_exp_f32 v37, v37
ds_read_b128 v[176:179], v28 offset:23232
v_cvt_pk_bf16_f32 v9, v34, v35
v_exp_f32 v38, v38
v_mfma_f32_32x32x16_bf16 v[48:63], v[172:175], a[196:199], 0
v_exp_f32 v39, v39
v_cvt_pk_bf16_f32 v10, v36, v37
ds_read_b128 v[64:67], v27 offset:22528
v_exp_f32 v40, v40
v_exp_f32 v41, v41
v_cvt_pk_bf16_f32 v11, v38, v39
v_exp_f32 v42, v42
v_exp_f32 v43, v43
v_mfma_f32_32x32x16_bf16 a[128:143], v[180:183], v[8:11], a[128:143]
v_cvt_pk_bf16_f32 v12, v40, v41
v_exp_f32 v44, v44
v_exp_f32 v45, v45
v_cvt_pk_bf16_f32 v13, v42, v43
v_exp_f32 v46, v46
v_exp_f32 v47, v47
v_cvt_pk_bf16_f32 v14, v44, v45
v_exp_f32 v48, v48
)ASM"
R"ASM(v_cvt_pk_bf16_f32 v15, v46, v47
v_exp_f32 v49, v49
v_exp_f32 v50, v50
v_mfma_f32_32x32x16_bf16 a[128:143], v[188:191], v[12:15], a[128:143]
v_exp_f32 v51, v51
v_cvt_pk_bf16_f32 v8, v48, v49
s_add_u32 s87, s75, 196608
buffer_load_dwordx4 v[136:139], v3, s[20:23], s87 offen offset:0 sc1
v_exp_f32 v52, v52
v_exp_f32 v53, v53
v_cvt_pk_bf16_f32 v9, v50, v51
v_exp_f32 v54, v54
v_mfma_f32_32x32x16_bf16 v[32:47], v[172:175], a[200:203], 0
v_exp_f32 v55, v55
v_cvt_pk_bf16_f32 v10, v52, v53
v_exp_f32 v56, v56
v_exp_f32 v57, v57
v_cvt_pk_bf16_f32 v11, v54, v55
v_exp_f32 v58, v58
v_exp_f32 v59, v59
v_mfma_f32_32x32x16_bf16 a[144:159], v[180:183], v[8:11], a[144:159]
v_cvt_pk_bf16_f32 v12, v56, v57
v_exp_f32 v60, v60
v_exp_f32 v61, v61
v_cvt_pk_bf16_f32 v13, v58, v59
v_exp_f32 v62, v62
v_exp_f32 v63, v63
v_cvt_pk_bf16_f32 v14, v60, v61
v_exp_f32 v32, v32
v_cvt_pk_bf16_f32 v15, v62, v63
v_exp_f32 v33, v33
v_exp_f32 v34, v34
)ASM"
R"ASM(v_mfma_f32_32x32x16_bf16 a[144:159], v[188:191], v[12:15], a[144:159]
v_exp_f32 v35, v35
v_cvt_pk_bf16_f32 v8, v32, v33
buffer_load_dwordx4 a[36:39], v3, s[20:23], s81 offen offset:1024 sc1
v_exp_f32 v36, v36
v_exp_f32 v37, v37
v_cvt_pk_bf16_f32 v9, v34, v35
v_exp_f32 v38, v38
v_mfma_f32_32x32x16_bf16 v[48:63], v[172:175], a[204:207], 0
v_exp_f32 v39, v39
v_cvt_pk_bf16_f32 v10, v36, v37
v_exp_f32 v40, v40
v_exp_f32 v41, v41
v_cvt_pk_bf16_f32 v11, v38, v39
v_exp_f32 v42, v42
v_exp_f32 v43, v43
v_mfma_f32_32x32x16_bf16 a[160:175], v[180:183], v[8:11], a[160:175]
v_cvt_pk_bf16_f32 v12, v40, v41
v_exp_f32 v44, v44
v_exp_f32 v45, v45
v_cvt_pk_bf16_f32 v13, v42, v43
v_exp_f32 v46, v46
v_exp_f32 v47, v47
v_cvt_pk_bf16_f32 v14, v44, v45
v_exp_f32 v48, v48
v_cvt_pk_bf16_f32 v15, v46, v47
v_exp_f32 v49, v49
v_exp_f32 v50, v50
v_mfma_f32_32x32x16_bf16 a[160:175], v[188:191], v[12:15], a[160:175]
v_exp_f32 v51, v51
v_cvt_pk_bf16_f32 v8, v48, v49
buffer_load_dwordx4 v[140:143], v3, s[20:23], s86 offen offset:2048 sc1
)ASM"
R"ASM(v_exp_f32 v52, v52
v_exp_f32 v53, v53
v_cvt_pk_bf16_f32 v9, v50, v51
v_exp_f32 v54, v54
s_waitcnt lgkmcnt(0)
v_mfma_f32_32x32x16_bf16 v[32:47], v[64:67], a[192:195], 0
v_exp_f32 v55, v55
v_cvt_pk_bf16_f32 v10, v52, v53
v_exp_f32 v56, v56
v_exp_f32 v57, v57
v_cvt_pk_bf16_f32 v11, v54, v55
v_exp_f32 v58, v58
v_exp_f32 v59, v59
v_mfma_f32_32x32x16_bf16 a[176:191], v[180:183], v[8:11], a[176:191]
v_cvt_pk_bf16_f32 v12, v56, v57
v_exp_f32 v60, v60
v_exp_f32 v61, v61
v_cvt_pk_bf16_f32 v13, v58, v59
v_exp_f32 v62, v62
v_exp_f32 v63, v63
v_cvt_pk_bf16_f32 v14, v60, v61
v_exp_f32 v32, v32
v_cvt_pk_bf16_f32 v15, v62, v63
v_exp_f32 v33, v33
v_exp_f32 v34, v34
v_mfma_f32_32x32x16_bf16 a[176:191], v[188:191], v[12:15], a[176:191]
v_exp_f32 v35, v35
ds_read_b128 v[188:191], v29 offset:24288
v_cvt_pk_bf16_f32 v8, v32, v33
buffer_load_dwordx4 a[100:103], v3, s[20:23], s82 offen offset:1024 sc1
v_exp_f32 v36, v36
v_exp_f32 v37, v37
)ASM"
R"ASM(ds_read_b128 v[180:183], v28 offset:24288
v_cvt_pk_bf16_f32 v9, v34, v35
v_exp_f32 v38, v38
v_mfma_f32_32x32x16_bf16 v[48:63], v[64:67], a[196:199], 0
v_exp_f32 v39, v39
v_cvt_pk_bf16_f32 v10, v36, v37
ds_read_b128 v[172:175], v27 offset:23552
v_exp_f32 v40, v40
v_exp_f32 v41, v41
v_cvt_pk_bf16_f32 v11, v38, v39
v_exp_f32 v42, v42
v_exp_f32 v43, v43
v_mfma_f32_32x32x16_bf16 a[128:143], v[176:179], v[8:11], a[128:143]
v_cvt_pk_bf16_f32 v12, v40, v41
v_exp_f32 v44, v44
v_exp_f32 v45, v45
v_cvt_pk_bf16_f32 v13, v42, v43
v_exp_f32 v46, v46
v_exp_f32 v47, v47
v_cvt_pk_bf16_f32 v14, v44, v45
v_exp_f32 v48, v48
v_cvt_pk_bf16_f32 v15, v46, v47
v_exp_f32 v49, v49
v_exp_f32 v50, v50
v_mfma_f32_32x32x16_bf16 a[128:143], v[184:187], v[12:15], a[128:143]
v_exp_f32 v51, v51
v_cvt_pk_bf16_f32 v8, v48, v49
buffer_load_dwordx4 v[144:147], v3, s[20:23], s87 offen offset:2048 sc1
v_exp_f32 v52, v52
v_exp_f32 v53, v53
v_cvt_pk_bf16_f32 v9, v50, v51
v_exp_f32 v54, v54
)ASM"
R"ASM(v_mfma_f32_32x32x16_bf16 v[32:47], v[64:67], a[200:203], 0
v_exp_f32 v55, v55
v_cvt_pk_bf16_f32 v10, v52, v53
v_exp_f32 v56, v56
v_exp_f32 v57, v57
v_cvt_pk_bf16_f32 v11, v54, v55
v_exp_f32 v58, v58
v_exp_f32 v59, v59
v_mfma_f32_32x32x16_bf16 a[144:159], v[176:179], v[8:11], a[144:159]
v_cvt_pk_bf16_f32 v12, v56, v57
v_exp_f32 v60, v60
v_exp_f32 v61, v61
v_cvt_pk_bf16_f32 v13, v58, v59
v_exp_f32 v62, v62
v_exp_f32 v63, v63
v_cvt_pk_bf16_f32 v14, v60, v61
v_exp_f32 v32, v32
v_cvt_pk_bf16_f32 v15, v62, v63
v_exp_f32 v33, v33
v_exp_f32 v34, v34
v_mfma_f32_32x32x16_bf16 a[144:159], v[184:187], v[12:15], a[144:159]
v_exp_f32 v35, v35
v_cvt_pk_bf16_f32 v8, v32, v33
buffer_load_dwordx4 a[40:43], v3, s[20:23], s81 offen offset:2048 sc1
v_exp_f32 v36, v36
v_exp_f32 v37, v37
v_cvt_pk_bf16_f32 v9, v34, v35
v_exp_f32 v38, v38
v_mfma_f32_32x32x16_bf16 v[48:63], v[64:67], a[204:207], 0
v_exp_f32 v39, v39
v_cvt_pk_bf16_f32 v10, v36, v37
v_exp_f32 v40, v40
)ASM"
R"ASM(v_exp_f32 v41, v41
v_cvt_pk_bf16_f32 v11, v38, v39
v_exp_f32 v42, v42
v_exp_f32 v43, v43
v_mfma_f32_32x32x16_bf16 a[160:175], v[176:179], v[8:11], a[160:175]
v_cvt_pk_bf16_f32 v12, v40, v41
v_exp_f32 v44, v44
v_exp_f32 v45, v45
v_cvt_pk_bf16_f32 v13, v42, v43
v_exp_f32 v46, v46
v_exp_f32 v47, v47
v_cvt_pk_bf16_f32 v14, v44, v45
v_exp_f32 v48, v48
v_cvt_pk_bf16_f32 v15, v46, v47
v_exp_f32 v49, v49
v_exp_f32 v50, v50
v_mfma_f32_32x32x16_bf16 a[160:175], v[184:187], v[12:15], a[160:175]
v_exp_f32 v51, v51
v_cvt_pk_bf16_f32 v8, v48, v49
s_add_u32 s86, s74, 200704
buffer_load_dwordx4 v[148:151], v3, s[20:23], s86 offen offset:0 sc1
v_exp_f32 v52, v52
v_exp_f32 v53, v53
v_cvt_pk_bf16_f32 v9, v50, v51
v_exp_f32 v54, v54
s_waitcnt lgkmcnt(0)
v_mfma_f32_32x32x16_bf16 v[32:47], v[172:175], a[192:195], 0
v_exp_f32 v55, v55
v_cvt_pk_bf16_f32 v10, v52, v53
v_exp_f32 v56, v56
v_exp_f32 v57, v57
v_cvt_pk_bf16_f32 v11, v54, v55
)ASM"
R"ASM(v_exp_f32 v58, v58
v_exp_f32 v59, v59
v_mfma_f32_32x32x16_bf16 a[176:191], v[176:179], v[8:11], a[176:191]
v_cvt_pk_bf16_f32 v12, v56, v57
v_exp_f32 v60, v60
v_exp_f32 v61, v61
v_cvt_pk_bf16_f32 v13, v58, v59
v_exp_f32 v62, v62
v_exp_f32 v63, v63
v_cvt_pk_bf16_f32 v14, v60, v61
v_exp_f32 v32, v32
v_cvt_pk_bf16_f32 v15, v62, v63
v_exp_f32 v33, v33
v_exp_f32 v34, v34
v_mfma_f32_32x32x16_bf16 a[176:191], v[184:187], v[12:15], a[176:191]
v_exp_f32 v35, v35
ds_read_b128 v[184:187], v29 offset:25344
v_cvt_pk_bf16_f32 v8, v32, v33
buffer_load_dwordx4 a[104:107], v3, s[20:23], s82 offen offset:2048 sc1
v_exp_f32 v36, v36
v_exp_f32 v37, v37
ds_read_b128 v[176:179], v28 offset:25344
v_cvt_pk_bf16_f32 v9, v34, v35
v_exp_f32 v38, v38
v_mfma_f32_32x32x16_bf16 v[48:63], v[172:175], a[196:199], 0
v_exp_f32 v39, v39
v_cvt_pk_bf16_f32 v10, v36, v37
ds_read_b128 v[64:67], v27 offset:24576
v_exp_f32 v40, v40
v_exp_f32 v41, v41
v_cvt_pk_bf16_f32 v11, v38, v39
v_exp_f32 v42, v42
)ASM"
R"ASM(v_exp_f32 v43, v43
v_mfma_f32_32x32x16_bf16 a[128:143], v[180:183], v[8:11], a[128:143]
v_cvt_pk_bf16_f32 v12, v40, v41
v_exp_f32 v44, v44
v_exp_f32 v45, v45
v_cvt_pk_bf16_f32 v13, v42, v43
v_exp_f32 v46, v46
v_exp_f32 v47, v47
v_cvt_pk_bf16_f32 v14, v44, v45
v_exp_f32 v48, v48
v_cvt_pk_bf16_f32 v15, v46, v47
v_exp_f32 v49, v49
v_exp_f32 v50, v50
v_mfma_f32_32x32x16_bf16 a[128:143], v[188:191], v[12:15], a[128:143]
v_exp_f32 v51, v51
v_cvt_pk_bf16_f32 v8, v48, v49
s_add_u32 s87, s75, 200704
buffer_load_dwordx4 v[152:155], v3, s[20:23], s87 offen offset:0 sc1
v_exp_f32 v52, v52
v_exp_f32 v53, v53
v_cvt_pk_bf16_f32 v9, v50, v51
v_exp_f32 v54, v54
v_mfma_f32_32x32x16_bf16 v[32:47], v[172:175], a[200:203], 0
v_exp_f32 v55, v55
v_cvt_pk_bf16_f32 v10, v52, v53
v_exp_f32 v56, v56
v_exp_f32 v57, v57
v_cvt_pk_bf16_f32 v11, v54, v55
v_exp_f32 v58, v58
v_exp_f32 v59, v59
v_mfma_f32_32x32x16_bf16 a[144:159], v[180:183], v[8:11], a[144:159]
v_cvt_pk_bf16_f32 v12, v56, v57
)ASM"
R"ASM(v_exp_f32 v60, v60
v_exp_f32 v61, v61
v_cvt_pk_bf16_f32 v13, v58, v59
v_exp_f32 v62, v62
v_exp_f32 v63, v63
v_cvt_pk_bf16_f32 v14, v60, v61
v_exp_f32 v32, v32
v_cvt_pk_bf16_f32 v15, v62, v63
v_exp_f32 v33, v33
v_exp_f32 v34, v34
v_mfma_f32_32x32x16_bf16 a[144:159], v[188:191], v[12:15], a[144:159]
v_exp_f32 v35, v35
v_cvt_pk_bf16_f32 v8, v32, v33
buffer_load_dwordx4 a[44:47], v3, s[20:23], s81 offen offset:3072 sc1
v_exp_f32 v36, v36
v_exp_f32 v37, v37
v_cvt_pk_bf16_f32 v9, v34, v35
v_exp_f32 v38, v38
v_mfma_f32_32x32x16_bf16 v[48:63], v[172:175], a[204:207], 0
v_exp_f32 v39, v39
v_cvt_pk_bf16_f32 v10, v36, v37
v_exp_f32 v40, v40
v_exp_f32 v41, v41
v_cvt_pk_bf16_f32 v11, v38, v39
v_exp_f32 v42, v42
v_exp_f32 v43, v43
v_mfma_f32_32x32x16_bf16 a[160:175], v[180:183], v[8:11], a[160:175]
v_cvt_pk_bf16_f32 v12, v40, v41
v_exp_f32 v44, v44
v_exp_f32 v45, v45
v_cvt_pk_bf16_f32 v13, v42, v43
v_exp_f32 v46, v46
)ASM"
R"ASM(v_exp_f32 v47, v47
v_cvt_pk_bf16_f32 v14, v44, v45
v_exp_f32 v48, v48
v_cvt_pk_bf16_f32 v15, v46, v47
v_exp_f32 v49, v49
v_exp_f32 v50, v50
v_mfma_f32_32x32x16_bf16 a[160:175], v[188:191], v[12:15], a[160:175]
v_exp_f32 v51, v51
v_cvt_pk_bf16_f32 v8, v48, v49
buffer_load_dwordx4 v[156:159], v3, s[20:23], s86 offen offset:2048 sc1
v_exp_f32 v52, v52
v_exp_f32 v53, v53
v_cvt_pk_bf16_f32 v9, v50, v51
v_exp_f32 v54, v54
s_waitcnt lgkmcnt(0)
v_mfma_f32_32x32x16_bf16 v[32:47], v[64:67], a[192:195], 0
v_exp_f32 v55, v55
v_cvt_pk_bf16_f32 v10, v52, v53
v_exp_f32 v56, v56
v_exp_f32 v57, v57
v_cvt_pk_bf16_f32 v11, v54, v55
v_exp_f32 v58, v58
v_exp_f32 v59, v59
v_mfma_f32_32x32x16_bf16 a[176:191], v[180:183], v[8:11], a[176:191]
v_cvt_pk_bf16_f32 v12, v56, v57
v_exp_f32 v60, v60
v_exp_f32 v61, v61
v_cvt_pk_bf16_f32 v13, v58, v59
v_exp_f32 v62, v62
v_exp_f32 v63, v63
v_cvt_pk_bf16_f32 v14, v60, v61
v_exp_f32 v32, v32
)ASM"
R"ASM(v_cvt_pk_bf16_f32 v15, v62, v63
v_exp_f32 v33, v33
v_exp_f32 v34, v34
v_mfma_f32_32x32x16_bf16 a[176:191], v[188:191], v[12:15], a[176:191]
v_exp_f32 v35, v35
ds_read_b128 v[188:191], v29 offset:26400
v_cvt_pk_bf16_f32 v8, v32, v33
buffer_load_dwordx4 a[108:111], v3, s[20:23], s82 offen offset:3072 sc1
v_exp_f32 v36, v36
v_exp_f32 v37, v37
ds_read_b128 v[180:183], v28 offset:26400
v_cvt_pk_bf16_f32 v9, v34, v35
v_exp_f32 v38, v38
v_mfma_f32_32x32x16_bf16 v[48:63], v[64:67], a[196:199], 0
v_exp_f32 v39, v39
v_cvt_pk_bf16_f32 v10, v36, v37
ds_read_b128 v[172:175], v27 offset:25600
v_exp_f32 v40, v40
v_lshrrev_b32 v31, 4, v1
v_min_u32 v31, 2, v31
v_lshlrev_b32 v31, 8, v31
v_and_or_b32 v31, v1, 15, v31
v_lshlrev_b32 v31, 7, v31
s_lshl_b32 s85, s12, 2
s_add_u32 s85, s85, s13
s_lshr_b32 s84, s85, 4
s_and_b32 s85, s85, 15
s_lshl_b32 s85, s85, 11
s_mul_i32 s84, s84, 229376
s_add_u32 s85, s85, s84
s_and_b32 s84, s11, 1
s_mul_i32 s84, s84, 1835008
)ASM"
R"ASM(s_add_u32 s85, s85, s84
s_add_u32 s85, s85, 0xc00000
buffer_load_dword v164, v31, s[20:23], s85 offen sc1
v_exp_f32 v41, v41
v_cvt_pk_bf16_f32 v11, v38, v39
v_exp_f32 v42, v42
v_exp_f32 v43, v43
v_mfma_f32_32x32x16_bf16 a[128:143], v[176:179], v[8:11], a[128:143]
v_cvt_pk_bf16_f32 v12, v40, v41
v_exp_f32 v44, v44
v_exp_f32 v45, v45
v_cvt_pk_bf16_f32 v13, v42, v43
v_exp_f32 v46, v46
v_exp_f32 v47, v47
v_cvt_pk_bf16_f32 v14, v44, v45
v_exp_f32 v48, v48
v_cvt_pk_bf16_f32 v15, v46, v47
v_exp_f32 v49, v49
v_exp_f32 v50, v50
v_mfma_f32_32x32x16_bf16 a[128:143], v[184:187], v[12:15], a[128:143]
v_exp_f32 v51, v51
v_cvt_pk_bf16_f32 v8, v48, v49
buffer_load_dwordx4 v[160:163], v3, s[20:23], s87 offen offset:2048 sc1
v_exp_f32 v52, v52
v_exp_f32 v53, v53
v_cvt_pk_bf16_f32 v9, v50, v51
v_exp_f32 v54, v54
v_mfma_f32_32x32x16_bf16 v[32:47], v[64:67], a[200:203], 0
v_exp_f32 v55, v55
v_cvt_pk_bf16_f32 v10, v52, v53
v_exp_f32 v56, v56
v_exp_f32 v57, v57
)ASM"
R"ASM(v_cvt_pk_bf16_f32 v11, v54, v55
v_exp_f32 v58, v58
v_exp_f32 v59, v59
v_mfma_f32_32x32x16_bf16 a[144:159], v[176:179], v[8:11], a[144:159]
v_cvt_pk_bf16_f32 v12, v56, v57
v_exp_f32 v60, v60
v_exp_f32 v61, v61
v_cvt_pk_bf16_f32 v13, v58, v59
v_exp_f32 v62, v62
v_exp_f32 v63, v63
v_cvt_pk_bf16_f32 v14, v60, v61
v_exp_f32 v32, v32
v_cvt_pk_bf16_f32 v15, v62, v63
v_exp_f32 v33, v33
v_exp_f32 v34, v34
v_mfma_f32_32x32x16_bf16 a[144:159], v[184:187], v[12:15], a[144:159]
v_exp_f32 v35, v35
v_cvt_pk_bf16_f32 v8, v32, v33
s_add_u32 s81, s81, 4096
buffer_load_dwordx4 a[48:51], v3, s[20:23], s81 offen offset:0 sc1
v_exp_f32 v36, v36
v_exp_f32 v37, v37
v_cvt_pk_bf16_f32 v9, v34, v35
v_exp_f32 v38, v38
v_mfma_f32_32x32x16_bf16 v[48:63], v[64:67], a[204:207], 0
v_exp_f32 v39, v39
v_cvt_pk_bf16_f32 v10, v36, v37
v_exp_f32 v40, v40
v_exp_f32 v41, v41
v_cvt_pk_bf16_f32 v11, v38, v39
v_exp_f32 v42, v42
v_exp_f32 v43, v43
)ASM"
R"ASM(v_mfma_f32_32x32x16_bf16 a[160:175], v[176:179], v[8:11], a[160:175]
v_cvt_pk_bf16_f32 v12, v40, v41
v_exp_f32 v44, v44
v_exp_f32 v45, v45
v_cvt_pk_bf16_f32 v13, v42, v43
v_exp_f32 v46, v46
v_exp_f32 v47, v47
v_cvt_pk_bf16_f32 v14, v44, v45
v_exp_f32 v48, v48
v_cvt_pk_bf16_f32 v15, v46, v47
v_exp_f32 v49, v49
v_exp_f32 v50, v50
v_mfma_f32_32x32x16_bf16 a[160:175], v[184:187], v[12:15], a[160:175]
v_exp_f32 v51, v51
v_cvt_pk_bf16_f32 v8, v48, v49
s_add_u32 s86, s74, 69632
buffer_load_dwordx4 a[208:211], v3, s[20:23], s86 offen offset:0 sc1
v_exp_f32 v52, v52
v_exp_f32 v53, v53
v_cvt_pk_bf16_f32 v9, v50, v51
v_exp_f32 v54, v54
s_waitcnt lgkmcnt(0)
v_mfma_f32_32x32x16_bf16 v[32:47], v[172:175], a[192:195], 0
v_exp_f32 v55, v55
v_cvt_pk_bf16_f32 v10, v52, v53
v_exp_f32 v56, v56
v_exp_f32 v57, v57
v_cvt_pk_bf16_f32 v11, v54, v55
v_exp_f32 v58, v58
v_exp_f32 v59, v59
v_mfma_f32_32x32x16_bf16 a[176:191], v[176:179], v[8:11], a[176:191]
v_cvt_pk_bf16_f32 v12, v56, v57
)ASM"
R"ASM(v_exp_f32 v60, v60
v_exp_f32 v61, v61
v_cvt_pk_bf16_f32 v13, v58, v59
v_exp_f32 v62, v62
v_exp_f32 v63, v63
v_cvt_pk_bf16_f32 v14, v60, v61
v_exp_f32 v32, v32
v_cvt_pk_bf16_f32 v15, v62, v63
v_exp_f32 v33, v33
v_exp_f32 v34, v34
v_mfma_f32_32x32x16_bf16 a[176:191], v[184:187], v[12:15], a[176:191]
v_exp_f32 v35, v35
ds_read_b128 v[184:187], v29 offset:27456
v_cvt_pk_bf16_f32 v8, v32, v33
s_add_u32 s82, s82, 4096
buffer_load_dwordx4 a[112:115], v3, s[20:23], s82 offen offset:0 sc1
v_exp_f32 v36, v36
v_exp_f32 v37, v37
ds_read_b128 v[176:179], v28 offset:27456
v_cvt_pk_bf16_f32 v9, v34, v35
v_exp_f32 v38, v38
v_mfma_f32_32x32x16_bf16 v[48:63], v[172:175], a[196:199], 0
v_exp_f32 v39, v39
v_cvt_pk_bf16_f32 v10, v36, v37
ds_read_b128 v[64:67], v27 offset:26624
v_exp_f32 v40, v40
v_exp_f32 v41, v41
v_cvt_pk_bf16_f32 v11, v38, v39
v_exp_f32 v42, v42
v_exp_f32 v43, v43
v_mfma_f32_32x32x16_bf16 a[128:143], v[180:183], v[8:11], a[128:143]
v_cvt_pk_bf16_f32 v12, v40, v41
)ASM"
R"ASM(v_exp_f32 v44, v44
v_exp_f32 v45, v45
v_cvt_pk_bf16_f32 v13, v42, v43
v_exp_f32 v46, v46
v_exp_f32 v47, v47
v_cvt_pk_bf16_f32 v14, v44, v45
v_exp_f32 v48, v48
v_cvt_pk_bf16_f32 v15, v46, v47
v_exp_f32 v49, v49
v_exp_f32 v50, v50
v_mfma_f32_32x32x16_bf16 a[128:143], v[188:191], v[12:15], a[128:143]
v_exp_f32 v51, v51
v_cvt_pk_bf16_f32 v8, v48, v49
s_add_u32 s87, s75, 69632
buffer_load_dwordx4 a[212:215], v3, s[20:23], s87 offen offset:0 sc1
v_exp_f32 v52, v52
v_exp_f32 v53, v53
v_cvt_pk_bf16_f32 v9, v50, v51
v_exp_f32 v54, v54
v_mfma_f32_32x32x16_bf16 v[32:47], v[172:175], a[200:203], 0
v_exp_f32 v55, v55
v_cvt_pk_bf16_f32 v10, v52, v53
v_exp_f32 v56, v56
v_exp_f32 v57, v57
v_cvt_pk_bf16_f32 v11, v54, v55
v_exp_f32 v58, v58
v_exp_f32 v59, v59
v_mfma_f32_32x32x16_bf16 a[144:159], v[180:183], v[8:11], a[144:159]
v_cvt_pk_bf16_f32 v12, v56, v57
v_exp_f32 v60, v60
v_exp_f32 v61, v61
v_cvt_pk_bf16_f32 v13, v58, v59
)ASM"
R"ASM(v_exp_f32 v62, v62
v_exp_f32 v63, v63
v_cvt_pk_bf16_f32 v14, v60, v61
v_exp_f32 v32, v32
v_cvt_pk_bf16_f32 v15, v62, v63
v_exp_f32 v33, v33
v_exp_f32 v34, v34
v_mfma_f32_32x32x16_bf16 a[144:159], v[188:191], v[12:15], a[144:159]
v_exp_f32 v35, v35
v_cvt_pk_bf16_f32 v8, v32, v33
buffer_load_dwordx4 a[52:55], v3, s[20:23], s81 offen offset:1024 sc1
v_exp_f32 v36, v36
v_exp_f32 v37, v37
v_cvt_pk_bf16_f32 v9, v34, v35
v_exp_f32 v38, v38
v_mfma_f32_32x32x16_bf16 v[48:63], v[172:175], a[204:207], 0
v_exp_f32 v39, v39
v_cvt_pk_bf16_f32 v10, v36, v37
v_exp_f32 v40, v40
v_exp_f32 v41, v41
v_cvt_pk_bf16_f32 v11, v38, v39
v_exp_f32 v42, v42
v_exp_f32 v43, v43
v_mfma_f32_32x32x16_bf16 a[160:175], v[180:183], v[8:11], a[160:175]
v_cvt_pk_bf16_f32 v12, v40, v41
v_exp_f32 v44, v44
v_exp_f32 v45, v45
v_cvt_pk_bf16_f32 v13, v42, v43
v_exp_f32 v46, v46
v_exp_f32 v47, v47
v_cvt_pk_bf16_f32 v14, v44, v45
v_exp_f32 v48, v48
)ASM"
R"ASM(v_cvt_pk_bf16_f32 v15, v46, v47
v_exp_f32 v49, v49
v_exp_f32 v50, v50
v_mfma_f32_32x32x16_bf16 a[160:175], v[188:191], v[12:15], a[160:175]
v_exp_f32 v51, v51
v_cvt_pk_bf16_f32 v8, v48, v49
buffer_load_dwordx4 a[216:219], v3, s[20:23], s86 offen offset:2048 sc1
v_exp_f32 v52, v52
v_exp_f32 v53, v53
v_cvt_pk_bf16_f32 v9, v50, v51
v_exp_f32 v54, v54
s_waitcnt lgkmcnt(0)
v_mfma_f32_32x32x16_bf16 v[32:47], v[64:67], a[192:195], 0
v_exp_f32 v55, v55
v_cvt_pk_bf16_f32 v10, v52, v53
v_exp_f32 v56, v56
v_exp_f32 v57, v57
v_cvt_pk_bf16_f32 v11, v54, v55
v_exp_f32 v58, v58
v_exp_f32 v59, v59
v_mfma_f32_32x32x16_bf16 a[176:191], v[180:183], v[8:11], a[176:191]
v_cvt_pk_bf16_f32 v12, v56, v57
v_exp_f32 v60, v60
v_exp_f32 v61, v61
v_cvt_pk_bf16_f32 v13, v58, v59
v_exp_f32 v62, v62
v_exp_f32 v63, v63
v_cvt_pk_bf16_f32 v14, v60, v61
v_exp_f32 v32, v32
v_cvt_pk_bf16_f32 v15, v62, v63
v_exp_f32 v33, v33
v_exp_f32 v34, v34
)ASM"
R"ASM(v_mfma_f32_32x32x16_bf16 a[176:191], v[188:191], v[12:15], a[176:191]
v_exp_f32 v35, v35
ds_read_b128 v[188:191], v29 offset:28512
v_cvt_pk_bf16_f32 v8, v32, v33
buffer_load_dwordx4 a[116:119], v3, s[20:23], s82 offen offset:1024 sc1
v_exp_f32 v36, v36
v_exp_f32 v37, v37
ds_read_b128 v[180:183], v28 offset:28512
v_cvt_pk_bf16_f32 v9, v34, v35
v_exp_f32 v38, v38
v_mfma_f32_32x32x16_bf16 v[48:63], v[64:67], a[196:199], 0
v_exp_f32 v39, v39
v_cvt_pk_bf16_f32 v10, v36, v37
ds_read_b128 v[172:175], v27 offset:27648
v_exp_f32 v40, v40
v_exp_f32 v41, v41
v_cvt_pk_bf16_f32 v11, v38, v39
v_exp_f32 v42, v42
v_exp_f32 v43, v43
v_mfma_f32_32x32x16_bf16 a[128:143], v[176:179], v[8:11], a[128:143]
v_cvt_pk_bf16_f32 v12, v40, v41
v_exp_f32 v44, v44
v_exp_f32 v45, v45
v_cvt_pk_bf16_f32 v13, v42, v43
v_exp_f32 v46, v46
v_exp_f32 v47, v47
v_cvt_pk_bf16_f32 v14, v44, v45
v_exp_f32 v48, v48
v_cvt_pk_bf16_f32 v15, v46, v47
v_exp_f32 v49, v49
v_exp_f32 v50, v50
v_mfma_f32_32x32x16_bf16 a[128:143], v[184:187], v[12:15], a[128:143]
)ASM"
R"ASM(v_exp_f32 v51, v51
v_cvt_pk_bf16_f32 v8, v48, v49
buffer_load_dwordx4 a[220:223], v3, s[20:23], s87 offen offset:2048 sc1
v_exp_f32 v52, v52
v_exp_f32 v53, v53
v_cvt_pk_bf16_f32 v9, v50, v51
v_exp_f32 v54, v54
v_mfma_f32_32x32x16_bf16 v[32:47], v[64:67], a[200:203], 0
v_exp_f32 v55, v55
v_cvt_pk_bf16_f32 v10, v52, v53
v_exp_f32 v56, v56
v_exp_f32 v57, v57
v_cvt_pk_bf16_f32 v11, v54, v55
v_exp_f32 v58, v58
v_exp_f32 v59, v59
v_mfma_f32_32x32x16_bf16 a[144:159], v[176:179], v[8:11], a[144:159]
v_cvt_pk_bf16_f32 v12, v56, v57
v_exp_f32 v60, v60
v_exp_f32 v61, v61
v_cvt_pk_bf16_f32 v13, v58, v59
v_exp_f32 v62, v62
v_exp_f32 v63, v63
v_cvt_pk_bf16_f32 v14, v60, v61
v_exp_f32 v32, v32
v_cvt_pk_bf16_f32 v15, v62, v63
v_exp_f32 v33, v33
v_exp_f32 v34, v34
v_mfma_f32_32x32x16_bf16 a[144:159], v[184:187], v[12:15], a[144:159]
v_exp_f32 v35, v35
v_cvt_pk_bf16_f32 v8, v32, v33
buffer_load_dwordx4 a[56:59], v3, s[20:23], s81 offen offset:2048 sc1
v_exp_f32 v36, v36
)ASM"
R"ASM(v_exp_f32 v37, v37
v_cvt_pk_bf16_f32 v9, v34, v35
v_exp_f32 v38, v38
v_mfma_f32_32x32x16_bf16 v[48:63], v[64:67], a[204:207], 0
v_exp_f32 v39, v39
v_cvt_pk_bf16_f32 v10, v36, v37
v_exp_f32 v40, v40
v_exp_f32 v41, v41
v_cvt_pk_bf16_f32 v11, v38, v39
v_exp_f32 v42, v42
v_exp_f32 v43, v43
v_mfma_f32_32x32x16_bf16 a[160:175], v[176:179], v[8:11], a[160:175]
v_cvt_pk_bf16_f32 v12, v40, v41
v_exp_f32 v44, v44
v_exp_f32 v45, v45
v_cvt_pk_bf16_f32 v13, v42, v43
v_exp_f32 v46, v46
v_exp_f32 v47, v47
v_cvt_pk_bf16_f32 v14, v44, v45
v_exp_f32 v48, v48
v_cvt_pk_bf16_f32 v15, v46, v47
v_exp_f32 v49, v49
v_exp_f32 v50, v50
v_mfma_f32_32x32x16_bf16 a[160:175], v[184:187], v[12:15], a[160:175]
v_exp_f32 v51, v51
v_cvt_pk_bf16_f32 v8, v48, v49
s_add_u32 s86, s74, 98304
buffer_load_dwordx4 a[224:227], v3, s[20:23], s86 offen offset:0 sc1
v_exp_f32 v52, v52
v_exp_f32 v53, v53
v_cvt_pk_bf16_f32 v9, v50, v51
v_exp_f32 v54, v54
)ASM"
R"ASM(s_waitcnt lgkmcnt(0)
v_mfma_f32_32x32x16_bf16 v[32:47], v[172:175], a[192:195], 0
v_exp_f32 v55, v55
v_cvt_pk_bf16_f32 v10, v52, v53
v_exp_f32 v56, v56
v_exp_f32 v57, v57
v_cvt_pk_bf16_f32 v11, v54, v55
v_exp_f32 v58, v58
v_exp_f32 v59, v59
v_mfma_f32_32x32x16_bf16 a[176:191], v[176:179], v[8:11], a[176:191]
v_cvt_pk_bf16_f32 v12, v56, v57
v_exp_f32 v60, v60
v_exp_f32 v61, v61
v_cvt_pk_bf16_f32 v13, v58, v59
v_exp_f32 v62, v62
v_exp_f32 v63, v63
v_cvt_pk_bf16_f32 v14, v60, v61
v_exp_f32 v32, v32
v_cvt_pk_bf16_f32 v15, v62, v63
v_exp_f32 v33, v33
v_exp_f32 v34, v34
v_mfma_f32_32x32x16_bf16 a[176:191], v[184:187], v[12:15], a[176:191]
v_exp_f32 v35, v35
ds_read_b128 v[184:187], v29 offset:29568
v_cvt_pk_bf16_f32 v8, v32, v33
buffer_load_dwordx4 a[120:123], v3, s[20:23], s82 offen offset:2048 sc1
v_exp_f32 v36, v36
v_exp_f32 v37, v37
ds_read_b128 v[176:179], v28 offset:29568
v_cvt_pk_bf16_f32 v9, v34, v35
v_exp_f32 v38, v38
v_mfma_f32_32x32x16_bf16 v[48:63], v[172:175], a[196:199], 0
)ASM"
R"ASM(v_exp_f32 v39, v39
v_cvt_pk_bf16_f32 v10, v36, v37
ds_read_b128 v[64:67], v27 offset:28672
v_exp_f32 v40, v40
v_exp_f32 v41, v41
v_cvt_pk_bf16_f32 v11, v38, v39
v_exp_f32 v42, v42
v_exp_f32 v43, v43
v_mfma_f32_32x32x16_bf16 a[128:143], v[180:183], v[8:11], a[128:143]
v_cvt_pk_bf16_f32 v12, v40, v41
v_exp_f32 v44, v44
v_exp_f32 v45, v45
v_cvt_pk_bf16_f32 v13, v42, v43
v_exp_f32 v46, v46
v_exp_f32 v47, v47
v_cvt_pk_bf16_f32 v14, v44, v45
v_exp_f32 v48, v48
v_cvt_pk_bf16_f32 v15, v46, v47
v_exp_f32 v49, v49
v_exp_f32 v50, v50
v_mfma_f32_32x32x16_bf16 a[128:143], v[188:191], v[12:15], a[128:143]
v_exp_f32 v51, v51
v_cvt_pk_bf16_f32 v8, v48, v49
s_add_u32 s87, s75, 98304
buffer_load_dwordx4 a[228:231], v3, s[20:23], s87 offen offset:0 sc1
v_exp_f32 v52, v52
v_exp_f32 v53, v53
v_cvt_pk_bf16_f32 v9, v50, v51
v_exp_f32 v54, v54
v_mfma_f32_32x32x16_bf16 v[32:47], v[172:175], a[200:203], 0
v_exp_f32 v55, v55
v_cvt_pk_bf16_f32 v10, v52, v53
)ASM"
R"ASM(v_exp_f32 v56, v56
v_exp_f32 v57, v57
v_cvt_pk_bf16_f32 v11, v54, v55
v_exp_f32 v58, v58
v_exp_f32 v59, v59
v_mfma_f32_32x32x16_bf16 a[144:159], v[180:183], v[8:11], a[144:159]
v_cvt_pk_bf16_f32 v12, v56, v57
v_exp_f32 v60, v60
v_exp_f32 v61, v61
v_cvt_pk_bf16_f32 v13, v58, v59
v_exp_f32 v62, v62
v_exp_f32 v63, v63
v_cvt_pk_bf16_f32 v14, v60, v61
v_exp_f32 v32, v32
v_cvt_pk_bf16_f32 v15, v62, v63
v_exp_f32 v33, v33
v_exp_f32 v34, v34
v_mfma_f32_32x32x16_bf16 a[144:159], v[188:191], v[12:15], a[144:159]
v_exp_f32 v35, v35
v_cvt_pk_bf16_f32 v8, v32, v33
buffer_load_dwordx4 a[60:63], v3, s[20:23], s81 offen offset:3072 sc1
v_exp_f32 v36, v36
v_exp_f32 v37, v37
v_cvt_pk_bf16_f32 v9, v34, v35
v_exp_f32 v38, v38
v_mfma_f32_32x32x16_bf16 v[48:63], v[172:175], a[204:207], 0
v_exp_f32 v39, v39
v_cvt_pk_bf16_f32 v10, v36, v37
v_exp_f32 v40, v40
v_exp_f32 v41, v41
v_cvt_pk_bf16_f32 v11, v38, v39
v_exp_f32 v42, v42
)ASM"
R"ASM(v_exp_f32 v43, v43
v_mfma_f32_32x32x16_bf16 a[160:175], v[180:183], v[8:11], a[160:175]
v_cvt_pk_bf16_f32 v12, v40, v41
v_exp_f32 v44, v44
v_exp_f32 v45, v45
v_cvt_pk_bf16_f32 v13, v42, v43
v_exp_f32 v46, v46
v_exp_f32 v47, v47
v_cvt_pk_bf16_f32 v14, v44, v45
v_exp_f32 v48, v48
v_cvt_pk_bf16_f32 v15, v46, v47
v_exp_f32 v49, v49
v_exp_f32 v50, v50
v_mfma_f32_32x32x16_bf16 a[160:175], v[188:191], v[12:15], a[160:175]
v_exp_f32 v51, v51
v_cvt_pk_bf16_f32 v8, v48, v49
buffer_load_dwordx4 a[232:235], v3, s[20:23], s86 offen offset:2048 sc1
v_exp_f32 v52, v52
v_exp_f32 v53, v53
v_cvt_pk_bf16_f32 v9, v50, v51
v_exp_f32 v54, v54
s_waitcnt lgkmcnt(0)
v_mfma_f32_32x32x16_bf16 v[32:47], v[64:67], a[192:195], 0
v_exp_f32 v55, v55
v_cvt_pk_bf16_f32 v10, v52, v53
v_exp_f32 v56, v56
v_exp_f32 v57, v57
v_cvt_pk_bf16_f32 v11, v54, v55
v_exp_f32 v58, v58
v_exp_f32 v59, v59
v_mfma_f32_32x32x16_bf16 a[176:191], v[180:183], v[8:11], a[176:191]
v_cvt_pk_bf16_f32 v12, v56, v57
)ASM"
R"ASM(v_exp_f32 v60, v60
v_exp_f32 v61, v61
v_cvt_pk_bf16_f32 v13, v58, v59
v_exp_f32 v62, v62
v_exp_f32 v63, v63
v_cvt_pk_bf16_f32 v14, v60, v61
v_exp_f32 v32, v32
v_cvt_pk_bf16_f32 v15, v62, v63
v_exp_f32 v33, v33
v_exp_f32 v34, v34
v_mfma_f32_32x32x16_bf16 a[176:191], v[188:191], v[12:15], a[176:191]
v_exp_f32 v35, v35
ds_read_b128 v[188:191], v29 offset:30624
v_cvt_pk_bf16_f32 v8, v32, v33
buffer_load_dwordx4 a[124:127], v3, s[20:23], s82 offen offset:3072 sc1
v_exp_f32 v36, v36
v_exp_f32 v37, v37
ds_read_b128 v[180:183], v28 offset:30624
v_cvt_pk_bf16_f32 v9, v34, v35
v_exp_f32 v38, v38
v_mfma_f32_32x32x16_bf16 v[48:63], v[64:67], a[196:199], 0
v_exp_f32 v39, v39
v_cvt_pk_bf16_f32 v10, v36, v37
ds_read_b128 v[172:175], v27 offset:29696
v_exp_f32 v40, v40
v_exp_f32 v41, v41
v_cvt_pk_bf16_f32 v11, v38, v39
v_exp_f32 v42, v42
v_exp_f32 v43, v43
v_mfma_f32_32x32x16_bf16 a[128:143], v[176:179], v[8:11], a[128:143]
v_cvt_pk_bf16_f32 v12, v40, v41
v_exp_f32 v44, v44
)ASM"
R"ASM(v_exp_f32 v45, v45
v_cvt_pk_bf16_f32 v13, v42, v43
v_exp_f32 v46, v46
v_exp_f32 v47, v47
v_cvt_pk_bf16_f32 v14, v44, v45
v_exp_f32 v48, v48
v_cvt_pk_bf16_f32 v15, v46, v47
v_exp_f32 v49, v49
v_exp_f32 v50, v50
v_mfma_f32_32x32x16_bf16 a[128:143], v[184:187], v[12:15], a[128:143]
v_exp_f32 v51, v51
v_cvt_pk_bf16_f32 v8, v48, v49
buffer_load_dwordx4 a[236:239], v3, s[20:23], s87 offen offset:2048 sc1
v_exp_f32 v52, v52
v_exp_f32 v53, v53
v_cvt_pk_bf16_f32 v9, v50, v51
v_exp_f32 v54, v54
v_mfma_f32_32x32x16_bf16 v[32:47], v[64:67], a[200:203], 0
v_exp_f32 v55, v55
v_cvt_pk_bf16_f32 v10, v52, v53
v_exp_f32 v56, v56
v_exp_f32 v57, v57
v_cvt_pk_bf16_f32 v11, v54, v55
v_exp_f32 v58, v58
v_exp_f32 v59, v59
v_mfma_f32_32x32x16_bf16 a[144:159], v[176:179], v[8:11], a[144:159]
v_cvt_pk_bf16_f32 v12, v56, v57
v_exp_f32 v60, v60
v_exp_f32 v61, v61
v_cvt_pk_bf16_f32 v13, v58, v59
v_exp_f32 v62, v62
v_exp_f32 v63, v63
)ASM"
R"ASM(v_cvt_pk_bf16_f32 v14, v60, v61
v_exp_f32 v32, v32
v_cvt_pk_bf16_f32 v15, v62, v63
v_exp_f32 v33, v33
v_exp_f32 v34, v34
v_mfma_f32_32x32x16_bf16 a[144:159], v[184:187], v[12:15], a[144:159]
v_exp_f32 v35, v35
v_cvt_pk_bf16_f32 v8, v32, v33
v_exp_f32 v36, v36
v_exp_f32 v37, v37
v_cvt_pk_bf16_f32 v9, v34, v35
v_exp_f32 v38, v38
v_mfma_f32_32x32x16_bf16 v[48:63], v[64:67], a[204:207], 0
v_exp_f32 v39, v39
v_cvt_pk_bf16_f32 v10, v36, v37
v_exp_f32 v40, v40
v_exp_f32 v41, v41
v_cvt_pk_bf16_f32 v11, v38, v39
v_exp_f32 v42, v42
v_exp_f32 v43, v43
v_mfma_f32_32x32x16_bf16 a[160:175], v[176:179], v[8:11], a[160:175]
v_cvt_pk_bf16_f32 v12, v40, v41
v_exp_f32 v44, v44
v_exp_f32 v45, v45
v_cvt_pk_bf16_f32 v13, v42, v43
v_exp_f32 v46, v46
v_exp_f32 v47, v47
v_cvt_pk_bf16_f32 v14, v44, v45
v_exp_f32 v48, v48
v_cvt_pk_bf16_f32 v15, v46, v47
v_exp_f32 v49, v49
v_exp_f32 v50, v50
)ASM"
R"ASM(v_mfma_f32_32x32x16_bf16 a[160:175], v[184:187], v[12:15], a[160:175]
v_exp_f32 v51, v51
v_cvt_pk_bf16_f32 v8, v48, v49
s_add_u32 s86, s74, 102400
buffer_load_dwordx4 a[240:243], v3, s[20:23], s86 offen offset:0 sc1
v_exp_f32 v52, v52
v_exp_f32 v53, v53
v_cvt_pk_bf16_f32 v9, v50, v51
v_exp_f32 v54, v54
s_waitcnt lgkmcnt(0)
v_mfma_f32_32x32x16_bf16 v[32:47], v[172:175], a[192:195], 0
v_exp_f32 v55, v55
v_cvt_pk_bf16_f32 v10, v52, v53
v_exp_f32 v56, v56
v_exp_f32 v57, v57
v_cvt_pk_bf16_f32 v11, v54, v55
v_exp_f32 v58, v58
v_exp_f32 v59, v59
v_mfma_f32_32x32x16_bf16 a[176:191], v[176:179], v[8:11], a[176:191]
v_cvt_pk_bf16_f32 v12, v56, v57
v_exp_f32 v60, v60
v_exp_f32 v61, v61
v_cvt_pk_bf16_f32 v13, v58, v59
v_exp_f32 v62, v62
v_exp_f32 v63, v63
v_cvt_pk_bf16_f32 v14, v60, v61
v_exp_f32 v32, v32
v_cvt_pk_bf16_f32 v15, v62, v63
v_exp_f32 v33, v33
v_exp_f32 v34, v34
v_mfma_f32_32x32x16_bf16 a[176:191], v[184:187], v[12:15], a[176:191]
v_exp_f32 v35, v35
)ASM"
R"ASM(ds_read_b128 v[184:187], v29 offset:31680
v_cvt_pk_bf16_f32 v8, v32, v33
v_exp_f32 v36, v36
v_exp_f32 v37, v37
ds_read_b128 v[176:179], v28 offset:31680
v_cvt_pk_bf16_f32 v9, v34, v35
v_exp_f32 v38, v38
v_mfma_f32_32x32x16_bf16 v[48:63], v[172:175], a[196:199], 0
v_exp_f32 v39, v39
v_cvt_pk_bf16_f32 v10, v36, v37
ds_read_b128 v[64:67], v27 offset:30720
v_exp_f32 v40, v40
v_lshlrev_b32 v30, 10, v4
v_lshl_add_u32 v30, v2, 4, v30
s_lshl_b32 s83, s59, 15
s_lshl_b32 s84, s15, 7
s_add_u32 s83, s83, s84
s_lshl_b32 s84, s101, 6
s_add_u32 s60, s83, s84
buffer_load_dwordx4 v[164:167], v30, s[24:27], s60 offen offset:0
buffer_load_dwordx4 v[168:171], v30, s[24:27], s60 offen offset:32
v_exp_f32 v41, v41
v_cvt_pk_bf16_f32 v11, v38, v39
v_exp_f32 v42, v42
v_exp_f32 v43, v43
v_mfma_f32_32x32x16_bf16 a[128:143], v[180:183], v[8:11], a[128:143]
v_cvt_pk_bf16_f32 v12, v40, v41
v_exp_f32 v44, v44
v_exp_f32 v45, v45
v_cvt_pk_bf16_f32 v13, v42, v43
v_exp_f32 v46, v46
v_exp_f32 v47, v47
)ASM"
R"ASM(v_cvt_pk_bf16_f32 v14, v44, v45
v_exp_f32 v48, v48
v_cvt_pk_bf16_f32 v15, v46, v47
v_exp_f32 v49, v49
v_exp_f32 v50, v50
v_mfma_f32_32x32x16_bf16 a[128:143], v[188:191], v[12:15], a[128:143]
v_exp_f32 v51, v51
v_cvt_pk_bf16_f32 v8, v48, v49
s_add_u32 s87, s75, 102400
buffer_load_dwordx4 a[244:247], v3, s[20:23], s87 offen offset:0 sc1
v_exp_f32 v52, v52
v_exp_f32 v53, v53
v_cvt_pk_bf16_f32 v9, v50, v51
v_exp_f32 v54, v54
v_mfma_f32_32x32x16_bf16 v[32:47], v[172:175], a[200:203], 0
v_exp_f32 v55, v55
v_cvt_pk_bf16_f32 v10, v52, v53
v_exp_f32 v56, v56
v_exp_f32 v57, v57
v_cvt_pk_bf16_f32 v11, v54, v55
v_exp_f32 v58, v58
v_exp_f32 v59, v59
v_mfma_f32_32x32x16_bf16 a[144:159], v[180:183], v[8:11], a[144:159]
v_cvt_pk_bf16_f32 v12, v56, v57
v_exp_f32 v60, v60
v_exp_f32 v61, v61
v_cvt_pk_bf16_f32 v13, v58, v59
v_exp_f32 v62, v62
v_exp_f32 v63, v63
v_cvt_pk_bf16_f32 v14, v60, v61
v_exp_f32 v32, v32
v_cvt_pk_bf16_f32 v15, v62, v63
)ASM"
R"ASM(v_exp_f32 v33, v33
v_exp_f32 v34, v34
v_mfma_f32_32x32x16_bf16 a[144:159], v[188:191], v[12:15], a[144:159]
v_exp_f32 v35, v35
v_cvt_pk_bf16_f32 v8, v32, v33
v_exp_f32 v36, v36
v_exp_f32 v37, v37
v_cvt_pk_bf16_f32 v9, v34, v35
v_exp_f32 v38, v38
v_mfma_f32_32x32x16_bf16 v[48:63], v[172:175], a[204:207], 0
v_exp_f32 v39, v39
v_cvt_pk_bf16_f32 v10, v36, v37
v_exp_f32 v40, v40
v_exp_f32 v41, v41
v_cvt_pk_bf16_f32 v11, v38, v39
v_exp_f32 v42, v42
v_exp_f32 v43, v43
v_mfma_f32_32x32x16_bf16 a[160:175], v[180:183], v[8:11], a[160:175]
v_cvt_pk_bf16_f32 v12, v40, v41
v_exp_f32 v44, v44
v_exp_f32 v45, v45
v_cvt_pk_bf16_f32 v13, v42, v43
v_exp_f32 v46, v46
v_exp_f32 v47, v47
v_cvt_pk_bf16_f32 v14, v44, v45
v_exp_f32 v48, v48
v_cvt_pk_bf16_f32 v15, v46, v47
v_exp_f32 v49, v49
v_exp_f32 v50, v50
v_mfma_f32_32x32x16_bf16 a[160:175], v[188:191], v[12:15], a[160:175]
v_exp_f32 v51, v51
v_cvt_pk_bf16_f32 v8, v48, v49
)ASM"
R"ASM(buffer_load_dwordx4 a[248:251], v3, s[20:23], s86 offen offset:2048 sc1
v_exp_f32 v52, v52
v_exp_f32 v53, v53
v_cvt_pk_bf16_f32 v9, v50, v51
v_exp_f32 v54, v54
s_waitcnt lgkmcnt(0)
v_mfma_f32_32x32x16_bf16 v[32:47], v[64:67], a[192:195], 0
v_exp_f32 v55, v55
v_cvt_pk_bf16_f32 v10, v52, v53
v_exp_f32 v56, v56
v_exp_f32 v57, v57
v_cvt_pk_bf16_f32 v11, v54, v55
v_exp_f32 v58, v58
v_exp_f32 v59, v59
v_mfma_f32_32x32x16_bf16 a[176:191], v[180:183], v[8:11], a[176:191]
v_cvt_pk_bf16_f32 v12, v56, v57
v_exp_f32 v60, v60
v_exp_f32 v61, v61
v_cvt_pk_bf16_f32 v13, v58, v59
v_exp_f32 v62, v62
v_exp_f32 v63, v63
v_cvt_pk_bf16_f32 v14, v60, v61
v_exp_f32 v32, v32
v_cvt_pk_bf16_f32 v15, v62, v63
v_exp_f32 v33, v33
v_exp_f32 v34, v34
v_mfma_f32_32x32x16_bf16 a[176:191], v[188:191], v[12:15], a[176:191]
v_exp_f32 v35, v35
ds_read_b128 v[188:191], v29 offset:32736
v_cvt_pk_bf16_f32 v8, v32, v33
v_exp_f32 v36, v36
v_exp_f32 v37, v37
)ASM"
R"ASM(ds_read_b128 v[180:183], v28 offset:32736
v_cvt_pk_bf16_f32 v9, v34, v35
v_exp_f32 v38, v38
v_mfma_f32_32x32x16_bf16 v[48:63], v[64:67], a[196:199], 0
v_exp_f32 v39, v39
v_cvt_pk_bf16_f32 v10, v36, v37
ds_read_b128 v[172:175], v27 offset:31744
v_exp_f32 v40, v40
v_exp_f32 v41, v41
v_cvt_pk_bf16_f32 v11, v38, v39
v_exp_f32 v42, v42
v_exp_f32 v43, v43
v_mfma_f32_32x32x16_bf16 a[128:143], v[176:179], v[8:11], a[128:143]
v_cvt_pk_bf16_f32 v12, v40, v41
v_exp_f32 v44, v44
v_exp_f32 v45, v45
v_cvt_pk_bf16_f32 v13, v42, v43
v_exp_f32 v46, v46
v_exp_f32 v47, v47
v_cvt_pk_bf16_f32 v14, v44, v45
v_exp_f32 v48, v48
v_cvt_pk_bf16_f32 v15, v46, v47
v_exp_f32 v49, v49
v_exp_f32 v50, v50
v_mfma_f32_32x32x16_bf16 a[128:143], v[184:187], v[12:15], a[128:143]
v_exp_f32 v51, v51
v_cvt_pk_bf16_f32 v8, v48, v49
buffer_load_dwordx4 a[252:255], v3, s[20:23], s87 offen offset:2048 sc1
v_exp_f32 v52, v52
v_exp_f32 v53, v53
v_cvt_pk_bf16_f32 v9, v50, v51
v_exp_f32 v54, v54
)ASM"
R"ASM(v_mfma_f32_32x32x16_bf16 v[32:47], v[64:67], a[200:203], 0
v_exp_f32 v55, v55
v_cvt_pk_bf16_f32 v10, v52, v53
v_exp_f32 v56, v56
v_exp_f32 v57, v57
v_cvt_pk_bf16_f32 v11, v54, v55
v_exp_f32 v58, v58
v_exp_f32 v59, v59
v_mfma_f32_32x32x16_bf16 a[144:159], v[176:179], v[8:11], a[144:159]
v_cvt_pk_bf16_f32 v12, v56, v57
v_exp_f32 v60, v60
v_exp_f32 v61, v61
v_cvt_pk_bf16_f32 v13, v58, v59
v_exp_f32 v62, v62
v_exp_f32 v63, v63
v_cvt_pk_bf16_f32 v14, v60, v61
v_exp_f32 v32, v32
v_cvt_pk_bf16_f32 v15, v62, v63
v_exp_f32 v33, v33
v_exp_f32 v34, v34
v_mfma_f32_32x32x16_bf16 a[144:159], v[184:187], v[12:15], a[144:159]
v_exp_f32 v35, v35
v_cvt_pk_bf16_f32 v8, v32, v33
v_exp_f32 v36, v36
v_exp_f32 v37, v37
v_cvt_pk_bf16_f32 v9, v34, v35
v_exp_f32 v38, v38
v_mfma_f32_32x32x16_bf16 v[48:63], v[64:67], a[204:207], 0
v_exp_f32 v39, v39
v_cvt_pk_bf16_f32 v10, v36, v37
v_exp_f32 v40, v40
v_exp_f32 v41, v41
)ASM"
R"ASM(v_cvt_pk_bf16_f32 v11, v38, v39
v_exp_f32 v42, v42
v_exp_f32 v43, v43
v_mfma_f32_32x32x16_bf16 a[160:175], v[176:179], v[8:11], a[160:175]
v_cvt_pk_bf16_f32 v12, v40, v41
v_exp_f32 v44, v44
v_exp_f32 v45, v45
v_cvt_pk_bf16_f32 v13, v42, v43
v_exp_f32 v46, v46
v_exp_f32 v47, v47
v_cvt_pk_bf16_f32 v14, v44, v45
v_exp_f32 v48, v48
v_cvt_pk_bf16_f32 v15, v46, v47
v_exp_f32 v49, v49
v_exp_f32 v50, v50
v_mfma_f32_32x32x16_bf16 a[160:175], v[184:187], v[12:15], a[160:175]
v_exp_f32 v51, v51
v_cvt_pk_bf16_f32 v8, v48, v49
v_exp_f32 v52, v52
v_exp_f32 v53, v53
v_cvt_pk_bf16_f32 v9, v50, v51
v_exp_f32 v54, v54
s_waitcnt lgkmcnt(0)
v_mfma_f32_32x32x16_bf16 v[32:47], v[172:175], a[192:195], 0
v_exp_f32 v55, v55
v_cvt_pk_bf16_f32 v10, v52, v53
v_exp_f32 v56, v56
v_exp_f32 v57, v57
v_cvt_pk_bf16_f32 v11, v54, v55
v_exp_f32 v58, v58
v_exp_f32 v59, v59
v_mfma_f32_32x32x16_bf16 a[176:191], v[176:179], v[8:11], a[176:191]
)ASM"
R"ASM(v_cvt_pk_bf16_f32 v12, v56, v57
v_exp_f32 v60, v60
v_exp_f32 v61, v61
v_cvt_pk_bf16_f32 v13, v58, v59
v_exp_f32 v62, v62
v_exp_f32 v63, v63
v_cvt_pk_bf16_f32 v14, v60, v61
v_exp_f32 v32, v32
v_cvt_pk_bf16_f32 v15, v62, v63
v_exp_f32 v33, v33
v_exp_f32 v34, v34
v_mfma_f32_32x32x16_bf16 a[176:191], v[184:187], v[12:15], a[176:191]
v_exp_f32 v35, v35
v_cvt_pk_bf16_f32 v8, v32, v33
v_lshrrev_b32 v24, 5, v0
v_min_u32 v24, 6, v24
v_lshlrev_b32 v25, 10, v24
v_lshl_add_u32 v25, v4, 3, v25
s_mul_i32 s81, s17, 7168
s_add_u32 s81, s81, 0x12c0000
buffer_load_dwordx2 v[16:17], v25, s[20:23], s81 offen offset:0 sc1
buffer_load_dwordx2 v[18:19], v25, s[20:23], s81 offen offset:256 sc1
buffer_load_dwordx2 v[20:21], v25, s[20:23], s81 offen offset:512 sc1
buffer_load_dwordx2 v[22:23], v25, s[20:23], s81 offen offset:768 sc1
v_exp_f32 v36, v36
v_exp_f32 v37, v37
v_cvt_pk_bf16_f32 v9, v34, v35
v_exp_f32 v38, v38
v_mfma_f32_32x32x16_bf16 v[48:63], v[172:175], a[196:199], 0
v_exp_f32 v39, v39
v_cvt_pk_bf16_f32 v10, v36, v37
v_exp_f32 v40, v40
)ASM"
R"ASM(v_exp_f32 v41, v41
v_cvt_pk_bf16_f32 v11, v38, v39
v_exp_f32 v42, v42
v_exp_f32 v43, v43
v_mfma_f32_32x32x16_bf16 a[128:143], v[180:183], v[8:11], a[128:143]
v_cvt_pk_bf16_f32 v12, v40, v41
v_exp_f32 v44, v44
v_exp_f32 v45, v45
v_cvt_pk_bf16_f32 v13, v42, v43
v_exp_f32 v46, v46
v_exp_f32 v47, v47
v_cvt_pk_bf16_f32 v14, v44, v45
v_exp_f32 v48, v48
v_cvt_pk_bf16_f32 v15, v46, v47
v_exp_f32 v49, v49
v_exp_f32 v50, v50
v_mfma_f32_32x32x16_bf16 a[128:143], v[188:191], v[12:15], a[128:143]
v_exp_f32 v51, v51
v_cvt_pk_bf16_f32 v8, v48, v49
v_exp_f32 v52, v52
v_exp_f32 v53, v53
v_cvt_pk_bf16_f32 v9, v50, v51
v_exp_f32 v54, v54
v_mfma_f32_32x32x16_bf16 v[32:47], v[172:175], a[200:203], 0
v_exp_f32 v55, v55
v_cvt_pk_bf16_f32 v10, v52, v53
v_exp_f32 v56, v56
v_exp_f32 v57, v57
v_cvt_pk_bf16_f32 v11, v54, v55
v_exp_f32 v58, v58
v_exp_f32 v59, v59
v_mfma_f32_32x32x16_bf16 a[144:159], v[180:183], v[8:11], a[144:159]
)ASM"
R"ASM(v_cvt_pk_bf16_f32 v12, v56, v57
v_exp_f32 v60, v60
v_exp_f32 v61, v61
v_cvt_pk_bf16_f32 v13, v58, v59
v_exp_f32 v62, v62
v_exp_f32 v63, v63
v_cvt_pk_bf16_f32 v14, v60, v61
v_exp_f32 v32, v32
v_cvt_pk_bf16_f32 v15, v62, v63
v_exp_f32 v33, v33
v_exp_f32 v34, v34
v_mfma_f32_32x32x16_bf16 a[144:159], v[188:191], v[12:15], a[144:159]
v_exp_f32 v35, v35
v_cvt_pk_bf16_f32 v8, v32, v33
v_exp_f32 v36, v36
v_exp_f32 v37, v37
v_cvt_pk_bf16_f32 v9, v34, v35
v_exp_f32 v38, v38
v_mfma_f32_32x32x16_bf16 v[48:63], v[172:175], a[204:207], 0
v_exp_f32 v39, v39
v_cvt_pk_bf16_f32 v10, v36, v37
v_exp_f32 v40, v40
v_exp_f32 v41, v41
v_cvt_pk_bf16_f32 v11, v38, v39
v_exp_f32 v42, v42
v_exp_f32 v43, v43
v_mfma_f32_32x32x16_bf16 a[160:175], v[180:183], v[8:11], a[160:175]
v_cvt_pk_bf16_f32 v12, v40, v41
v_exp_f32 v44, v44
v_exp_f32 v45, v45
v_cvt_pk_bf16_f32 v13, v42, v43
v_exp_f32 v46, v46
)ASM"
R"ASM(v_exp_f32 v47, v47
v_cvt_pk_bf16_f32 v14, v44, v45
v_exp_f32 v48, v48
v_cvt_pk_bf16_f32 v15, v46, v47
v_exp_f32 v49, v49
v_exp_f32 v50, v50
v_mfma_f32_32x32x16_bf16 a[160:175], v[188:191], v[12:15], a[160:175]
v_exp_f32 v51, v51
v_cvt_pk_bf16_f32 v8, v48, v49
v_exp_f32 v52, v52
v_exp_f32 v53, v53
v_cvt_pk_bf16_f32 v9, v50, v51
v_exp_f32 v54, v54
v_exp_f32 v55, v55
v_cvt_pk_bf16_f32 v10, v52, v53
v_exp_f32 v56, v56
v_exp_f32 v57, v57
v_cvt_pk_bf16_f32 v11, v54, v55
v_exp_f32 v58, v58
v_exp_f32 v59, v59
v_mfma_f32_32x32x16_bf16 a[176:191], v[180:183], v[8:11], a[176:191]
v_cvt_pk_bf16_f32 v12, v56, v57
v_exp_f32 v60, v60
v_exp_f32 v61, v61
v_cvt_pk_bf16_f32 v13, v58, v59
v_exp_f32 v62, v62
v_exp_f32 v63, v63
v_cvt_pk_bf16_f32 v14, v60, v61
v_cvt_pk_bf16_f32 v15, v62, v63
s_nop 1
v_mfma_f32_32x32x16_bf16 a[176:191], v[188:191], v[12:15], a[176:191]
s_mov_b32 s89, 0
)ASM"
R"ASM(s_mul_i32 s80, s13, 4608
s_add_u32 s80, s80, 145408
v_add_u32 v8, s80, v3
v_lshlrev_b32 v10, 2, v1
v_add_u32 v11, s80, v10
s_xor_b32 s81, s13, 2
s_mul_i32 s81, s81, 4608
s_add_u32 s81, s81, 145408
v_add_u32 v9, s81, v3
v_add_u32 v10, s81, v10
Lcomb_28_%=:
ds_write_b128 v8, a[160:163] offset:0
ds_write_b128 v8, a[164:167] offset:1024
ds_write_b32 v11, a168 offset:4096
ds_write_b128 v8, a[176:179] offset:2048
ds_write_b128 v8, a[180:183] offset:3072
ds_write_b32 v11, a184 offset:4352
v_accvgpr_read_b32 v52, a128
v_accvgpr_read_b32 v53, a129
v_accvgpr_read_b32 v54, a130
v_accvgpr_read_b32 v55, a131
v_accvgpr_read_b32 v56, a132
v_accvgpr_read_b32 v57, a133
v_accvgpr_read_b32 v58, a134
v_accvgpr_read_b32 v59, a135
v_accvgpr_read_b32 v60, a136
v_accvgpr_read_b32 v172, a144
v_accvgpr_read_b32 v173, a145
v_accvgpr_read_b32 v174, a146
v_accvgpr_read_b32 v175, a147
v_accvgpr_read_b32 v176, a148
v_accvgpr_read_b32 v177, a149
)ASM"
R"ASM(v_accvgpr_read_b32 v178, a150
v_accvgpr_read_b32 v179, a151
v_accvgpr_read_b32 v180, a152
s_waitcnt lgkmcnt(0)
s_barrier
ds_read_b128 v[32:35], v9 offset:0
ds_read_b128 v[36:39], v9 offset:1024
ds_read_b32 v40, v10 offset:4096
ds_read_b128 v[42:45], v9 offset:2048
ds_read_b128 v[46:49], v9 offset:3072
ds_read_b32 v50, v10 offset:4352
s_waitcnt lgkmcnt(5)
v_pk_add_f32 v[52:53], v[52:53], v[32:33]
v_pk_add_f32 v[54:55], v[54:55], v[34:35]
s_waitcnt lgkmcnt(4)
v_pk_add_f32 v[56:57], v[56:57], v[36:37]
v_pk_add_f32 v[58:59], v[58:59], v[38:39]
s_waitcnt lgkmcnt(3)
v_add_f32 v60, v60, v40
s_waitcnt lgkmcnt(2)
v_pk_add_f32 v[172:173], v[172:173], v[42:43]
v_pk_add_f32 v[174:175], v[174:175], v[44:45]
s_waitcnt lgkmcnt(1)
v_pk_add_f32 v[176:177], v[176:177], v[46:47]
v_pk_add_f32 v[178:179], v[178:179], v[48:49]
s_waitcnt lgkmcnt(0)
v_add_f32 v180, v180, v50
v_add_f32_e64 v12, |v52|, |v53|
v_add_f32_e64 v12, v12, |v54|
v_add_f32_e64 v12, v12, |v55|
v_add_f32_e64 v12, v12, |v56|
v_add_f32_e64 v12, v12, |v57|
)ASM"
R"ASM(v_add_f32_e64 v12, v12, |v58|
v_add_f32_e64 v12, v12, |v59|
v_rcp_f32 v14, v60
v_add_f32_e64 v13, |v172|, |v173|
v_add_f32_e64 v13, v13, |v174|
v_add_f32_e64 v13, v13, |v175|
v_add_f32_e64 v13, v13, |v176|
v_add_f32_e64 v13, v13, |v177|
v_add_f32_e64 v13, v13, |v178|
v_add_f32_e64 v13, v13, |v179|
v_rcp_f32 v15, v180
v_cmp_le_f32 vcc, 0x17800000, v60
s_mov_b64 s[82:83], vcc
v_cmp_ge_f32 vcc, 0x71800000, v60
s_and_b64 s[82:83], s[82:83], vcc
v_cmp_le_f32 vcc, 0x17800000, v180
s_and_b64 s[82:83], s[82:83], vcc
v_cmp_ge_f32 vcc, 0x71800000, v180
s_and_b64 s[82:83], s[82:83], vcc
v_cmp_ge_f32 vcc, 0x7f61b1e6, v12
s_and_b64 s[82:83], s[82:83], vcc
v_cmp_ge_f32 vcc, 0x7f61b1e6, v13
s_and_b64 s[82:83], s[82:83], vcc
v_pk_mul_f32 v[52:53], v[52:53], v[14:15] op_sel_hi:[1,0]
v_pk_mul_f32 v[54:55], v[54:55], v[14:15] op_sel_hi:[1,0]
v_pk_mul_f32 v[56:57], v[56:57], v[14:15] op_sel_hi:[1,0]
v_pk_mul_f32 v[58:59], v[58:59], v[14:15] op_sel_hi:[1,0]
v_pk_mul_f32 v[172:173], v[172:173], v[14:15] op_sel:[0,1] op_sel_hi:[1,1]
v_pk_mul_f32 v[174:175], v[174:175], v[14:15] op_sel:[0,1] op_sel_hi:[1,1]
v_pk_mul_f32 v[176:177], v[176:177], v[14:15] op_sel:[0,1] op_sel_hi:[1,1]
v_pk_mul_f32 v[178:179], v[178:179], v[14:15] op_sel:[0,1] op_sel_hi:[1,1]
v_cvt_pk_bf16_f32 v32, v52, v53
)ASM"
R"ASM(v_cvt_pk_bf16_f32 v33, v54, v55
v_cvt_pk_bf16_f32 v34, v56, v57
v_cvt_pk_bf16_f32 v35, v58, v59
v_cvt_pk_bf16_f32 v42, v172, v173
v_cvt_pk_bf16_f32 v43, v174, v175
v_cvt_pk_bf16_f32 v44, v176, v177
v_cvt_pk_bf16_f32 v45, v178, v179
s_cmp_eq_u64 s[82:83], -1
s_cselect_b32 s81, 1, 0
s_or_b32 s81, s81, s89
s_andn2_b32 s80, s81, s46
s_cbranch_scc0 Lnost_29_%=
s_add_u32 s80, s47, 0x100000
s_mov_b32 s46, 1
buffer_store_dwordx4 v[32:35], v3, s[20:23], s80 offen offset:0 sc1
buffer_store_dwordx4 v[42:45], v3, s[20:23], s80 offen offset:1024 sc1
Lnost_29_%=:
v_mov_b32 v62, s81
s_lshl_b32 s80, s13, 2
s_add_u32 s80, s80, 140288
v_mov_b32 v63, s80
ds_write_b32 v63, v62
s_waitcnt lgkmcnt(0)
s_barrier
v_mov_b32 v63, 140288
ds_read_b128 v[64:67], v63
s_waitcnt lgkmcnt(0)
v_and_b32 v64, v64, v65
v_and_b32 v66, v66, v67
v_and_b32 v64, v64, v66
s_nop 0
v_readfirstlane_b32 s80, v64
)ASM"
R"ASM(s_cmp_lg_u32 s80, 0
s_cbranch_scc1 Lcombok_30_%=
v_mov_b32 v189, v27
v_mov_b32 v190, v28
v_mov_b32 v191, v29
v_mov_b32 v64, 0xff61b1e6
v_mov_b32 v172, 0
v_mov_b32 v173, 0
v_mov_b32 v174, 0
v_mov_b32 v175, 0
v_mov_b32 v176, 0
v_mov_b32 v177, 0
v_mov_b32 v178, 0
v_mov_b32 v179, 0
v_mov_b32 v180, 0
v_mov_b32 v181, 0
v_mov_b32 v182, 0
v_mov_b32 v183, 0
v_mov_b32 v184, 0
v_mov_b32 v185, 0
v_mov_b32 v186, 0
v_mov_b32 v187, 0
s_mov_b32 s80, 0
Lfb_31_%=:
ds_read_b128 v[12:15], v189
ds_read_b128 v[56:59], v190
ds_read_b128 v[60:63], v191
v_add_u32 v189, 1024, v189
v_add_u32 v190, 1056, v190
v_add_u32 v191, 1056, v191
s_waitcnt lgkmcnt(2)
v_mfma_f32_32x32x16_bf16 v[32:47], v[12:15], a[192:195], 0
)ASM"
R"ASM(s_nop 11
v_max3_f32 v26, v32, v33, v34
v_max3_f32 v26, v26, v35, v36
v_max3_f32 v26, v26, v37, v38
v_max3_f32 v26, v26, v39, v40
v_max3_f32 v26, v26, v41, v42
v_max3_f32 v26, v26, v43, v44
v_max3_f32 v26, v26, v45, v46
v_max_f32 v26, v26, v47
v_mov_b32 v31, v26
s_nop 1
v_permlane32_swap_b32 v26, v31
v_max3_f32 v25, v26, v31, v64
v_sub_f32 v188, v64, v25
v_exp_f32 v188, v188
v_mov_b32 v64, v25
v_sub_f32 v32, v32, v25
v_sub_f32 v33, v33, v25
v_sub_f32 v34, v34, v25
v_sub_f32 v35, v35, v25
v_sub_f32 v36, v36, v25
v_sub_f32 v37, v37, v25
v_sub_f32 v38, v38, v25
v_sub_f32 v39, v39, v25
v_sub_f32 v40, v40, v25
v_sub_f32 v41, v41, v25
v_sub_f32 v42, v42, v25
v_sub_f32 v43, v43, v25
v_sub_f32 v44, v44, v25
v_sub_f32 v45, v45, v25
v_sub_f32 v46, v46, v25
v_sub_f32 v47, v47, v25
)ASM"
R"ASM(v_exp_f32 v32, v32
v_exp_f32 v33, v33
v_exp_f32 v34, v34
v_exp_f32 v35, v35
v_exp_f32 v36, v36
v_exp_f32 v37, v37
v_exp_f32 v38, v38
v_exp_f32 v39, v39
v_exp_f32 v40, v40
v_exp_f32 v41, v41
v_exp_f32 v42, v42
v_exp_f32 v43, v43
v_exp_f32 v44, v44
v_exp_f32 v45, v45
v_exp_f32 v46, v46
v_exp_f32 v47, v47
v_mul_f32 v172, v172, v188
v_mul_f32 v173, v173, v188
v_mul_f32 v174, v174, v188
v_mul_f32 v175, v175, v188
v_mul_f32 v176, v176, v188
v_mul_f32 v177, v177, v188
v_mul_f32 v178, v178, v188
v_mul_f32 v179, v179, v188
v_mul_f32 v180, v180, v188
v_cvt_pk_bf16_f32 v48, v32, v33
v_cvt_pk_bf16_f32 v49, v34, v35
v_cvt_pk_bf16_f32 v50, v36, v37
v_cvt_pk_bf16_f32 v51, v38, v39
v_cvt_pk_bf16_f32 v52, v40, v41
v_cvt_pk_bf16_f32 v53, v42, v43
v_cvt_pk_bf16_f32 v54, v44, v45
)ASM"
R"ASM(v_cvt_pk_bf16_f32 v55, v46, v47
s_waitcnt lgkmcnt(1)
v_mfma_f32_32x32x16_bf16 v[172:187], v[56:59], v[48:51], v[172:187]
s_waitcnt lgkmcnt(0)
v_mfma_f32_32x32x16_bf16 v[172:187], v[60:63], v[52:55], v[172:187]
s_add_u32 s80, s80, 1
s_cmp_lt_u32 s80, 32
s_cbranch_scc1 Lfb_31_%=
s_nop 8
v_accvgpr_write_b32 a128, v172
v_accvgpr_write_b32 a129, v173
v_accvgpr_write_b32 a130, v174
v_accvgpr_write_b32 a131, v175
v_accvgpr_write_b32 a132, v176
v_accvgpr_write_b32 a133, v177
v_accvgpr_write_b32 a134, v178
v_accvgpr_write_b32 a135, v179
v_accvgpr_write_b32 a136, v180
v_mov_b32 v189, v27
v_mov_b32 v190, v28
v_mov_b32 v191, v29
v_mov_b32 v65, 0xff61b1e6
v_mov_b32 v172, 0
v_mov_b32 v173, 0
v_mov_b32 v174, 0
v_mov_b32 v175, 0
v_mov_b32 v176, 0
v_mov_b32 v177, 0
v_mov_b32 v178, 0
v_mov_b32 v179, 0
v_mov_b32 v180, 0
v_mov_b32 v181, 0
)ASM"
R"ASM(v_mov_b32 v182, 0
v_mov_b32 v183, 0
v_mov_b32 v184, 0
v_mov_b32 v185, 0
v_mov_b32 v186, 0
v_mov_b32 v187, 0
s_mov_b32 s80, 0
Lfb_32_%=:
ds_read_b128 v[12:15], v189
ds_read_b128 v[56:59], v190
ds_read_b128 v[60:63], v191
v_add_u32 v189, 1024, v189
v_add_u32 v190, 1056, v190
v_add_u32 v191, 1056, v191
s_waitcnt lgkmcnt(2)
v_mfma_f32_32x32x16_bf16 v[32:47], v[12:15], a[196:199], 0
s_nop 11
v_max3_f32 v26, v32, v33, v34
v_max3_f32 v26, v26, v35, v36
v_max3_f32 v26, v26, v37, v38
v_max3_f32 v26, v26, v39, v40
v_max3_f32 v26, v26, v41, v42
v_max3_f32 v26, v26, v43, v44
v_max3_f32 v26, v26, v45, v46
v_max_f32 v26, v26, v47
v_mov_b32 v31, v26
s_nop 1
v_permlane32_swap_b32 v26, v31
v_max3_f32 v25, v26, v31, v65
v_sub_f32 v188, v65, v25
v_exp_f32 v188, v188
v_mov_b32 v65, v25
)ASM"
R"ASM(v_sub_f32 v32, v32, v25
v_sub_f32 v33, v33, v25
v_sub_f32 v34, v34, v25
v_sub_f32 v35, v35, v25
v_sub_f32 v36, v36, v25
v_sub_f32 v37, v37, v25
v_sub_f32 v38, v38, v25
v_sub_f32 v39, v39, v25
v_sub_f32 v40, v40, v25
v_sub_f32 v41, v41, v25
v_sub_f32 v42, v42, v25
v_sub_f32 v43, v43, v25
v_sub_f32 v44, v44, v25
v_sub_f32 v45, v45, v25
v_sub_f32 v46, v46, v25
v_sub_f32 v47, v47, v25
v_exp_f32 v32, v32
v_exp_f32 v33, v33
v_exp_f32 v34, v34
v_exp_f32 v35, v35
v_exp_f32 v36, v36
v_exp_f32 v37, v37
v_exp_f32 v38, v38
v_exp_f32 v39, v39
v_exp_f32 v40, v40
v_exp_f32 v41, v41
v_exp_f32 v42, v42
v_exp_f32 v43, v43
v_exp_f32 v44, v44
v_exp_f32 v45, v45
v_exp_f32 v46, v46
v_exp_f32 v47, v47
)ASM"
R"ASM(v_mul_f32 v172, v172, v188
v_mul_f32 v173, v173, v188
v_mul_f32 v174, v174, v188
v_mul_f32 v175, v175, v188
v_mul_f32 v176, v176, v188
v_mul_f32 v177, v177, v188
v_mul_f32 v178, v178, v188
v_mul_f32 v179, v179, v188
v_mul_f32 v180, v180, v188
v_cvt_pk_bf16_f32 v48, v32, v33
v_cvt_pk_bf16_f32 v49, v34, v35
v_cvt_pk_bf16_f32 v50, v36, v37
v_cvt_pk_bf16_f32 v51, v38, v39
v_cvt_pk_bf16_f32 v52, v40, v41
v_cvt_pk_bf16_f32 v53, v42, v43
v_cvt_pk_bf16_f32 v54, v44, v45
v_cvt_pk_bf16_f32 v55, v46, v47
s_waitcnt lgkmcnt(1)
v_mfma_f32_32x32x16_bf16 v[172:187], v[56:59], v[48:51], v[172:187]
s_waitcnt lgkmcnt(0)
v_mfma_f32_32x32x16_bf16 v[172:187], v[60:63], v[52:55], v[172:187]
s_add_u32 s80, s80, 1
s_cmp_lt_u32 s80, 32
s_cbranch_scc1 Lfb_32_%=
s_nop 8
v_accvgpr_write_b32 a144, v172
v_accvgpr_write_b32 a145, v173
v_accvgpr_write_b32 a146, v174
v_accvgpr_write_b32 a147, v175
v_accvgpr_write_b32 a148, v176
v_accvgpr_write_b32 a149, v177
v_accvgpr_write_b32 a150, v178
)ASM"
R"ASM(v_accvgpr_write_b32 a151, v179
v_accvgpr_write_b32 a152, v180
v_mov_b32 v189, v27
v_mov_b32 v190, v28
v_mov_b32 v191, v29
v_mov_b32 v66, 0xff61b1e6
v_mov_b32 v172, 0
v_mov_b32 v173, 0
v_mov_b32 v174, 0
v_mov_b32 v175, 0
v_mov_b32 v176, 0
v_mov_b32 v177, 0
v_mov_b32 v178, 0
v_mov_b32 v179, 0
v_mov_b32 v180, 0
v_mov_b32 v181, 0
v_mov_b32 v182, 0
v_mov_b32 v183, 0
v_mov_b32 v184, 0
v_mov_b32 v185, 0
v_mov_b32 v186, 0
v_mov_b32 v187, 0
s_mov_b32 s80, 0
Lfb_33_%=:
ds_read_b128 v[12:15], v189
ds_read_b128 v[56:59], v190
ds_read_b128 v[60:63], v191
v_add_u32 v189, 1024, v189
v_add_u32 v190, 1056, v190
v_add_u32 v191, 1056, v191
s_waitcnt lgkmcnt(2)
v_mfma_f32_32x32x16_bf16 v[32:47], v[12:15], a[200:203], 0
)ASM"
R"ASM(s_nop 11
v_max3_f32 v26, v32, v33, v34
v_max3_f32 v26, v26, v35, v36
v_max3_f32 v26, v26, v37, v38
v_max3_f32 v26, v26, v39, v40
v_max3_f32 v26, v26, v41, v42
v_max3_f32 v26, v26, v43, v44
v_max3_f32 v26, v26, v45, v46
v_max_f32 v26, v26, v47
v_mov_b32 v31, v26
s_nop 1
v_permlane32_swap_b32 v26, v31
v_max3_f32 v25, v26, v31, v66
v_sub_f32 v188, v66, v25
v_exp_f32 v188, v188
v_mov_b32 v66, v25
v_sub_f32 v32, v32, v25
v_sub_f32 v33, v33, v25
v_sub_f32 v34, v34, v25
v_sub_f32 v35, v35, v25
v_sub_f32 v36, v36, v25
v_sub_f32 v37, v37, v25
v_sub_f32 v38, v38, v25
v_sub_f32 v39, v39, v25
v_sub_f32 v40, v40, v25
v_sub_f32 v41, v41, v25
v_sub_f32 v42, v42, v25
v_sub_f32 v43, v43, v25
v_sub_f32 v44, v44, v25
v_sub_f32 v45, v45, v25
v_sub_f32 v46, v46, v25
v_sub_f32 v47, v47, v25
)ASM"
R"ASM(v_exp_f32 v32, v32
v_exp_f32 v33, v33
v_exp_f32 v34, v34
v_exp_f32 v35, v35
v_exp_f32 v36, v36
v_exp_f32 v37, v37
v_exp_f32 v38, v38
v_exp_f32 v39, v39
v_exp_f32 v40, v40
v_exp_f32 v41, v41
v_exp_f32 v42, v42
v_exp_f32 v43, v43
v_exp_f32 v44, v44
v_exp_f32 v45, v45
v_exp_f32 v46, v46
v_exp_f32 v47, v47
v_mul_f32 v172, v172, v188
v_mul_f32 v173, v173, v188
v_mul_f32 v174, v174, v188
v_mul_f32 v175, v175, v188
v_mul_f32 v176, v176, v188
v_mul_f32 v177, v177, v188
v_mul_f32 v178, v178, v188
v_mul_f32 v179, v179, v188
v_mul_f32 v180, v180, v188
v_cvt_pk_bf16_f32 v48, v32, v33
v_cvt_pk_bf16_f32 v49, v34, v35
v_cvt_pk_bf16_f32 v50, v36, v37
v_cvt_pk_bf16_f32 v51, v38, v39
v_cvt_pk_bf16_f32 v52, v40, v41
v_cvt_pk_bf16_f32 v53, v42, v43
v_cvt_pk_bf16_f32 v54, v44, v45
)ASM"
R"ASM(v_cvt_pk_bf16_f32 v55, v46, v47
s_waitcnt lgkmcnt(1)
v_mfma_f32_32x32x16_bf16 v[172:187], v[56:59], v[48:51], v[172:187]
s_waitcnt lgkmcnt(0)
v_mfma_f32_32x32x16_bf16 v[172:187], v[60:63], v[52:55], v[172:187]
s_add_u32 s80, s80, 1
s_cmp_lt_u32 s80, 32
s_cbranch_scc1 Lfb_33_%=
s_nop 8
v_accvgpr_write_b32 a160, v172
v_accvgpr_write_b32 a161, v173
v_accvgpr_write_b32 a162, v174
v_accvgpr_write_b32 a163, v175
v_accvgpr_write_b32 a164, v176
v_accvgpr_write_b32 a165, v177
v_accvgpr_write_b32 a166, v178
v_accvgpr_write_b32 a167, v179
v_accvgpr_write_b32 a168, v180
v_mov_b32 v189, v27
v_mov_b32 v190, v28
v_mov_b32 v191, v29
v_mov_b32 v67, 0xff61b1e6
v_mov_b32 v172, 0
v_mov_b32 v173, 0
v_mov_b32 v174, 0
v_mov_b32 v175, 0
v_mov_b32 v176, 0
v_mov_b32 v177, 0
v_mov_b32 v178, 0
v_mov_b32 v179, 0
v_mov_b32 v180, 0
v_mov_b32 v181, 0
)ASM"
R"ASM(v_mov_b32 v182, 0
v_mov_b32 v183, 0
v_mov_b32 v184, 0
v_mov_b32 v185, 0
v_mov_b32 v186, 0
v_mov_b32 v187, 0
s_mov_b32 s80, 0
Lfb_34_%=:
ds_read_b128 v[12:15], v189
ds_read_b128 v[56:59], v190
ds_read_b128 v[60:63], v191
v_add_u32 v189, 1024, v189
v_add_u32 v190, 1056, v190
v_add_u32 v191, 1056, v191
s_waitcnt lgkmcnt(2)
v_mfma_f32_32x32x16_bf16 v[32:47], v[12:15], a[204:207], 0
s_nop 11
v_max3_f32 v26, v32, v33, v34
v_max3_f32 v26, v26, v35, v36
v_max3_f32 v26, v26, v37, v38
v_max3_f32 v26, v26, v39, v40
v_max3_f32 v26, v26, v41, v42
v_max3_f32 v26, v26, v43, v44
v_max3_f32 v26, v26, v45, v46
v_max_f32 v26, v26, v47
v_mov_b32 v31, v26
s_nop 1
v_permlane32_swap_b32 v26, v31
v_max3_f32 v25, v26, v31, v67
v_sub_f32 v188, v67, v25
v_exp_f32 v188, v188
v_mov_b32 v67, v25
)ASM"
R"ASM(v_sub_f32 v32, v32, v25
v_sub_f32 v33, v33, v25
v_sub_f32 v34, v34, v25
v_sub_f32 v35, v35, v25
v_sub_f32 v36, v36, v25
v_sub_f32 v37, v37, v25
v_sub_f32 v38, v38, v25
v_sub_f32 v39, v39, v25
v_sub_f32 v40, v40, v25
v_sub_f32 v41, v41, v25
v_sub_f32 v42, v42, v25
v_sub_f32 v43, v43, v25
v_sub_f32 v44, v44, v25
v_sub_f32 v45, v45, v25
v_sub_f32 v46, v46, v25
v_sub_f32 v47, v47, v25
v_exp_f32 v32, v32
v_exp_f32 v33, v33
v_exp_f32 v34, v34
v_exp_f32 v35, v35
v_exp_f32 v36, v36
v_exp_f32 v37, v37
v_exp_f32 v38, v38
v_exp_f32 v39, v39
v_exp_f32 v40, v40
v_exp_f32 v41, v41
v_exp_f32 v42, v42
v_exp_f32 v43, v43
v_exp_f32 v44, v44
v_exp_f32 v45, v45
v_exp_f32 v46, v46
v_exp_f32 v47, v47
)ASM"
R"ASM(v_mul_f32 v172, v172, v188
v_mul_f32 v173, v173, v188
v_mul_f32 v174, v174, v188
v_mul_f32 v175, v175, v188
v_mul_f32 v176, v176, v188
v_mul_f32 v177, v177, v188
v_mul_f32 v178, v178, v188
v_mul_f32 v179, v179, v188
v_mul_f32 v180, v180, v188
v_cvt_pk_bf16_f32 v48, v32, v33
v_cvt_pk_bf16_f32 v49, v34, v35
v_cvt_pk_bf16_f32 v50, v36, v37
v_cvt_pk_bf16_f32 v51, v38, v39
v_cvt_pk_bf16_f32 v52, v40, v41
v_cvt_pk_bf16_f32 v53, v42, v43
v_cvt_pk_bf16_f32 v54, v44, v45
v_cvt_pk_bf16_f32 v55, v46, v47
s_waitcnt lgkmcnt(1)
v_mfma_f32_32x32x16_bf16 v[172:187], v[56:59], v[48:51], v[172:187]
s_waitcnt lgkmcnt(0)
v_mfma_f32_32x32x16_bf16 v[172:187], v[60:63], v[52:55], v[172:187]
s_add_u32 s80, s80, 1
s_cmp_lt_u32 s80, 32
s_cbranch_scc1 Lfb_34_%=
s_nop 8
v_accvgpr_write_b32 a176, v172
v_accvgpr_write_b32 a177, v173
v_accvgpr_write_b32 a178, v174
v_accvgpr_write_b32 a179, v175
v_accvgpr_write_b32 a180, v176
v_accvgpr_write_b32 a181, v177
v_accvgpr_write_b32 a182, v178
)ASM"
R"ASM(v_accvgpr_write_b32 a183, v179
v_accvgpr_write_b32 a184, v180
ds_write_b32 v11, v64 offset:0
ds_write_b32 v11, v65 offset:256
ds_write_b32 v11, v66 offset:512
ds_write_b32 v11, v67 offset:768
s_waitcnt lgkmcnt(0)
s_barrier
ds_read_b32 v192, v10 offset:512
ds_read_b32 v193, v10 offset:768
ds_read_b32 v194, v10 offset:0
ds_read_b32 v195, v10 offset:256
s_waitcnt lgkmcnt(3)
v_max_f32 v192, v192, v64
v_sub_f32 v192, v64, v192
v_exp_f32 v192, v192
s_waitcnt lgkmcnt(2)
v_max_f32 v193, v193, v65
v_sub_f32 v193, v65, v193
v_exp_f32 v193, v193
s_waitcnt lgkmcnt(1)
v_max_f32 v194, v194, v66
v_sub_f32 v194, v66, v194
v_exp_f32 v194, v194
s_waitcnt lgkmcnt(0)
v_max_f32 v195, v195, v67
v_sub_f32 v195, v67, v195
v_exp_f32 v195, v195
v_accvgpr_read_b32 v172, a128
v_accvgpr_read_b32 v173, a129
v_accvgpr_read_b32 v174, a130
v_accvgpr_read_b32 v175, a131
)ASM"
R"ASM(v_accvgpr_read_b32 v176, a132
v_accvgpr_read_b32 v177, a133
v_accvgpr_read_b32 v178, a134
v_accvgpr_read_b32 v179, a135
v_accvgpr_read_b32 v180, a136
v_mul_f32 v172, v172, v192
v_mul_f32 v173, v173, v192
v_mul_f32 v174, v174, v192
v_mul_f32 v175, v175, v192
v_mul_f32 v176, v176, v192
v_mul_f32 v177, v177, v192
v_mul_f32 v178, v178, v192
v_mul_f32 v179, v179, v192
v_mul_f32 v180, v180, v192
v_accvgpr_write_b32 a128, v172
v_accvgpr_write_b32 a129, v173
v_accvgpr_write_b32 a130, v174
v_accvgpr_write_b32 a131, v175
v_accvgpr_write_b32 a132, v176
v_accvgpr_write_b32 a133, v177
v_accvgpr_write_b32 a134, v178
v_accvgpr_write_b32 a135, v179
v_accvgpr_write_b32 a136, v180
v_accvgpr_read_b32 v172, a144
v_accvgpr_read_b32 v173, a145
v_accvgpr_read_b32 v174, a146
v_accvgpr_read_b32 v175, a147
v_accvgpr_read_b32 v176, a148
v_accvgpr_read_b32 v177, a149
v_accvgpr_read_b32 v178, a150
v_accvgpr_read_b32 v179, a151
v_accvgpr_read_b32 v180, a152
)ASM"
R"ASM(v_mul_f32 v172, v172, v193
v_mul_f32 v173, v173, v193
v_mul_f32 v174, v174, v193
v_mul_f32 v175, v175, v193
v_mul_f32 v176, v176, v193
v_mul_f32 v177, v177, v193
v_mul_f32 v178, v178, v193
v_mul_f32 v179, v179, v193
v_mul_f32 v180, v180, v193
v_accvgpr_write_b32 a144, v172
v_accvgpr_write_b32 a145, v173
v_accvgpr_write_b32 a146, v174
v_accvgpr_write_b32 a147, v175
v_accvgpr_write_b32 a148, v176
v_accvgpr_write_b32 a149, v177
v_accvgpr_write_b32 a150, v178
v_accvgpr_write_b32 a151, v179
v_accvgpr_write_b32 a152, v180
v_accvgpr_read_b32 v172, a160
v_accvgpr_read_b32 v173, a161
v_accvgpr_read_b32 v174, a162
v_accvgpr_read_b32 v175, a163
v_accvgpr_read_b32 v176, a164
v_accvgpr_read_b32 v177, a165
v_accvgpr_read_b32 v178, a166
v_accvgpr_read_b32 v179, a167
v_accvgpr_read_b32 v180, a168
v_mul_f32 v172, v172, v194
v_mul_f32 v173, v173, v194
v_mul_f32 v174, v174, v194
v_mul_f32 v175, v175, v194
v_mul_f32 v176, v176, v194
)ASM"
R"ASM(v_mul_f32 v177, v177, v194
v_mul_f32 v178, v178, v194
v_mul_f32 v179, v179, v194
v_mul_f32 v180, v180, v194
v_accvgpr_write_b32 a160, v172
v_accvgpr_write_b32 a161, v173
v_accvgpr_write_b32 a162, v174
v_accvgpr_write_b32 a163, v175
v_accvgpr_write_b32 a164, v176
v_accvgpr_write_b32 a165, v177
v_accvgpr_write_b32 a166, v178
v_accvgpr_write_b32 a167, v179
v_accvgpr_write_b32 a168, v180
v_accvgpr_read_b32 v172, a176
v_accvgpr_read_b32 v173, a177
v_accvgpr_read_b32 v174, a178
v_accvgpr_read_b32 v175, a179
v_accvgpr_read_b32 v176, a180
v_accvgpr_read_b32 v177, a181
v_accvgpr_read_b32 v178, a182
v_accvgpr_read_b32 v179, a183
v_accvgpr_read_b32 v180, a184
v_mul_f32 v172, v172, v195
v_mul_f32 v173, v173, v195
v_mul_f32 v174, v174, v195
v_mul_f32 v175, v175, v195
v_mul_f32 v176, v176, v195
v_mul_f32 v177, v177, v195
v_mul_f32 v178, v178, v195
v_mul_f32 v179, v179, v195
v_mul_f32 v180, v180, v195
v_accvgpr_write_b32 a176, v172
)ASM"
R"ASM(v_accvgpr_write_b32 a177, v173
v_accvgpr_write_b32 a178, v174
v_accvgpr_write_b32 a179, v175
v_accvgpr_write_b32 a180, v176
v_accvgpr_write_b32 a181, v177
v_accvgpr_write_b32 a182, v178
v_accvgpr_write_b32 a183, v179
v_accvgpr_write_b32 a184, v180
s_waitcnt vmcnt(0) lgkmcnt(0)
s_barrier
s_mov_b32 s89, 1
s_waitcnt vmcnt(0) lgkmcnt(0)
s_branch Lcomb_28_%=
Lcombok_30_%=:
s_add_u32 m0, s57, 0
s_add_u32 s85, s58, 0
buffer_load_dwordx4 v3, s[20:23], s85 offen offset:0 sc1 lds
buffer_load_dwordx4 v3, s[20:23], s85 offen offset:1024 sc1 lds
buffer_load_dwordx4 v3, s[20:23], s85 offen offset:2048 sc1 lds
buffer_load_dwordx4 v3, s[20:23], s85 offen offset:3072 sc1 lds
s_add_u32 m0, s57, 4096
s_add_u32 s85, s58, 4096
buffer_load_dwordx4 v3, s[20:23], s85 offen offset:0 sc1 lds
buffer_load_dwordx4 v3, s[20:23], s85 offen offset:1024 sc1 lds
buffer_load_dwordx4 v3, s[20:23], s85 offen offset:2048 sc1 lds
buffer_load_dwordx4 v3, s[20:23], s85 offen offset:3072 sc1 lds
s_add_u32 s80, s54, 0
buffer_load_dwordx4 v[172:175], v3, s[20:23], s80 offen offset:0 sc1
buffer_load_dwordx4 v[176:179], v3, s[20:23], s80 offen offset:1024 sc1
buffer_load_dwordx4 v[180:183], v3, s[20:23], s80 offen offset:2048 sc1
buffer_load_dwordx4 v[184:187], v3, s[20:23], s80 offen offset:3072 sc1
s_add_u32 s80, s54, 4096
)ASM"
R"ASM(buffer_load_dwordx4 v[188:191], v3, s[20:23], s80 offen offset:0 sc1
buffer_load_dwordx4 v[192:195], v3, s[20:23], s80 offen offset:1024 sc1
buffer_load_dwordx4 v[196:199], v3, s[20:23], s80 offen offset:2048 sc1
buffer_load_dwordx4 v[200:203], v3, s[20:23], s80 offen offset:3072 sc1
s_add_u32 s80, s54, 8192
buffer_load_dwordx4 v[204:207], v3, s[20:23], s80 offen offset:0 sc1
buffer_load_dwordx4 v[208:211], v3, s[20:23], s80 offen offset:1024 sc1
buffer_load_dwordx4 v[212:215], v3, s[20:23], s80 offen offset:2048 sc1
buffer_load_dwordx4 v[216:219], v3, s[20:23], s80 offen offset:3072 sc1
s_add_u32 s80, s54, 12288
buffer_load_dwordx4 v[220:223], v3, s[20:23], s80 offen offset:0 sc1
buffer_load_dwordx4 v[224:227], v3, s[20:23], s80 offen offset:1024 sc1
buffer_load_dwordx4 v[228:231], v3, s[20:23], s80 offen offset:2048 sc1
buffer_load_dwordx4 v[232:235], v3, s[20:23], s80 offen offset:3072 sc1
s_lshl_b32 s80, s15, 8
s_lshl_b32 s81, s101, 7
s_add_u32 s80, s80, s81
s_add_u32 s80, s80, 133120
v_lshlrev_b32 v9, 4, v2
v_add_u32 v9, s80, v9
s_waitcnt vmcnt(32)
v_permlane32_swap_b32 v164, v166
v_permlane32_swap_b32 v165, v167
s_waitcnt vmcnt(31)
v_permlane32_swap_b32 v168, v170
v_permlane32_swap_b32 v169, v171
v_lshlrev_b32 v236, 16, v164
v_and_b32 v237, 0xffff0000, v164
v_lshlrev_b32 v238, 16, v165
v_and_b32 v239, 0xffff0000, v165
v_lshlrev_b32 v240, 16, v166
v_and_b32 v241, 0xffff0000, v166
)ASM"
R"ASM(v_lshlrev_b32 v242, 16, v167
v_and_b32 v243, 0xffff0000, v167
v_lshlrev_b32 v244, 16, v168
v_and_b32 v245, 0xffff0000, v168
v_lshlrev_b32 v246, 16, v169
v_and_b32 v247, 0xffff0000, v169
v_lshlrev_b32 v248, 16, v170
v_and_b32 v249, 0xffff0000, v170
v_lshlrev_b32 v250, 16, v171
v_and_b32 v251, 0xffff0000, v171
s_mul_i32 s80, s13, 4608
s_add_u32 s80, s80, 145408
v_add_u32 v10, s80, v3
s_xor_b32 s81, s13, 2
s_mul_i32 s81, s81, 4608
s_add_u32 s81, s81, 145408
v_add_u32 v11, s81, v3
v_lshlrev_b32 v25, 10, v24
v_add_u32 v25, 131072, v25
v_mov_b32 v31, 139264
v_cmp_eq_u32 vcc, 6, v24
s_nop 1
v_cndmask_b32 v25, v25, v31, vcc
v_lshl_add_u32 v25, v4, 4, v25
v_mov_b32 v26, 0
v_mov_b32 v27, 0
v_mov_b32 v28, 0
v_mov_b32 v29, 0
s_waitcnt vmcnt(24)
v_add_f32 v20, v20, v22
v_add_f32 v21, v21, v23
v_add_f32 v16, v16, v18
)ASM"
R"ASM(v_add_f32 v17, v17, v19
v_add_f32 v16, v16, v20
v_add_f32 v17, v17, v21
v_cvt_pk_bf16_f32 v18, v16, v17
v_lshlrev_b32 v19, 16, v18
v_and_b32 v20, 0xffff0000, v18
v_sub_f32 v16, v16, v19
v_sub_f32 v17, v17, v20
v_cvt_pk_bf16_f32 v12, v19, v19
v_cvt_pk_bf16_f32 v13, v16, v20
v_cvt_pk_bf16_f32 v14, v20, v17
v_mov_b32 v15, 0
ds_write_b128 v25, v[12:15]
ds_write_b128 v25, v[26:29] offset:512
s_mov_b32 s67, 0
Lchko_40_%=:
s_waitcnt vmcnt(13)
v_max3_u32 v8, v175, v179, v183
s_waitcnt vmcnt(12)
v_max_u32 v8, v8, v187
v_cmp_eq_u32 vcc, -1, v8
s_cbranch_vccz Lcok_41_%=
s_call_b64 s[44:45], Lslowo0_35_%=
Lcok_41_%=:
v_mfma_f32_32x32x16_bf16 v[48:63], a[64:67], v[172:175], 0
v_mfma_f32_32x32x16_bf16 v[32:47], a[0:3], v[172:175], 0
v_mfma_f32_32x32x16_bf16 v[48:63], a[68:71], v[176:179], v[48:63]
v_mfma_f32_32x32x16_bf16 v[32:47], a[4:7], v[176:179], v[32:47]
v_mfma_f32_32x32x16_bf16 v[48:63], a[72:75], v[180:183], v[48:63]
v_mfma_f32_32x32x16_bf16 v[32:47], a[8:11], v[180:183], v[32:47]
v_mfma_f32_32x32x16_bf16 v[48:63], a[76:79], v[184:187], v[48:63]
v_mfma_f32_32x32x16_bf16 v[32:47], a[12:15], v[184:187], v[32:47]
)ASM"
R"ASM(s_waitcnt vmcnt(9)
v_max3_u32 v8, v191, v195, v199
s_waitcnt vmcnt(8)
v_max_u32 v8, v8, v203
v_cmp_eq_u32 vcc, -1, v8
s_cbranch_vccz Lcok_42_%=
s_call_b64 s[44:45], Lslowo1_36_%=
Lcok_42_%=:
v_mfma_f32_32x32x16_bf16 v[48:63], a[80:83], v[188:191], v[48:63]
v_mfma_f32_32x32x16_bf16 v[32:47], a[16:19], v[188:191], v[32:47]
v_mfma_f32_32x32x16_bf16 v[48:63], a[84:87], v[192:195], v[48:63]
v_mfma_f32_32x32x16_bf16 v[32:47], a[20:23], v[192:195], v[32:47]
v_mfma_f32_32x32x16_bf16 v[48:63], a[88:91], v[196:199], v[48:63]
v_mfma_f32_32x32x16_bf16 v[32:47], a[24:27], v[196:199], v[32:47]
v_mfma_f32_32x32x16_bf16 v[48:63], a[92:95], v[200:203], v[48:63]
v_mfma_f32_32x32x16_bf16 v[32:47], a[28:31], v[200:203], v[32:47]
s_waitcnt vmcnt(5)
v_max3_u32 v8, v207, v211, v215
s_waitcnt vmcnt(4)
v_max_u32 v8, v8, v219
v_cmp_eq_u32 vcc, -1, v8
s_cbranch_vccz Lcok_43_%=
s_call_b64 s[44:45], Lslowo2_37_%=
Lcok_43_%=:
v_mfma_f32_32x32x16_bf16 v[48:63], a[96:99], v[204:207], v[48:63]
s_add_u32 m0, s57, 32768
s_add_u32 s85, s58, 32768
buffer_load_dwordx4 v3, s[20:23], s85 offen offset:0 sc1 lds
v_mfma_f32_32x32x16_bf16 v[32:47], a[32:35], v[204:207], v[32:47]
buffer_load_dwordx4 v3, s[20:23], s85 offen offset:1024 sc1 lds
v_mfma_f32_32x32x16_bf16 v[48:63], a[100:103], v[208:211], v[48:63]
v_mfma_f32_32x32x16_bf16 v[32:47], a[36:39], v[208:211], v[32:47]
)ASM"
R"ASM(buffer_load_dwordx4 v3, s[20:23], s85 offen offset:2048 sc1 lds
v_mfma_f32_32x32x16_bf16 v[48:63], a[104:107], v[212:215], v[48:63]
ds_read_b128 v[172:175], v9 offset:4096
v_mfma_f32_32x32x16_bf16 v[32:47], a[40:43], v[212:215], v[32:47]
buffer_load_dwordx4 v3, s[20:23], s85 offen offset:3072 sc1 lds
ds_read_b128 v[176:179], v9 offset:4128
v_mfma_f32_32x32x16_bf16 v[48:63], a[108:111], v[216:219], v[48:63]
ds_read_b128 v[180:183], v9 offset:4160
v_mfma_f32_32x32x16_bf16 v[32:47], a[44:47], v[216:219], v[32:47]
s_add_u32 m0, s57, 36864
s_add_u32 s85, s58, 36864
buffer_load_dwordx4 v3, s[20:23], s85 offen offset:0 sc1 lds
ds_read_b128 v[184:187], v9 offset:4192
s_waitcnt vmcnt(6)
v_max3_u32 v8, v223, v227, v231
s_waitcnt vmcnt(5)
v_max_u32 v8, v8, v235
v_cmp_eq_u32 vcc, -1, v8
s_cbranch_vccz Lcok_44_%=
s_call_b64 s[44:45], Lslowo3_38_%=
Lcok_44_%=:
v_mfma_f32_32x32x16_bf16 v[48:63], a[112:115], v[220:223], v[48:63]
buffer_load_dwordx4 v3, s[20:23], s85 offen offset:1024 sc1 lds
ds_read_b128 v[188:191], v9 offset:8192
v_mfma_f32_32x32x16_bf16 v[48:63], a[116:119], v[224:227], v[48:63]
ds_read_b128 v[192:195], v9 offset:8224
v_mfma_f32_32x32x16_bf16 v[48:63], a[120:123], v[228:231], v[48:63]
buffer_load_dwordx4 v3, s[20:23], s85 offen offset:2048 sc1 lds
ds_read_b128 v[196:199], v9 offset:8256
v_mfma_f32_32x32x16_bf16 v[48:63], a[124:127], v[232:235], v[48:63]
ds_read_b128 v[200:203], v9 offset:8288
v_mfma_f32_32x32x16_bf16 v[32:47], a[48:51], v[220:223], v[32:47]
)ASM"
R"ASM(buffer_load_dwordx4 v3, s[20:23], s85 offen offset:3072 sc1 lds
v_mfma_f32_32x32x16_bf16 v[32:47], a[52:55], v[224:227], v[32:47]
v_mfma_f32_32x32x16_bf16 v[32:47], a[56:59], v[228:231], v[32:47]
s_nop 6
ds_write_b128 v10, v[48:51] offset:0
ds_write_b128 v10, v[52:55] offset:1024
v_mfma_f32_32x32x16_bf16 v[32:47], a[60:63], v[232:235], v[32:47]
ds_write_b128 v10, v[56:59] offset:2048
ds_write_b128 v10, v[60:63] offset:3072
v_cmp_u_f32 vcc, v48, v48
s_cbranch_vccnz Lredoo_39_%=
Lredox_45_%=:
s_waitcnt lgkmcnt(0)
s_barrier
ds_read_b128 v[48:51], v11 offset:0
ds_read_b128 v[52:55], v11 offset:1024
ds_read_b128 v[56:59], v11 offset:2048
ds_read_b128 v[60:63], v11 offset:3072
s_waitcnt lgkmcnt(3)
s_nop 0
v_pk_add_f32 v[32:33], v[32:33], v[48:49]
v_pk_add_f32 v[34:35], v[34:35], v[50:51]
s_waitcnt lgkmcnt(2)
v_pk_add_f32 v[36:37], v[36:37], v[52:53]
v_pk_add_f32 v[38:39], v[38:39], v[54:55]
s_waitcnt lgkmcnt(1)
v_pk_add_f32 v[40:41], v[40:41], v[56:57]
v_pk_add_f32 v[42:43], v[42:43], v[58:59]
s_waitcnt lgkmcnt(0)
v_pk_add_f32 v[44:45], v[44:45], v[60:61]
v_pk_add_f32 v[46:47], v[46:47], v[62:63]
v_pk_fma_f32 v[236:237], v[172:173], v[32:33], v[236:237]
)ASM"
R"ASM(v_pk_fma_f32 v[238:239], v[174:175], v[34:35], v[238:239]
v_pk_fma_f32 v[240:241], v[176:177], v[36:37], v[240:241]
v_pk_fma_f32 v[242:243], v[178:179], v[38:39], v[242:243]
v_pk_fma_f32 v[244:245], v[180:181], v[40:41], v[244:245]
v_pk_fma_f32 v[246:247], v[182:183], v[42:43], v[246:247]
v_pk_fma_f32 v[248:249], v[184:185], v[44:45], v[248:249]
v_pk_fma_f32 v[250:251], v[186:187], v[46:47], v[250:251]
v_pk_mul_f32 v[32:33], v[236:237], v[188:189]
v_pk_mul_f32 v[34:35], v[238:239], v[190:191]
v_pk_mul_f32 v[36:37], v[240:241], v[192:193]
v_pk_mul_f32 v[38:39], v[242:243], v[194:195]
v_pk_mul_f32 v[40:41], v[244:245], v[196:197]
v_pk_mul_f32 v[42:43], v[246:247], v[198:199]
v_pk_mul_f32 v[44:45], v[248:249], v[200:201]
v_pk_mul_f32 v[46:47], v[250:251], v[202:203]
v_cvt_pk_bf16_f32 v48, v32, v33
v_cvt_pk_bf16_f32 v49, v34, v35
v_cvt_pk_bf16_f32 v50, v36, v37
v_cvt_pk_bf16_f32 v51, v38, v39
v_cvt_pk_bf16_f32 v52, v40, v41
v_cvt_pk_bf16_f32 v53, v42, v43
v_cvt_pk_bf16_f32 v54, v44, v45
v_cvt_pk_bf16_f32 v55, v46, v47
s_add_u32 s80, s47, 0x300000
buffer_store_dwordx4 v[48:51], v3, s[20:23], s80 offen offset:0 sc1
buffer_store_dwordx4 v[52:55], v3, s[20:23], s80 offen offset:1024 sc1
v_pk_add_f32 v[10:11], v[236:237], v[238:239]
v_pk_mul_f32 v[12:13], v[236:237], v[236:237]
v_pk_fma_f32 v[12:13], v[238:239], v[238:239], v[12:13]
v_pk_add_f32 v[10:11], v[10:11], v[240:241]
v_pk_fma_f32 v[12:13], v[240:241], v[240:241], v[12:13]
v_pk_add_f32 v[10:11], v[10:11], v[242:243]
)ASM"
R"ASM(v_pk_fma_f32 v[12:13], v[242:243], v[242:243], v[12:13]
v_pk_add_f32 v[10:11], v[10:11], v[244:245]
v_pk_fma_f32 v[12:13], v[244:245], v[244:245], v[12:13]
v_pk_add_f32 v[10:11], v[10:11], v[246:247]
v_pk_fma_f32 v[12:13], v[246:247], v[246:247], v[12:13]
v_pk_add_f32 v[10:11], v[10:11], v[248:249]
v_pk_fma_f32 v[12:13], v[248:249], v[248:249], v[12:13]
v_pk_add_f32 v[10:11], v[10:11], v[250:251]
v_pk_fma_f32 v[12:13], v[250:251], v[250:251], v[12:13]
v_add_f32 v10, v10, v11
v_add_f32 v12, v12, v13
s_nop 1
v_permlane32_swap_b32 v10, v12
v_add_f32 v10, v10, v12
v_lshlrev_b32 v13, 3, v4
v_lshl_add_u32 v13, v2, 2, v13
s_lshr_b32 s80, s47, 3
s_add_u32 s80, s80, 0x60000
buffer_store_dword v10, v13, s[20:23], s80 offen sc1
s_add_u32 m0, s57, 65536
s_add_u32 s85, s58, 65536
buffer_load_dwordx4 v3, s[20:23], s85 offen offset:0 sc1 lds
buffer_load_dwordx4 v3, s[20:23], s85 offen offset:1024 sc1 lds
buffer_load_dwordx4 v3, s[20:23], s85 offen offset:2048 sc1 lds
buffer_load_dwordx4 v3, s[20:23], s85 offen offset:3072 sc1 lds
s_add_u32 m0, s57, 69632
s_add_u32 s85, s58, 69632
buffer_load_dwordx4 v3, s[20:23], s85 offen offset:0 sc1 lds
buffer_load_dwordx4 v3, s[20:23], s85 offen offset:1024 sc1 lds
buffer_load_dwordx4 v3, s[20:23], s85 offen offset:2048 sc1 lds
buffer_load_dwordx4 v3, s[20:23], s85 offen offset:3072 sc1 lds
v_lshlrev_b32 v8, 8, v2
)ASM"
R"ASM(v_lshl_add_u32 v8, v4, 3, v8
v_mov_b32 v200, v3
v_add_u32 v201, 0x10000, v3
v_add_u32 v202, 0x20000, v3
s_mov_b32 s67, 0
Lyr1_46_%=:
buffer_load_dwordx2 v[10:11], v8, s[20:23], s56 offen offset:0 sc1
buffer_load_dwordx2 v[12:13], v8, s[20:23], s56 offen offset:512 sc1
buffer_load_dwordx2 v[14:15], v8, s[20:23], s56 offen offset:1024 sc1
buffer_load_dwordx2 v[16:17], v8, s[20:23], s56 offen offset:1536 sc1
buffer_load_dwordx2 v[18:19], v8, s[20:23], s56 offen offset:2048 sc1
buffer_load_dwordx2 v[20:21], v8, s[20:23], s56 offen offset:2560 sc1
buffer_load_dwordx2 v[22:23], v8, s[20:23], s56 offen offset:3072 sc1
buffer_load_dwordx2 v[24:25], v8, s[20:23], s56 offen offset:3584 sc1
ds_read_b128 v[64:67], v200 offset:0
ds_read_b128 v[164:167], v200 offset:1024
ds_read_b128 v[168:171], v200 offset:2048
ds_read_b128 v[172:175], v200 offset:3072
s_waitcnt vmcnt(6)
v_max3_u32 v26, v10, v11, v12
s_waitcnt vmcnt(5)
v_max3_u32 v26, v26, v13, v14
s_waitcnt vmcnt(4)
v_max3_u32 v26, v26, v15, v16
s_waitcnt vmcnt(3)
v_max3_u32 v26, v26, v17, v18
s_waitcnt vmcnt(2)
v_max3_u32 v26, v26, v19, v20
s_waitcnt vmcnt(1)
v_max3_u32 v26, v26, v21, v22
s_waitcnt vmcnt(0)
v_max3_u32 v26, v26, v23, v24
)ASM"
R"ASM(v_max_u32 v26, v26, v25
v_cmp_eq_u32 vcc, -1, v26
s_cbranch_vccnz Lslowy_47_%=
s_add_u32 s80, s55, 0
buffer_load_dwordx4 a[0:3], v3, s[20:23], s80 offen offset:0 sc1
buffer_load_dwordx4 a[4:7], v3, s[20:23], s80 offen offset:1024 sc1
buffer_load_dwordx4 a[8:11], v3, s[20:23], s80 offen offset:2048 sc1
buffer_load_dwordx4 a[12:15], v3, s[20:23], s80 offen offset:3072 sc1
s_add_u32 s80, s55, 4096
buffer_load_dwordx4 a[16:19], v3, s[20:23], s80 offen offset:0 sc1
buffer_load_dwordx4 a[20:23], v3, s[20:23], s80 offen offset:1024 sc1
buffer_load_dwordx4 a[24:27], v3, s[20:23], s80 offen offset:2048 sc1
buffer_load_dwordx4 a[28:31], v3, s[20:23], s80 offen offset:3072 sc1
s_add_u32 s80, s55, 8192
buffer_load_dwordx4 a[32:35], v3, s[20:23], s80 offen offset:0 sc1
buffer_load_dwordx4 a[36:39], v3, s[20:23], s80 offen offset:1024 sc1
buffer_load_dwordx4 a[40:43], v3, s[20:23], s80 offen offset:2048 sc1
buffer_load_dwordx4 a[44:47], v3, s[20:23], s80 offen offset:3072 sc1
s_add_u32 s80, s55, 12288
buffer_load_dwordx4 a[48:51], v3, s[20:23], s80 offen offset:0 sc1
buffer_load_dwordx4 a[52:55], v3, s[20:23], s80 offen offset:1024 sc1
buffer_load_dwordx4 a[56:59], v3, s[20:23], s80 offen offset:2048 sc1
buffer_load_dwordx4 a[60:63], v3, s[20:23], s80 offen offset:3072 sc1
s_add_u32 s80, s55, 16384
buffer_load_dwordx4 a[64:67], v3, s[20:23], s80 offen offset:0 sc1
buffer_load_dwordx4 a[68:71], v3, s[20:23], s80 offen offset:1024 sc1
buffer_load_dwordx4 a[72:75], v3, s[20:23], s80 offen offset:2048 sc1
buffer_load_dwordx4 a[76:79], v3, s[20:23], s80 offen offset:3072 sc1
s_add_u32 s80, s55, 20480
buffer_load_dwordx4 a[80:83], v3, s[20:23], s80 offen offset:0 sc1
buffer_load_dwordx4 a[84:87], v3, s[20:23], s80 offen offset:1024 sc1
buffer_load_dwordx4 a[88:91], v3, s[20:23], s80 offen offset:2048 sc1
)ASM"
R"ASM(buffer_load_dwordx4 a[92:95], v3, s[20:23], s80 offen offset:3072 sc1
s_add_u32 s80, s55, 24576
buffer_load_dwordx4 a[96:99], v3, s[20:23], s80 offen offset:0 sc1
buffer_load_dwordx4 a[100:103], v3, s[20:23], s80 offen offset:1024 sc1
buffer_load_dwordx4 a[104:107], v3, s[20:23], s80 offen offset:2048 sc1
buffer_load_dwordx4 a[108:111], v3, s[20:23], s80 offen offset:3072 sc1
s_add_u32 s80, s55, 28672
buffer_load_dwordx4 a[112:115], v3, s[20:23], s80 offen offset:0 sc1
buffer_load_dwordx4 a[116:119], v3, s[20:23], s80 offen offset:1024 sc1
buffer_load_dwordx4 a[120:123], v3, s[20:23], s80 offen offset:2048 sc1
buffer_load_dwordx4 a[124:127], v3, s[20:23], s80 offen offset:3072 sc1
Lydone_50_%=:
ds_read_b128 v[176:179], v200 offset:4096
s_waitcnt vmcnt(31) lgkmcnt(4)
v_mfma_f32_32x32x16_bf16 v[32:47], v[64:67], a[0:3], 0
ds_read_b128 v[180:183], v200 offset:5120
s_waitcnt vmcnt(30) lgkmcnt(4)
v_mfma_f32_32x32x16_bf16 v[32:47], v[164:167], a[4:7], v[32:47]
ds_read_b128 v[64:67], v200 offset:6144
s_waitcnt vmcnt(29) lgkmcnt(4)
v_mfma_f32_32x32x16_bf16 v[32:47], v[168:171], a[8:11], v[32:47]
ds_read_b128 v[164:167], v200 offset:7168
s_waitcnt vmcnt(28) lgkmcnt(4)
v_mfma_f32_32x32x16_bf16 v[32:47], v[172:175], a[12:15], v[32:47]
ds_read_b128 v[168:171], v200 offset:8192
s_waitcnt vmcnt(27) lgkmcnt(4)
v_mfma_f32_32x32x16_bf16 v[32:47], v[176:179], a[16:19], v[32:47]
v_add_f32 v10, v10, v12
v_add_f32 v11, v11, v13
ds_read_b128 v[172:175], v200 offset:9216
s_waitcnt vmcnt(26) lgkmcnt(4)
v_mfma_f32_32x32x16_bf16 v[32:47], v[180:183], a[20:23], v[32:47]
)ASM"
R"ASM(v_add_f32 v10, v10, v14
v_add_f32 v11, v11, v15
ds_read_b128 v[176:179], v200 offset:10240
s_waitcnt vmcnt(25) lgkmcnt(4)
v_mfma_f32_32x32x16_bf16 v[32:47], v[64:67], a[24:27], v[32:47]
v_add_f32 v10, v10, v16
v_add_f32 v11, v11, v17
ds_read_b128 v[180:183], v200 offset:11264
s_waitcnt vmcnt(24) lgkmcnt(4)
v_mfma_f32_32x32x16_bf16 v[32:47], v[164:167], a[28:31], v[32:47]
v_add_f32 v10, v10, v18
v_add_f32 v11, v11, v19
ds_read_b128 v[64:67], v200 offset:12288
s_waitcnt vmcnt(23) lgkmcnt(4)
v_mfma_f32_32x32x16_bf16 v[32:47], v[168:171], a[32:35], v[32:47]
v_add_f32 v10, v10, v20
v_add_f32 v11, v11, v21
ds_read_b128 v[164:167], v200 offset:13312
s_waitcnt vmcnt(22) lgkmcnt(4)
v_mfma_f32_32x32x16_bf16 v[32:47], v[172:175], a[36:39], v[32:47]
v_add_f32 v10, v10, v22
v_add_f32 v11, v11, v23
ds_read_b128 v[168:171], v200 offset:14336
s_waitcnt vmcnt(21) lgkmcnt(4)
v_mfma_f32_32x32x16_bf16 v[32:47], v[176:179], a[40:43], v[32:47]
v_add_f32 v10, v10, v24
v_add_f32 v11, v11, v25
ds_read_b128 v[172:175], v200 offset:15360
s_waitcnt vmcnt(20) lgkmcnt(4)
v_mfma_f32_32x32x16_bf16 v[32:47], v[180:183], a[44:47], v[32:47]
v_mov_b32 v12, v10
v_mov_b32 v13, v11
)ASM"
R"ASM(ds_read_b128 v[176:179], v200 offset:16384
s_waitcnt vmcnt(19) lgkmcnt(4)
v_mfma_f32_32x32x16_bf16 v[32:47], v[64:67], a[48:51], v[32:47]
v_permlane32_swap_b32 v10, v12
v_permlane32_swap_b32 v11, v13
ds_read_b128 v[180:183], v200 offset:17408
s_waitcnt vmcnt(18) lgkmcnt(4)
v_mfma_f32_32x32x16_bf16 v[32:47], v[164:167], a[52:55], v[32:47]
v_add_f32 v10, v10, v12
v_add_f32 v11, v11, v13
ds_read_b128 v[64:67], v200 offset:18432
s_waitcnt vmcnt(17) lgkmcnt(4)
v_mfma_f32_32x32x16_bf16 v[32:47], v[168:171], a[56:59], v[32:47]
v_mul_f32 v10, 0xbb000000, v10
v_mul_f32 v11, 0x3b000000, v11
ds_read_b128 v[164:167], v200 offset:19456
s_waitcnt vmcnt(16) lgkmcnt(4)
v_mfma_f32_32x32x16_bf16 v[32:47], v[172:175], a[60:63], v[32:47]
v_fma_f32 v11, -v10, v10, v11
v_max_f32 v11, 0, v11
ds_read_b128 v[168:171], v200 offset:20480
s_waitcnt vmcnt(15) lgkmcnt(4)
v_mfma_f32_32x32x16_bf16 v[32:47], v[176:179], a[64:67], v[32:47]
v_add_f32 v11, 0x3727c5ac, v11
ds_read_b128 v[172:175], v200 offset:21504
s_waitcnt vmcnt(14) lgkmcnt(4)
v_mfma_f32_32x32x16_bf16 v[32:47], v[180:183], a[68:71], v[32:47]
v_rsq_f32 v12, v11
ds_read_b128 v[176:179], v200 offset:22528
s_waitcnt vmcnt(13) lgkmcnt(4)
v_mfma_f32_32x32x16_bf16 v[32:47], v[64:67], a[72:75], v[32:47]
v_mov_b32 v199, 0
)ASM"
R"ASM(ds_read_b128 v[180:183], v200 offset:23552
s_waitcnt vmcnt(12) lgkmcnt(4)
v_mfma_f32_32x32x16_bf16 v[32:47], v[164:167], a[76:79], v[32:47]
v_mul_f32 v28, v11, v12
ds_read_b128 v[64:67], v200 offset:24576
s_waitcnt vmcnt(11) lgkmcnt(4)
v_mfma_f32_32x32x16_bf16 v[32:47], v[168:171], a[80:83], v[32:47]
v_mul_f32 v29, 0xbfb8aa3b, v12
ds_read_b128 v[164:167], v200 offset:25600
s_waitcnt vmcnt(10) lgkmcnt(4)
v_mfma_f32_32x32x16_bf16 v[32:47], v[172:175], a[84:87], v[32:47]
v_cvt_pk_bf16_f32 v197, v10, v28
ds_read_b128 v[168:171], v200 offset:26624
s_waitcnt vmcnt(9) lgkmcnt(4)
v_mfma_f32_32x32x16_bf16 v[32:47], v[176:179], a[88:91], v[32:47]
v_lshlrev_b32 v14, 16, v197
ds_read_b128 v[172:175], v200 offset:27648
s_waitcnt vmcnt(8) lgkmcnt(4)
v_mfma_f32_32x32x16_bf16 v[32:47], v[180:183], a[92:95], v[32:47]
v_and_b32 v15, 0xffff0000, v197
ds_read_b128 v[176:179], v200 offset:28672
s_waitcnt vmcnt(7) lgkmcnt(4)
v_mfma_f32_32x32x16_bf16 v[32:47], v[64:67], a[96:99], v[32:47]
v_sub_f32 v16, v10, v14
ds_read_b128 v[180:183], v200 offset:29696
s_waitcnt vmcnt(6) lgkmcnt(4)
v_mfma_f32_32x32x16_bf16 v[32:47], v[164:167], a[100:103], v[32:47]
v_sub_f32 v17, v28, v15
ds_read_b128 v[64:67], v200 offset:30720
s_waitcnt vmcnt(5) lgkmcnt(4)
v_mfma_f32_32x32x16_bf16 v[32:47], v[168:171], a[104:107], v[32:47]
v_cvt_pk_bf16_f32 v196, v14, v16
)ASM"
R"ASM(ds_read_b128 v[164:167], v200 offset:31744
s_waitcnt vmcnt(4) lgkmcnt(4)
v_mfma_f32_32x32x16_bf16 v[32:47], v[172:175], a[108:111], v[32:47]
s_cmp_lg_u32 s67, 0
s_cbranch_scc1 Lnb1_51_%=
s_barrier
Lnb1_51_%=:
v_cvt_pk_bf16_f32 v198, v17, v15
ds_read_b128 v[168:171], v202 offset:0
s_waitcnt vmcnt(3) lgkmcnt(4)
v_mfma_f32_32x32x16_bf16 v[32:47], v[176:179], a[112:115], v[32:47]
ds_read_b128 v[172:175], v200 offset:32768
s_waitcnt vmcnt(2) lgkmcnt(4)
v_mfma_f32_32x32x16_bf16 v[32:47], v[180:183], a[116:119], v[32:47]
ds_read_b128 v[176:179], v200 offset:33792
s_waitcnt vmcnt(1) lgkmcnt(4)
v_mfma_f32_32x32x16_bf16 v[32:47], v[64:67], a[120:123], v[32:47]
ds_read_b128 v[180:183], v200 offset:34816
s_waitcnt vmcnt(0) lgkmcnt(4)
v_mfma_f32_32x32x16_bf16 v[32:47], v[164:167], a[124:127], v[32:47]
ds_read_b128 v[64:67], v200 offset:35840
s_waitcnt lgkmcnt(4)
v_mfma_f32_32x32x16_bf16 v[32:47], v[168:171], v[196:199], v[32:47]
ds_read_b128 v[164:167], v200 offset:36864
s_waitcnt lgkmcnt(4)
v_mfma_f32_32x32x16_bf16 v[48:63], v[172:175], a[0:3], 0
ds_read_b128 v[168:171], v200 offset:37888
s_waitcnt lgkmcnt(4)
v_mfma_f32_32x32x16_bf16 v[48:63], v[176:179], a[4:7], v[48:63]
ds_read_b128 v[172:175], v200 offset:38912
s_waitcnt lgkmcnt(4)
v_mfma_f32_32x32x16_bf16 v[48:63], v[180:183], a[8:11], v[48:63]
)ASM"
R"ASM(s_add_u32 m0, s57, 98304
s_add_u32 s85, s58, 98304
buffer_load_dwordx4 v3, s[20:23], s85 offen offset:0 sc1 lds
ds_read_b128 v[176:179], v200 offset:39936
s_waitcnt lgkmcnt(4)
v_mfma_f32_32x32x16_bf16 v[48:63], v[64:67], a[12:15], v[48:63]
v_cmp_u_f32 vcc, v32, v32
s_cbranch_vccnz Lredoy_48_%=
Lredoyx_49_%=:
v_mul_f32 v184, v29, v32
v_mul_f32 v185, v29, v33
ds_read_b128 v[180:183], v200 offset:40960
s_waitcnt lgkmcnt(4)
v_mfma_f32_32x32x16_bf16 v[48:63], v[164:167], a[16:19], v[48:63]
v_mul_f32 v186, v29, v34
v_mul_f32 v187, v29, v35
v_exp_f32 v184, v184
ds_read_b128 v[64:67], v200 offset:41984
s_waitcnt lgkmcnt(4)
v_mfma_f32_32x32x16_bf16 v[48:63], v[168:171], a[20:23], v[48:63]
buffer_load_dwordx4 v3, s[20:23], s85 offen offset:1024 sc1 lds
v_mul_f32 v188, v29, v36
ds_read_b128 v[164:167], v200 offset:43008
s_waitcnt lgkmcnt(4)
v_mfma_f32_32x32x16_bf16 v[48:63], v[172:175], a[24:27], v[48:63]
v_exp_f32 v185, v185
v_mul_f32 v189, v29, v37
ds_read_b128 v[168:171], v200 offset:44032
s_waitcnt lgkmcnt(4)
v_mfma_f32_32x32x16_bf16 v[48:63], v[176:179], a[28:31], v[48:63]
v_exp_f32 v186, v186
v_mul_f32 v190, v29, v38
)ASM"
R"ASM(ds_read_b128 v[172:175], v200 offset:45056
s_waitcnt lgkmcnt(4)
v_mfma_f32_32x32x16_bf16 v[48:63], v[180:183], a[32:35], v[48:63]
v_exp_f32 v187, v187
v_mul_f32 v191, v29, v39
ds_read_b128 v[176:179], v200 offset:46080
s_waitcnt lgkmcnt(4)
v_mfma_f32_32x32x16_bf16 v[48:63], v[64:67], a[36:39], v[48:63]
buffer_load_dwordx4 v3, s[20:23], s85 offen offset:2048 sc1 lds
v_fma_f32 v184, v184, v28, v28
ds_read_b128 v[180:183], v200 offset:47104
s_waitcnt lgkmcnt(4)
v_mfma_f32_32x32x16_bf16 v[48:63], v[164:167], a[40:43], v[48:63]
v_exp_f32 v188, v188
v_fma_f32 v185, v185, v28, v28
ds_read_b128 v[64:67], v200 offset:48128
s_waitcnt lgkmcnt(4)
v_mfma_f32_32x32x16_bf16 v[48:63], v[168:171], a[44:47], v[48:63]
v_exp_f32 v189, v189
v_fma_f32 v186, v186, v28, v28
ds_read_b128 v[164:167], v200 offset:49152
s_waitcnt lgkmcnt(4)
v_mfma_f32_32x32x16_bf16 v[48:63], v[172:175], a[48:51], v[48:63]
buffer_load_dwordx4 v3, s[20:23], s85 offen offset:3072 sc1 lds
v_exp_f32 v190, v190
ds_read_b128 v[168:171], v200 offset:50176
s_waitcnt lgkmcnt(4)
v_mfma_f32_32x32x16_bf16 v[48:63], v[176:179], a[52:55], v[48:63]
v_fma_f32 v187, v187, v28, v28
v_exp_f32 v191, v191
ds_read_b128 v[172:175], v200 offset:51200
s_waitcnt lgkmcnt(4)
)ASM"
R"ASM(v_mfma_f32_32x32x16_bf16 v[48:63], v[180:183], a[56:59], v[48:63]
v_rcp_f32 v184, v184
ds_read_b128 v[176:179], v200 offset:52224
s_waitcnt lgkmcnt(4)
v_mfma_f32_32x32x16_bf16 v[48:63], v[64:67], a[60:63], v[48:63]
v_fma_f32 v188, v188, v28, v28
v_rcp_f32 v185, v185
ds_read_b128 v[180:183], v200 offset:53248
s_waitcnt lgkmcnt(4)
v_mfma_f32_32x32x16_bf16 v[48:63], v[164:167], a[64:67], v[48:63]
s_add_u32 m0, s57, 102400
s_add_u32 s85, s58, 102400
buffer_load_dwordx4 v3, s[20:23], s85 offen offset:0 sc1 lds
ds_read_b128 v[64:67], v200 offset:54272
s_waitcnt lgkmcnt(4)
v_mfma_f32_32x32x16_bf16 v[48:63], v[168:171], a[68:71], v[48:63]
v_fma_f32 v189, v189, v28, v28
v_rcp_f32 v186, v186
ds_read_b128 v[164:167], v200 offset:55296
s_waitcnt lgkmcnt(4)
v_mfma_f32_32x32x16_bf16 v[48:63], v[172:175], a[72:75], v[48:63]
v_fma_f32 v190, v190, v28, v28
v_rcp_f32 v187, v187
ds_read_b128 v[168:171], v200 offset:56320
s_waitcnt lgkmcnt(4)
v_mfma_f32_32x32x16_bf16 v[48:63], v[176:179], a[76:79], v[48:63]
buffer_load_dwordx4 v3, s[20:23], s85 offen offset:1024 sc1 lds
v_fma_f32 v191, v191, v28, v28
v_mul_f32 v184, v184, v40
ds_read_b128 v[172:175], v200 offset:57344
s_waitcnt lgkmcnt(4)
v_mfma_f32_32x32x16_bf16 v[48:63], v[180:183], a[80:83], v[48:63]
)ASM"
R"ASM(v_rcp_f32 v188, v188
ds_read_b128 v[176:179], v200 offset:58368
s_waitcnt lgkmcnt(4)
v_mfma_f32_32x32x16_bf16 v[48:63], v[64:67], a[84:87], v[48:63]
v_mul_f32 v185, v185, v41
v_rcp_f32 v189, v189
ds_read_b128 v[180:183], v200 offset:59392
s_waitcnt lgkmcnt(4)
v_mfma_f32_32x32x16_bf16 v[48:63], v[164:167], a[88:91], v[48:63]
v_mul_f32 v186, v186, v42
v_rcp_f32 v190, v190
ds_read_b128 v[64:67], v200 offset:60416
s_waitcnt lgkmcnt(4)
v_mfma_f32_32x32x16_bf16 v[48:63], v[168:171], a[92:95], v[48:63]
buffer_load_dwordx4 v3, s[20:23], s85 offen offset:2048 sc1 lds
v_mul_f32 v187, v187, v43
ds_read_b128 v[164:167], v200 offset:61440
s_waitcnt lgkmcnt(4)
v_mfma_f32_32x32x16_bf16 v[48:63], v[172:175], a[96:99], v[48:63]
v_rcp_f32 v191, v191
ds_read_b128 v[168:171], v200 offset:62464
s_waitcnt lgkmcnt(4)
v_mfma_f32_32x32x16_bf16 v[48:63], v[176:179], a[100:103], v[48:63]
v_mul_f32 v188, v188, v44
v_cvt_pk_bf16_f32 v192, v184, v185
ds_read_b128 v[172:175], v200 offset:63488
s_waitcnt lgkmcnt(4)
v_mfma_f32_32x32x16_bf16 v[48:63], v[180:183], a[104:107], v[48:63]
buffer_load_dwordx4 v3, s[20:23], s85 offen offset:3072 sc1 lds
v_mul_f32 v189, v189, v45
v_cvt_pk_bf16_f32 v193, v186, v187
ds_read_b128 v[176:179], v200 offset:64512
)ASM"
R"ASM(s_waitcnt lgkmcnt(4)
v_mfma_f32_32x32x16_bf16 v[48:63], v[64:67], a[108:111], v[48:63]
v_mul_f32 v190, v190, v46
v_mul_f32 v191, v191, v47
ds_read_b128 v[180:183], v202 offset:1024
s_waitcnt lgkmcnt(4)
v_mfma_f32_32x32x16_bf16 v[48:63], v[164:167], a[112:115], v[48:63]
v_cvt_pk_bf16_f32 v194, v188, v189
v_cvt_pk_bf16_f32 v195, v190, v191
ds_read_b128 v[64:67], v201 offset:0
s_waitcnt lgkmcnt(4)
v_mfma_f32_32x32x16_bf16 v[48:63], v[168:171], a[116:119], v[48:63]
s_add_u32 s84, s73, 0
buffer_store_dwordx4 v[192:195], v3, s[20:23], s84 offen sc1
ds_read_b128 v[164:167], v201 offset:1024
s_waitcnt lgkmcnt(4)
v_mfma_f32_32x32x16_bf16 v[48:63], v[172:175], a[120:123], v[48:63]
ds_read_b128 v[168:171], v201 offset:2048
s_waitcnt lgkmcnt(4)
v_mfma_f32_32x32x16_bf16 v[48:63], v[176:179], a[124:127], v[48:63]
ds_read_b128 v[172:175], v201 offset:3072
s_waitcnt lgkmcnt(4)
v_mfma_f32_32x32x16_bf16 v[48:63], v[180:183], v[196:199], v[48:63]
ds_read_b128 v[176:179], v201 offset:4096
s_waitcnt lgkmcnt(4)
v_mfma_f32_32x32x16_bf16 v[32:47], v[64:67], a[0:3], 0
ds_read_b128 v[180:183], v201 offset:5120
s_waitcnt lgkmcnt(4)
v_mfma_f32_32x32x16_bf16 v[32:47], v[164:167], a[4:7], v[32:47]
s_waitcnt vmcnt(1)
s_barrier
ds_read_b128 v[64:67], v201 offset:6144
)ASM"
R"ASM(s_waitcnt lgkmcnt(4)
v_mfma_f32_32x32x16_bf16 v[32:47], v[168:171], a[8:11], v[32:47]
s_add_u32 m0, s57, 0
s_add_u32 s85, s58, 131072
buffer_load_dwordx4 v3, s[20:23], s85 offen offset:0 sc1 lds
ds_read_b128 v[164:167], v201 offset:7168
s_waitcnt lgkmcnt(4)
v_mfma_f32_32x32x16_bf16 v[32:47], v[172:175], a[12:15], v[32:47]
v_mul_f32 v184, v29, v48
v_mul_f32 v185, v29, v49
v_mul_f32 v186, v29, v50
ds_read_b128 v[168:171], v201 offset:8192
s_waitcnt lgkmcnt(4)
v_mfma_f32_32x32x16_bf16 v[32:47], v[176:179], a[16:19], v[32:47]
buffer_load_dwordx4 v3, s[20:23], s85 offen offset:1024 sc1 lds
v_mul_f32 v187, v29, v51
ds_read_b128 v[172:175], v201 offset:9216
s_waitcnt lgkmcnt(4)
v_mfma_f32_32x32x16_bf16 v[32:47], v[180:183], a[20:23], v[32:47]
v_exp_f32 v184, v184
v_mul_f32 v188, v29, v52
ds_read_b128 v[176:179], v201 offset:10240
s_waitcnt lgkmcnt(4)
v_mfma_f32_32x32x16_bf16 v[32:47], v[64:67], a[24:27], v[32:47]
v_exp_f32 v185, v185
v_mul_f32 v189, v29, v53
ds_read_b128 v[180:183], v201 offset:11264
s_waitcnt lgkmcnt(4)
v_mfma_f32_32x32x16_bf16 v[32:47], v[164:167], a[28:31], v[32:47]
buffer_load_dwordx4 v3, s[20:23], s85 offen offset:2048 sc1 lds
v_exp_f32 v186, v186
ds_read_b128 v[64:67], v201 offset:12288
)ASM"
R"ASM(s_waitcnt lgkmcnt(4)
v_mfma_f32_32x32x16_bf16 v[32:47], v[168:171], a[32:35], v[32:47]
v_mul_f32 v190, v29, v54
v_exp_f32 v187, v187
ds_read_b128 v[164:167], v201 offset:13312
s_waitcnt lgkmcnt(4)
v_mfma_f32_32x32x16_bf16 v[32:47], v[172:175], a[36:39], v[32:47]
buffer_load_dwordx4 v3, s[20:23], s85 offen offset:3072 sc1 lds
v_mul_f32 v191, v29, v55
v_fma_f32 v184, v184, v28, v28
ds_read_b128 v[168:171], v201 offset:14336
s_waitcnt lgkmcnt(4)
v_mfma_f32_32x32x16_bf16 v[32:47], v[176:179], a[40:43], v[32:47]
v_exp_f32 v188, v188
v_fma_f32 v185, v185, v28, v28
ds_read_b128 v[172:175], v201 offset:15360
s_waitcnt lgkmcnt(4)
v_mfma_f32_32x32x16_bf16 v[32:47], v[180:183], a[44:47], v[32:47]
v_exp_f32 v189, v189
v_fma_f32 v186, v186, v28, v28
ds_read_b128 v[176:179], v201 offset:16384
s_waitcnt lgkmcnt(4)
v_mfma_f32_32x32x16_bf16 v[32:47], v[64:67], a[48:51], v[32:47]
s_add_u32 m0, s57, 4096
s_add_u32 s85, s58, 135168
buffer_load_dwordx4 v3, s[20:23], s85 offen offset:0 sc1 lds
ds_read_b128 v[180:183], v201 offset:17408
s_waitcnt lgkmcnt(4)
v_mfma_f32_32x32x16_bf16 v[32:47], v[164:167], a[52:55], v[32:47]
v_exp_f32 v190, v190
v_fma_f32 v187, v187, v28, v28
ds_read_b128 v[64:67], v201 offset:18432
)ASM"
R"ASM(s_waitcnt lgkmcnt(4)
v_mfma_f32_32x32x16_bf16 v[32:47], v[168:171], a[56:59], v[32:47]
buffer_load_dwordx4 v3, s[20:23], s85 offen offset:1024 sc1 lds
v_exp_f32 v191, v191
ds_read_b128 v[164:167], v201 offset:19456
s_waitcnt lgkmcnt(4)
v_mfma_f32_32x32x16_bf16 v[32:47], v[172:175], a[60:63], v[32:47]
v_rcp_f32 v184, v184
v_fma_f32 v188, v188, v28, v28
ds_read_b128 v[168:171], v201 offset:20480
s_waitcnt lgkmcnt(4)
v_mfma_f32_32x32x16_bf16 v[32:47], v[176:179], a[64:67], v[32:47]
v_rcp_f32 v185, v185
ds_read_b128 v[172:175], v201 offset:21504
s_waitcnt lgkmcnt(4)
v_mfma_f32_32x32x16_bf16 v[32:47], v[180:183], a[68:71], v[32:47]
buffer_load_dwordx4 v3, s[20:23], s85 offen offset:2048 sc1 lds
v_fma_f32 v189, v189, v28, v28
ds_read_b128 v[176:179], v201 offset:22528
s_waitcnt lgkmcnt(4)
v_mfma_f32_32x32x16_bf16 v[32:47], v[64:67], a[72:75], v[32:47]
v_rcp_f32 v186, v186
v_fma_f32 v190, v190, v28, v28
ds_read_b128 v[180:183], v201 offset:23552
s_waitcnt lgkmcnt(4)
v_mfma_f32_32x32x16_bf16 v[32:47], v[164:167], a[76:79], v[32:47]
buffer_load_dwordx4 v3, s[20:23], s85 offen offset:3072 sc1 lds
v_rcp_f32 v187, v187
ds_read_b128 v[64:67], v201 offset:24576
s_waitcnt lgkmcnt(4)
v_mfma_f32_32x32x16_bf16 v[32:47], v[168:171], a[80:83], v[32:47]
v_fma_f32 v191, v191, v28, v28
)ASM"
R"ASM(v_mul_f32 v184, v184, v56
ds_read_b128 v[164:167], v201 offset:25600
s_waitcnt lgkmcnt(4)
v_mfma_f32_32x32x16_bf16 v[32:47], v[172:175], a[84:87], v[32:47]
v_rcp_f32 v188, v188
ds_read_b128 v[168:171], v201 offset:26624
s_waitcnt lgkmcnt(4)
v_mfma_f32_32x32x16_bf16 v[32:47], v[176:179], a[88:91], v[32:47]
s_add_u32 m0, s57, 32768
s_add_u32 s85, s58, 163840
buffer_load_dwordx4 v3, s[20:23], s85 offen offset:0 sc1 lds
ds_read_b128 v[172:175], v201 offset:27648
s_waitcnt lgkmcnt(4)
v_mfma_f32_32x32x16_bf16 v[32:47], v[180:183], a[92:95], v[32:47]
v_mul_f32 v185, v185, v57
v_rcp_f32 v189, v189
ds_read_b128 v[176:179], v201 offset:28672
s_waitcnt lgkmcnt(4)
v_mfma_f32_32x32x16_bf16 v[32:47], v[64:67], a[96:99], v[32:47]
buffer_load_dwordx4 v3, s[20:23], s85 offen offset:1024 sc1 lds
v_mul_f32 v186, v186, v58
ds_read_b128 v[180:183], v201 offset:29696
s_waitcnt lgkmcnt(4)
v_mfma_f32_32x32x16_bf16 v[32:47], v[164:167], a[100:103], v[32:47]
v_rcp_f32 v190, v190
v_mul_f32 v187, v187, v59
ds_read_b128 v[64:67], v201 offset:30720
s_waitcnt lgkmcnt(4)
v_mfma_f32_32x32x16_bf16 v[32:47], v[168:171], a[104:107], v[32:47]
v_rcp_f32 v191, v191
v_mul_f32 v188, v188, v60
ds_read_b128 v[164:167], v201 offset:31744
)ASM"
R"ASM(s_waitcnt lgkmcnt(4)
v_mfma_f32_32x32x16_bf16 v[32:47], v[172:175], a[108:111], v[32:47]
buffer_load_dwordx4 v3, s[20:23], s85 offen offset:2048 sc1 lds
v_cvt_pk_bf16_f32 v192, v184, v185
v_mul_f32 v189, v189, v61
ds_read_b128 v[168:171], v202 offset:2048
s_waitcnt lgkmcnt(4)
v_mfma_f32_32x32x16_bf16 v[32:47], v[176:179], a[112:115], v[32:47]
v_cvt_pk_bf16_f32 v193, v186, v187
v_mul_f32 v190, v190, v62
ds_read_b128 v[172:175], v201 offset:32768
s_waitcnt lgkmcnt(4)
v_mfma_f32_32x32x16_bf16 v[32:47], v[180:183], a[116:119], v[32:47]
buffer_load_dwordx4 v3, s[20:23], s85 offen offset:3072 sc1 lds
v_mul_f32 v191, v191, v63
v_cvt_pk_bf16_f32 v194, v188, v189
ds_read_b128 v[176:179], v201 offset:33792
s_waitcnt lgkmcnt(4)
v_mfma_f32_32x32x16_bf16 v[32:47], v[64:67], a[120:123], v[32:47]
v_cvt_pk_bf16_f32 v195, v190, v191
s_add_u32 s84, s73, 16384
buffer_store_dwordx4 v[192:195], v3, s[20:23], s84 offen sc1
ds_read_b128 v[180:183], v201 offset:34816
s_waitcnt lgkmcnt(4)
v_mfma_f32_32x32x16_bf16 v[32:47], v[164:167], a[124:127], v[32:47]
ds_read_b128 v[64:67], v201 offset:35840
s_waitcnt lgkmcnt(4)
v_mfma_f32_32x32x16_bf16 v[32:47], v[168:171], v[196:199], v[32:47]
ds_read_b128 v[164:167], v201 offset:36864
s_waitcnt lgkmcnt(4)
v_mfma_f32_32x32x16_bf16 v[48:63], v[172:175], a[0:3], 0
ds_read_b128 v[168:171], v201 offset:37888
)ASM"
R"ASM(s_waitcnt lgkmcnt(4)
v_mfma_f32_32x32x16_bf16 v[48:63], v[176:179], a[4:7], v[48:63]
ds_read_b128 v[172:175], v201 offset:38912
s_waitcnt lgkmcnt(4)
v_mfma_f32_32x32x16_bf16 v[48:63], v[180:183], a[8:11], v[48:63]
s_add_u32 m0, s57, 36864
s_add_u32 s85, s58, 167936
buffer_load_dwordx4 v3, s[20:23], s85 offen offset:0 sc1 lds
ds_read_b128 v[176:179], v201 offset:39936
s_waitcnt lgkmcnt(4)
v_mfma_f32_32x32x16_bf16 v[48:63], v[64:67], a[12:15], v[48:63]
v_mul_f32 v184, v29, v32
v_mul_f32 v185, v29, v33
v_mul_f32 v186, v29, v34
ds_read_b128 v[180:183], v201 offset:40960
s_waitcnt lgkmcnt(4)
v_mfma_f32_32x32x16_bf16 v[48:63], v[164:167], a[16:19], v[48:63]
buffer_load_dwordx4 v3, s[20:23], s85 offen offset:1024 sc1 lds
v_mul_f32 v187, v29, v35
ds_read_b128 v[64:67], v201 offset:41984
s_waitcnt lgkmcnt(4)
v_mfma_f32_32x32x16_bf16 v[48:63], v[168:171], a[20:23], v[48:63]
v_exp_f32 v184, v184
v_mul_f32 v188, v29, v36
ds_read_b128 v[164:167], v201 offset:43008
s_waitcnt lgkmcnt(4)
v_mfma_f32_32x32x16_bf16 v[48:63], v[172:175], a[24:27], v[48:63]
buffer_load_dwordx4 v3, s[20:23], s85 offen offset:2048 sc1 lds
v_exp_f32 v185, v185
ds_read_b128 v[168:171], v201 offset:44032
s_waitcnt lgkmcnt(4)
v_mfma_f32_32x32x16_bf16 v[48:63], v[176:179], a[28:31], v[48:63]
)ASM"
R"ASM(v_mul_f32 v189, v29, v37
v_exp_f32 v186, v186
ds_read_b128 v[172:175], v201 offset:45056
s_waitcnt lgkmcnt(4)
v_mfma_f32_32x32x16_bf16 v[48:63], v[180:183], a[32:35], v[48:63]
buffer_load_dwordx4 v3, s[20:23], s85 offen offset:3072 sc1 lds
v_mul_f32 v190, v29, v38
ds_read_b128 v[176:179], v201 offset:46080
s_waitcnt lgkmcnt(4)
v_mfma_f32_32x32x16_bf16 v[48:63], v[64:67], a[36:39], v[48:63]
v_exp_f32 v187, v187
v_mul_f32 v191, v29, v39
ds_read_b128 v[180:183], v201 offset:47104
s_waitcnt lgkmcnt(4)
v_mfma_f32_32x32x16_bf16 v[48:63], v[164:167], a[40:43], v[48:63]
s_add_u32 s86, s74, 0
buffer_load_dwordx4 a[128:131], v3, s[20:23], s86 offen offset:0 sc1
v_fma_f32 v184, v184, v28, v28
ds_read_b128 v[64:67], v201 offset:48128
s_waitcnt lgkmcnt(4)
v_mfma_f32_32x32x16_bf16 v[48:63], v[168:171], a[44:47], v[48:63]
v_exp_f32 v188, v188
v_fma_f32 v185, v185, v28, v28
ds_read_b128 v[164:167], v201 offset:49152
s_waitcnt lgkmcnt(4)
v_mfma_f32_32x32x16_bf16 v[48:63], v[172:175], a[48:51], v[48:63]
v_exp_f32 v189, v189
v_fma_f32 v186, v186, v28, v28
ds_read_b128 v[168:171], v201 offset:50176
s_waitcnt lgkmcnt(4)
v_mfma_f32_32x32x16_bf16 v[48:63], v[176:179], a[52:55], v[48:63]
v_exp_f32 v190, v190
)ASM"
R"ASM(v_fma_f32 v187, v187, v28, v28
ds_read_b128 v[172:175], v201 offset:51200
s_waitcnt lgkmcnt(4)
v_mfma_f32_32x32x16_bf16 v[48:63], v[180:183], a[56:59], v[48:63]
v_exp_f32 v191, v191
v_rcp_f32 v184, v184
ds_read_b128 v[176:179], v201 offset:52224
s_waitcnt lgkmcnt(4)
v_mfma_f32_32x32x16_bf16 v[48:63], v[64:67], a[60:63], v[48:63]
s_add_u32 s87, s75, 0
buffer_load_dwordx4 a[132:135], v3, s[20:23], s87 offen offset:0 sc1
v_fma_f32 v188, v188, v28, v28
ds_read_b128 v[180:183], v201 offset:53248
s_waitcnt lgkmcnt(4)
v_mfma_f32_32x32x16_bf16 v[48:63], v[164:167], a[64:67], v[48:63]
v_rcp_f32 v185, v185
ds_read_b128 v[64:67], v201 offset:54272
s_waitcnt lgkmcnt(4)
v_mfma_f32_32x32x16_bf16 v[48:63], v[168:171], a[68:71], v[48:63]
v_fma_f32 v189, v189, v28, v28
v_rcp_f32 v186, v186
ds_read_b128 v[164:167], v201 offset:55296
s_waitcnt lgkmcnt(4)
v_mfma_f32_32x32x16_bf16 v[48:63], v[172:175], a[72:75], v[48:63]
v_fma_f32 v190, v190, v28, v28
v_rcp_f32 v187, v187
ds_read_b128 v[168:171], v201 offset:56320
s_waitcnt lgkmcnt(4)
v_mfma_f32_32x32x16_bf16 v[48:63], v[176:179], a[76:79], v[48:63]
v_fma_f32 v191, v191, v28, v28
v_mul_f32 v184, v184, v40
ds_read_b128 v[172:175], v201 offset:57344
)ASM"
R"ASM(s_waitcnt lgkmcnt(4)
v_mfma_f32_32x32x16_bf16 v[48:63], v[180:183], a[80:83], v[48:63]
buffer_load_dwordx4 a[136:139], v3, s[20:23], s86 offen offset:2048 sc1
v_rcp_f32 v188, v188
ds_read_b128 v[176:179], v201 offset:58368
s_waitcnt lgkmcnt(4)
v_mfma_f32_32x32x16_bf16 v[48:63], v[64:67], a[84:87], v[48:63]
v_mul_f32 v185, v185, v41
v_rcp_f32 v189, v189
ds_read_b128 v[180:183], v201 offset:59392
s_waitcnt lgkmcnt(4)
v_mfma_f32_32x32x16_bf16 v[48:63], v[164:167], a[88:91], v[48:63]
v_mul_f32 v186, v186, v42
v_rcp_f32 v190, v190
ds_read_b128 v[64:67], v201 offset:60416
s_waitcnt lgkmcnt(4)
v_mfma_f32_32x32x16_bf16 v[48:63], v[168:171], a[92:95], v[48:63]
v_mul_f32 v187, v187, v43
v_rcp_f32 v191, v191
ds_read_b128 v[164:167], v201 offset:61440
s_waitcnt lgkmcnt(4)
v_mfma_f32_32x32x16_bf16 v[48:63], v[172:175], a[96:99], v[48:63]
v_mul_f32 v188, v188, v44
v_cvt_pk_bf16_f32 v192, v184, v185
ds_read_b128 v[168:171], v201 offset:62464
s_waitcnt lgkmcnt(4)
v_mfma_f32_32x32x16_bf16 v[48:63], v[176:179], a[100:103], v[48:63]
buffer_load_dwordx4 a[140:143], v3, s[20:23], s87 offen offset:2048 sc1
v_mul_f32 v189, v189, v45
v_cvt_pk_bf16_f32 v193, v186, v187
ds_read_b128 v[172:175], v201 offset:63488
s_waitcnt lgkmcnt(4)
)ASM"
R"ASM(v_mfma_f32_32x32x16_bf16 v[48:63], v[180:183], a[104:107], v[48:63]
v_mul_f32 v190, v190, v46
v_mul_f32 v191, v191, v47
ds_read_b128 v[176:179], v201 offset:64512
s_waitcnt lgkmcnt(4)
v_mfma_f32_32x32x16_bf16 v[48:63], v[64:67], a[108:111], v[48:63]
v_cvt_pk_bf16_f32 v194, v188, v189
ds_read_b128 v[180:183], v202 offset:3072
s_waitcnt lgkmcnt(4)
v_mfma_f32_32x32x16_bf16 v[48:63], v[164:167], a[112:115], v[48:63]
s_waitcnt vmcnt(4)
s_barrier
v_cvt_pk_bf16_f32 v195, v190, v191
ds_read_b128 v[64:67], v200 offset:0
s_waitcnt lgkmcnt(4)
v_mfma_f32_32x32x16_bf16 v[48:63], v[168:171], a[116:119], v[48:63]
s_add_u32 s84, s73, 32768
buffer_store_dwordx4 v[192:195], v3, s[20:23], s84 offen sc1
ds_read_b128 v[164:167], v200 offset:1024
s_waitcnt lgkmcnt(4)
v_mfma_f32_32x32x16_bf16 v[48:63], v[172:175], a[120:123], v[48:63]
ds_read_b128 v[168:171], v200 offset:2048
s_waitcnt lgkmcnt(4)
v_mfma_f32_32x32x16_bf16 v[48:63], v[176:179], a[124:127], v[48:63]
ds_read_b128 v[172:175], v200 offset:3072
s_waitcnt lgkmcnt(4)
v_mfma_f32_32x32x16_bf16 v[48:63], v[180:183], v[196:199], v[48:63]
ds_read_b128 v[176:179], v200 offset:4096
s_waitcnt lgkmcnt(4)
v_mfma_f32_32x32x16_bf16 v[32:47], v[64:67], a[0:3], 0
ds_read_b128 v[180:183], v200 offset:5120
s_waitcnt lgkmcnt(4)
)ASM"
R"ASM(v_mfma_f32_32x32x16_bf16 v[32:47], v[164:167], a[4:7], v[32:47]
ds_read_b128 v[64:67], v200 offset:6144
s_waitcnt lgkmcnt(4)
v_mfma_f32_32x32x16_bf16 v[32:47], v[168:171], a[8:11], v[32:47]
s_add_u32 m0, s57, 65536
s_add_u32 s85, s58, 196608
buffer_load_dwordx4 v3, s[20:23], s85 offen offset:0 sc1 lds
ds_read_b128 v[164:167], v200 offset:7168
s_waitcnt lgkmcnt(4)
v_mfma_f32_32x32x16_bf16 v[32:47], v[172:175], a[12:15], v[32:47]
v_mul_f32 v184, v29, v48
v_mul_f32 v185, v29, v49
v_mul_f32 v186, v29, v50
ds_read_b128 v[168:171], v200 offset:8192
s_waitcnt lgkmcnt(4)
v_mfma_f32_32x32x16_bf16 v[32:47], v[176:179], a[16:19], v[32:47]
buffer_load_dwordx4 v3, s[20:23], s85 offen offset:1024 sc1 lds
v_mul_f32 v187, v29, v51
ds_read_b128 v[172:175], v200 offset:9216
s_waitcnt lgkmcnt(4)
v_mfma_f32_32x32x16_bf16 v[32:47], v[180:183], a[20:23], v[32:47]
v_exp_f32 v184, v184
v_mul_f32 v188, v29, v52
ds_read_b128 v[176:179], v200 offset:10240
s_waitcnt lgkmcnt(4)
v_mfma_f32_32x32x16_bf16 v[32:47], v[64:67], a[24:27], v[32:47]
buffer_load_dwordx4 v3, s[20:23], s85 offen offset:2048 sc1 lds
v_exp_f32 v185, v185
ds_read_b128 v[180:183], v200 offset:11264
s_waitcnt lgkmcnt(4)
v_mfma_f32_32x32x16_bf16 v[32:47], v[164:167], a[28:31], v[32:47]
v_mul_f32 v189, v29, v53
)ASM"
R"ASM(v_exp_f32 v186, v186
ds_read_b128 v[64:67], v200 offset:12288
s_waitcnt lgkmcnt(4)
v_mfma_f32_32x32x16_bf16 v[32:47], v[168:171], a[32:35], v[32:47]
v_mul_f32 v190, v29, v54
v_exp_f32 v187, v187
ds_read_b128 v[164:167], v200 offset:13312
s_waitcnt lgkmcnt(4)
v_mfma_f32_32x32x16_bf16 v[32:47], v[172:175], a[36:39], v[32:47]
buffer_load_dwordx4 v3, s[20:23], s85 offen offset:3072 sc1 lds
v_mul_f32 v191, v29, v55
v_fma_f32 v184, v184, v28, v28
ds_read_b128 v[168:171], v200 offset:14336
s_waitcnt lgkmcnt(4)
v_mfma_f32_32x32x16_bf16 v[32:47], v[176:179], a[40:43], v[32:47]
v_exp_f32 v188, v188
v_fma_f32 v185, v185, v28, v28
ds_read_b128 v[172:175], v200 offset:15360
s_waitcnt lgkmcnt(4)
v_mfma_f32_32x32x16_bf16 v[32:47], v[180:183], a[44:47], v[32:47]
s_add_u32 m0, s57, 69632
s_add_u32 s85, s58, 200704
buffer_load_dwordx4 v3, s[20:23], s85 offen offset:0 sc1 lds
ds_read_b128 v[176:179], v200 offset:16384
s_waitcnt lgkmcnt(4)
v_mfma_f32_32x32x16_bf16 v[32:47], v[64:67], a[48:51], v[32:47]
v_exp_f32 v189, v189
v_fma_f32 v186, v186, v28, v28
ds_read_b128 v[180:183], v200 offset:17408
s_waitcnt lgkmcnt(4)
v_mfma_f32_32x32x16_bf16 v[32:47], v[164:167], a[52:55], v[32:47]
buffer_load_dwordx4 v3, s[20:23], s85 offen offset:1024 sc1 lds
)ASM"
R"ASM(v_exp_f32 v190, v190
ds_read_b128 v[64:67], v200 offset:18432
s_waitcnt lgkmcnt(4)
v_mfma_f32_32x32x16_bf16 v[32:47], v[168:171], a[56:59], v[32:47]
v_fma_f32 v187, v187, v28, v28
v_exp_f32 v191, v191
ds_read_b128 v[164:167], v200 offset:19456
s_waitcnt lgkmcnt(4)
v_mfma_f32_32x32x16_bf16 v[32:47], v[172:175], a[60:63], v[32:47]
v_rcp_f32 v184, v184
v_fma_f32 v188, v188, v28, v28
ds_read_b128 v[168:171], v200 offset:20480
s_waitcnt lgkmcnt(4)
v_mfma_f32_32x32x16_bf16 v[32:47], v[176:179], a[64:67], v[32:47]
buffer_load_dwordx4 v3, s[20:23], s85 offen offset:2048 sc1 lds
v_rcp_f32 v185, v185
ds_read_b128 v[172:175], v200 offset:21504
s_waitcnt lgkmcnt(4)
v_mfma_f32_32x32x16_bf16 v[32:47], v[180:183], a[68:71], v[32:47]
v_fma_f32 v189, v189, v28, v28
v_rcp_f32 v186, v186
ds_read_b128 v[176:179], v200 offset:22528
s_waitcnt lgkmcnt(4)
v_mfma_f32_32x32x16_bf16 v[32:47], v[64:67], a[72:75], v[32:47]
buffer_load_dwordx4 v3, s[20:23], s85 offen offset:3072 sc1 lds
v_fma_f32 v190, v190, v28, v28
ds_read_b128 v[180:183], v200 offset:23552
s_waitcnt lgkmcnt(4)
v_mfma_f32_32x32x16_bf16 v[32:47], v[164:167], a[76:79], v[32:47]
v_rcp_f32 v187, v187
ds_read_b128 v[64:67], v200 offset:24576
s_waitcnt lgkmcnt(4)
)ASM"
R"ASM(v_mfma_f32_32x32x16_bf16 v[32:47], v[168:171], a[80:83], v[32:47]
s_add_u32 s86, s74, 4096
buffer_load_dwordx4 a[144:147], v3, s[20:23], s86 offen offset:0 sc1
v_fma_f32 v191, v191, v28, v28
ds_read_b128 v[164:167], v200 offset:25600
s_waitcnt lgkmcnt(4)
v_mfma_f32_32x32x16_bf16 v[32:47], v[172:175], a[84:87], v[32:47]
v_mul_f32 v184, v184, v56
v_rcp_f32 v188, v188
ds_read_b128 v[168:171], v200 offset:26624
s_waitcnt lgkmcnt(4)
v_mfma_f32_32x32x16_bf16 v[32:47], v[176:179], a[88:91], v[32:47]
v_mul_f32 v185, v185, v57
v_rcp_f32 v189, v189
ds_read_b128 v[172:175], v200 offset:27648
s_waitcnt lgkmcnt(4)
v_mfma_f32_32x32x16_bf16 v[32:47], v[180:183], a[92:95], v[32:47]
s_add_u32 s87, s75, 4096
buffer_load_dwordx4 a[148:151], v3, s[20:23], s87 offen offset:0 sc1
v_mul_f32 v186, v186, v58
ds_read_b128 v[176:179], v200 offset:28672
s_waitcnt lgkmcnt(4)
v_mfma_f32_32x32x16_bf16 v[32:47], v[64:67], a[96:99], v[32:47]
v_rcp_f32 v190, v190
ds_read_b128 v[180:183], v200 offset:29696
s_waitcnt lgkmcnt(4)
v_mfma_f32_32x32x16_bf16 v[32:47], v[164:167], a[100:103], v[32:47]
buffer_load_dwordx4 a[152:155], v3, s[20:23], s86 offen offset:2048 sc1
v_mul_f32 v187, v187, v59
ds_read_b128 v[64:67], v200 offset:30720
s_waitcnt lgkmcnt(4)
v_mfma_f32_32x32x16_bf16 v[32:47], v[168:171], a[104:107], v[32:47]
)ASM"
R"ASM(v_rcp_f32 v191, v191
v_mul_f32 v188, v188, v60
ds_read_b128 v[164:167], v200 offset:31744
s_waitcnt lgkmcnt(4)
v_mfma_f32_32x32x16_bf16 v[32:47], v[172:175], a[108:111], v[32:47]
buffer_load_dwordx4 a[156:159], v3, s[20:23], s87 offen offset:2048 sc1
v_cvt_pk_bf16_f32 v192, v184, v185
v_mul_f32 v189, v189, v61
ds_read_b128 v[168:171], v202 offset:4096
s_waitcnt lgkmcnt(4)
v_mfma_f32_32x32x16_bf16 v[32:47], v[176:179], a[112:115], v[32:47]
v_cvt_pk_bf16_f32 v193, v186, v187
v_mul_f32 v190, v190, v62
ds_read_b128 v[172:175], v200 offset:32768
s_waitcnt lgkmcnt(4)
v_mfma_f32_32x32x16_bf16 v[32:47], v[180:183], a[116:119], v[32:47]
v_mul_f32 v191, v191, v63
v_cvt_pk_bf16_f32 v194, v188, v189
ds_read_b128 v[176:179], v200 offset:33792
s_waitcnt lgkmcnt(4)
v_mfma_f32_32x32x16_bf16 v[32:47], v[64:67], a[120:123], v[32:47]
v_cvt_pk_bf16_f32 v195, v190, v191
s_add_u32 s84, s73, 49152
buffer_store_dwordx4 v[192:195], v3, s[20:23], s84 offen sc1
ds_read_b128 v[180:183], v200 offset:34816
s_waitcnt lgkmcnt(4)
v_mfma_f32_32x32x16_bf16 v[32:47], v[164:167], a[124:127], v[32:47]
ds_read_b128 v[64:67], v200 offset:35840
s_waitcnt lgkmcnt(4)
v_mfma_f32_32x32x16_bf16 v[32:47], v[168:171], v[196:199], v[32:47]
ds_read_b128 v[164:167], v200 offset:36864
s_waitcnt lgkmcnt(4)
)ASM"
R"ASM(v_mfma_f32_32x32x16_bf16 v[48:63], v[172:175], a[0:3], 0
ds_read_b128 v[168:171], v200 offset:37888
s_waitcnt lgkmcnt(4)
v_mfma_f32_32x32x16_bf16 v[48:63], v[176:179], a[4:7], v[48:63]
ds_read_b128 v[172:175], v200 offset:38912
s_waitcnt lgkmcnt(4)
v_mfma_f32_32x32x16_bf16 v[48:63], v[180:183], a[8:11], v[48:63]
s_add_u32 s86, s74, 32768
buffer_load_dwordx4 a[160:163], v3, s[20:23], s86 offen offset:0 sc1
ds_read_b128 v[176:179], v200 offset:39936
s_waitcnt lgkmcnt(4)
v_mfma_f32_32x32x16_bf16 v[48:63], v[64:67], a[12:15], v[48:63]
v_mul_f32 v184, v29, v32
v_mul_f32 v185, v29, v33
v_mul_f32 v186, v29, v34
ds_read_b128 v[180:183], v200 offset:40960
s_waitcnt lgkmcnt(4)
v_mfma_f32_32x32x16_bf16 v[48:63], v[164:167], a[16:19], v[48:63]
s_add_u32 s87, s75, 32768
buffer_load_dwordx4 a[164:167], v3, s[20:23], s87 offen offset:0 sc1
v_mul_f32 v187, v29, v35
ds_read_b128 v[64:67], v200 offset:41984
s_waitcnt lgkmcnt(4)
v_mfma_f32_32x32x16_bf16 v[48:63], v[168:171], a[20:23], v[48:63]
v_exp_f32 v184, v184
v_mul_f32 v188, v29, v36
ds_read_b128 v[164:167], v200 offset:43008
s_waitcnt lgkmcnt(4)
v_mfma_f32_32x32x16_bf16 v[48:63], v[172:175], a[24:27], v[48:63]
buffer_load_dwordx4 a[168:171], v3, s[20:23], s86 offen offset:2048 sc1
v_exp_f32 v185, v185
ds_read_b128 v[168:171], v200 offset:44032
)ASM"
R"ASM(s_waitcnt lgkmcnt(4)
v_mfma_f32_32x32x16_bf16 v[48:63], v[176:179], a[28:31], v[48:63]
v_mul_f32 v189, v29, v37
v_exp_f32 v186, v186
ds_read_b128 v[172:175], v200 offset:45056
s_waitcnt lgkmcnt(4)
v_mfma_f32_32x32x16_bf16 v[48:63], v[180:183], a[32:35], v[48:63]
v_mul_f32 v190, v29, v38
v_exp_f32 v187, v187
ds_read_b128 v[176:179], v200 offset:46080
s_waitcnt lgkmcnt(4)
v_mfma_f32_32x32x16_bf16 v[48:63], v[64:67], a[36:39], v[48:63]
buffer_load_dwordx4 a[172:175], v3, s[20:23], s87 offen offset:2048 sc1
v_mul_f32 v191, v29, v39
v_fma_f32 v184, v184, v28, v28
ds_read_b128 v[180:183], v200 offset:47104
s_waitcnt lgkmcnt(4)
v_mfma_f32_32x32x16_bf16 v[48:63], v[164:167], a[40:43], v[48:63]
v_exp_f32 v188, v188
v_fma_f32 v185, v185, v28, v28
ds_read_b128 v[64:67], v200 offset:48128
s_waitcnt lgkmcnt(4)
v_mfma_f32_32x32x16_bf16 v[48:63], v[168:171], a[44:47], v[48:63]
s_add_u32 s86, s74, 36864
buffer_load_dwordx4 a[176:179], v3, s[20:23], s86 offen offset:0 sc1
ds_read_b128 v[164:167], v200 offset:49152
s_waitcnt lgkmcnt(4)
v_mfma_f32_32x32x16_bf16 v[48:63], v[172:175], a[48:51], v[48:63]
v_exp_f32 v189, v189
v_fma_f32 v186, v186, v28, v28
ds_read_b128 v[168:171], v200 offset:50176
s_waitcnt lgkmcnt(4)
)ASM"
R"ASM(v_mfma_f32_32x32x16_bf16 v[48:63], v[176:179], a[52:55], v[48:63]
s_add_u32 s87, s75, 36864
buffer_load_dwordx4 a[180:183], v3, s[20:23], s87 offen offset:0 sc1
ds_read_b128 v[172:175], v200 offset:51200
s_waitcnt lgkmcnt(4)
v_mfma_f32_32x32x16_bf16 v[48:63], v[180:183], a[56:59], v[48:63]
v_exp_f32 v190, v190
v_fma_f32 v187, v187, v28, v28
ds_read_b128 v[176:179], v200 offset:52224
s_waitcnt lgkmcnt(4)
v_mfma_f32_32x32x16_bf16 v[48:63], v[64:67], a[60:63], v[48:63]
v_exp_f32 v191, v191
v_rcp_f32 v184, v184
ds_read_b128 v[180:183], v200 offset:53248
s_waitcnt lgkmcnt(4)
v_mfma_f32_32x32x16_bf16 v[48:63], v[164:167], a[64:67], v[48:63]
buffer_load_dwordx4 a[184:187], v3, s[20:23], s86 offen offset:2048 sc1
v_fma_f32 v188, v188, v28, v28
ds_read_b128 v[64:67], v200 offset:54272
s_waitcnt lgkmcnt(4)
v_mfma_f32_32x32x16_bf16 v[48:63], v[168:171], a[68:71], v[48:63]
v_rcp_f32 v185, v185
v_fma_f32 v189, v189, v28, v28
ds_read_b128 v[164:167], v200 offset:55296
s_waitcnt lgkmcnt(4)
v_mfma_f32_32x32x16_bf16 v[48:63], v[172:175], a[72:75], v[48:63]
buffer_load_dwordx4 a[188:191], v3, s[20:23], s87 offen offset:2048 sc1
v_rcp_f32 v186, v186
ds_read_b128 v[168:171], v200 offset:56320
s_waitcnt lgkmcnt(4)
v_mfma_f32_32x32x16_bf16 v[48:63], v[176:179], a[76:79], v[48:63]
v_fma_f32 v190, v190, v28, v28
)ASM"
R"ASM(v_rcp_f32 v187, v187
ds_read_b128 v[172:175], v200 offset:57344
s_waitcnt lgkmcnt(4)
v_mfma_f32_32x32x16_bf16 v[48:63], v[180:183], a[80:83], v[48:63]
s_add_u32 s86, s74, 65536
buffer_load_dwordx4 a[192:195], v3, s[20:23], s86 offen offset:0 sc1
v_fma_f32 v191, v191, v28, v28
ds_read_b128 v[176:179], v200 offset:58368
s_waitcnt lgkmcnt(4)
v_mfma_f32_32x32x16_bf16 v[48:63], v[64:67], a[84:87], v[48:63]
v_mul_f32 v184, v184, v40
v_rcp_f32 v188, v188
ds_read_b128 v[180:183], v200 offset:59392
s_waitcnt lgkmcnt(4)
v_mfma_f32_32x32x16_bf16 v[48:63], v[164:167], a[88:91], v[48:63]
v_mul_f32 v185, v185, v41
v_rcp_f32 v189, v189
ds_read_b128 v[64:67], v200 offset:60416
s_waitcnt lgkmcnt(4)
v_mfma_f32_32x32x16_bf16 v[48:63], v[168:171], a[92:95], v[48:63]
s_add_u32 s87, s75, 65536
buffer_load_dwordx4 a[196:199], v3, s[20:23], s87 offen offset:0 sc1
v_mul_f32 v186, v186, v42
ds_read_b128 v[164:167], v200 offset:61440
s_waitcnt lgkmcnt(4)
v_mfma_f32_32x32x16_bf16 v[48:63], v[172:175], a[96:99], v[48:63]
v_rcp_f32 v190, v190
ds_read_b128 v[168:171], v200 offset:62464
s_waitcnt lgkmcnt(4)
v_mfma_f32_32x32x16_bf16 v[48:63], v[176:179], a[100:103], v[48:63]
buffer_load_dwordx4 a[200:203], v3, s[20:23], s86 offen offset:2048 sc1
v_mul_f32 v187, v187, v43
)ASM"
R"ASM(ds_read_b128 v[172:175], v200 offset:63488
s_waitcnt lgkmcnt(4)
v_mfma_f32_32x32x16_bf16 v[48:63], v[180:183], a[104:107], v[48:63]
v_rcp_f32 v191, v191
v_mul_f32 v188, v188, v44
ds_read_b128 v[176:179], v200 offset:64512
s_waitcnt lgkmcnt(4)
v_mfma_f32_32x32x16_bf16 v[48:63], v[64:67], a[108:111], v[48:63]
buffer_load_dwordx4 a[204:207], v3, s[20:23], s87 offen offset:2048 sc1
v_cvt_pk_bf16_f32 v192, v184, v185
v_mul_f32 v189, v189, v45
ds_read_b128 v[180:183], v202 offset:5120
s_waitcnt lgkmcnt(4)
v_mfma_f32_32x32x16_bf16 v[48:63], v[164:167], a[112:115], v[48:63]
s_waitcnt vmcnt(17)
s_barrier
v_cvt_pk_bf16_f32 v193, v186, v187
v_mul_f32 v190, v190, v46
ds_read_b128 v[64:67], v201 offset:0
s_waitcnt lgkmcnt(4)
v_mfma_f32_32x32x16_bf16 v[48:63], v[168:171], a[116:119], v[48:63]
v_mul_f32 v191, v191, v47
v_cvt_pk_bf16_f32 v194, v188, v189
ds_read_b128 v[164:167], v201 offset:1024
s_waitcnt lgkmcnt(4)
v_mfma_f32_32x32x16_bf16 v[48:63], v[172:175], a[120:123], v[48:63]
v_cvt_pk_bf16_f32 v195, v190, v191
s_add_u32 s84, s73, 65536
buffer_store_dwordx4 v[192:195], v3, s[20:23], s84 offen sc1
ds_read_b128 v[168:171], v201 offset:2048
s_waitcnt lgkmcnt(4)
v_mfma_f32_32x32x16_bf16 v[48:63], v[176:179], a[124:127], v[48:63]
)ASM"
R"ASM(ds_read_b128 v[172:175], v201 offset:3072
s_waitcnt lgkmcnt(4)
v_mfma_f32_32x32x16_bf16 v[48:63], v[180:183], v[196:199], v[48:63]
ds_read_b128 v[176:179], v201 offset:4096
s_waitcnt lgkmcnt(4)
v_mfma_f32_32x32x16_bf16 v[32:47], v[64:67], a[0:3], 0
s_add_u32 s50, s48, 0
buffer_load_dwordx4 a[0:3], v3, s[20:23], s50 offen offset:0 sc1
ds_read_b128 v[180:183], v201 offset:5120
s_waitcnt lgkmcnt(4)
v_mfma_f32_32x32x16_bf16 v[32:47], v[164:167], a[4:7], v[32:47]
ds_read_b128 v[64:67], v201 offset:6144
s_waitcnt lgkmcnt(4)
v_mfma_f32_32x32x16_bf16 v[32:47], v[168:171], a[8:11], v[32:47]
s_add_u32 s51, s49, 0
buffer_load_dwordx4 a[4:7], v3, s[20:23], s51 offen offset:0 sc1
ds_read_b128 v[164:167], v201 offset:7168
s_waitcnt lgkmcnt(4)
v_mfma_f32_32x32x16_bf16 v[32:47], v[172:175], a[12:15], v[32:47]
v_mul_f32 v184, v29, v48
v_mul_f32 v185, v29, v49
v_mul_f32 v186, v29, v50
ds_read_b128 v[168:171], v201 offset:8192
s_waitcnt lgkmcnt(4)
v_mfma_f32_32x32x16_bf16 v[32:47], v[176:179], a[16:19], v[32:47]
buffer_load_dwordx4 a[8:11], v3, s[20:23], s50 offen offset:1024 sc1
v_mul_f32 v187, v29, v51
ds_read_b128 v[172:175], v201 offset:9216
s_waitcnt lgkmcnt(4)
v_mfma_f32_32x32x16_bf16 v[32:47], v[180:183], a[20:23], v[32:47]
v_exp_f32 v184, v184
v_mul_f32 v188, v29, v52
)ASM"
R"ASM(ds_read_b128 v[176:179], v201 offset:10240
s_waitcnt lgkmcnt(4)
v_mfma_f32_32x32x16_bf16 v[32:47], v[64:67], a[24:27], v[32:47]
buffer_load_dwordx4 a[12:15], v3, s[20:23], s51 offen offset:1024 sc1
v_exp_f32 v185, v185
ds_read_b128 v[180:183], v201 offset:11264
s_waitcnt lgkmcnt(4)
v_mfma_f32_32x32x16_bf16 v[32:47], v[164:167], a[28:31], v[32:47]
v_mul_f32 v189, v29, v53
v_exp_f32 v186, v186
ds_read_b128 v[64:67], v201 offset:12288
s_waitcnt lgkmcnt(4)
v_mfma_f32_32x32x16_bf16 v[32:47], v[168:171], a[32:35], v[32:47]
buffer_load_dwordx4 a[16:19], v3, s[20:23], s50 offen offset:2048 sc1
v_mul_f32 v190, v29, v54
ds_read_b128 v[164:167], v201 offset:13312
s_waitcnt lgkmcnt(4)
v_mfma_f32_32x32x16_bf16 v[32:47], v[172:175], a[36:39], v[32:47]
v_exp_f32 v187, v187
v_mul_f32 v191, v29, v55
ds_read_b128 v[168:171], v201 offset:14336
s_waitcnt lgkmcnt(4)
v_mfma_f32_32x32x16_bf16 v[32:47], v[176:179], a[40:43], v[32:47]
buffer_load_dwordx4 a[20:23], v3, s[20:23], s51 offen offset:2048 sc1
v_fma_f32 v184, v184, v28, v28
ds_read_b128 v[172:175], v201 offset:15360
s_waitcnt lgkmcnt(4)
v_mfma_f32_32x32x16_bf16 v[32:47], v[180:183], a[44:47], v[32:47]
v_exp_f32 v188, v188
v_fma_f32 v185, v185, v28, v28
ds_read_b128 v[176:179], v201 offset:16384
s_waitcnt lgkmcnt(4)
)ASM"
R"ASM(v_mfma_f32_32x32x16_bf16 v[32:47], v[64:67], a[48:51], v[32:47]
buffer_load_dwordx4 a[24:27], v3, s[20:23], s50 offen offset:3072 sc1
v_exp_f32 v189, v189
ds_read_b128 v[180:183], v201 offset:17408
s_waitcnt lgkmcnt(4)
v_mfma_f32_32x32x16_bf16 v[32:47], v[164:167], a[52:55], v[32:47]
v_fma_f32 v186, v186, v28, v28
v_exp_f32 v190, v190
ds_read_b128 v[64:67], v201 offset:18432
s_waitcnt lgkmcnt(4)
v_mfma_f32_32x32x16_bf16 v[32:47], v[168:171], a[56:59], v[32:47]
buffer_load_dwordx4 a[28:31], v3, s[20:23], s51 offen offset:3072 sc1
v_fma_f32 v187, v187, v28, v28
ds_read_b128 v[164:167], v201 offset:19456
s_waitcnt lgkmcnt(4)
v_mfma_f32_32x32x16_bf16 v[32:47], v[172:175], a[60:63], v[32:47]
v_exp_f32 v191, v191
v_rcp_f32 v184, v184
ds_read_b128 v[168:171], v201 offset:20480
s_waitcnt lgkmcnt(4)
v_mfma_f32_32x32x16_bf16 v[32:47], v[176:179], a[64:67], v[32:47]
s_add_u32 s50, s48, 16384
buffer_load_dwordx4 a[32:35], v3, s[20:23], s50 offen offset:0 sc1
v_fma_f32 v188, v188, v28, v28
ds_read_b128 v[172:175], v201 offset:21504
s_waitcnt lgkmcnt(4)
v_mfma_f32_32x32x16_bf16 v[32:47], v[180:183], a[68:71], v[32:47]
v_rcp_f32 v185, v185
v_fma_f32 v189, v189, v28, v28
ds_read_b128 v[176:179], v201 offset:22528
s_waitcnt lgkmcnt(4)
v_mfma_f32_32x32x16_bf16 v[32:47], v[64:67], a[72:75], v[32:47]
)ASM"
R"ASM(s_add_u32 s51, s49, 16384
buffer_load_dwordx4 a[36:39], v3, s[20:23], s51 offen offset:0 sc1
ds_read_b128 v[180:183], v201 offset:23552
s_waitcnt lgkmcnt(4)
v_mfma_f32_32x32x16_bf16 v[32:47], v[164:167], a[76:79], v[32:47]
v_rcp_f32 v186, v186
v_fma_f32 v190, v190, v28, v28
ds_read_b128 v[64:67], v201 offset:24576
s_waitcnt lgkmcnt(4)
v_mfma_f32_32x32x16_bf16 v[32:47], v[168:171], a[80:83], v[32:47]
buffer_load_dwordx4 a[40:43], v3, s[20:23], s50 offen offset:1024 sc1
v_rcp_f32 v187, v187
ds_read_b128 v[164:167], v201 offset:25600
s_waitcnt lgkmcnt(4)
v_mfma_f32_32x32x16_bf16 v[32:47], v[172:175], a[84:87], v[32:47]
v_fma_f32 v191, v191, v28, v28
v_mul_f32 v184, v184, v56
v_rcp_f32 v188, v188
ds_read_b128 v[168:171], v201 offset:26624
s_waitcnt lgkmcnt(4)
v_mfma_f32_32x32x16_bf16 v[32:47], v[176:179], a[88:91], v[32:47]
buffer_load_dwordx4 a[44:47], v3, s[20:23], s51 offen offset:1024 sc1
v_mul_f32 v185, v185, v57
ds_read_b128 v[172:175], v201 offset:27648
s_waitcnt lgkmcnt(4)
v_mfma_f32_32x32x16_bf16 v[32:47], v[180:183], a[92:95], v[32:47]
v_rcp_f32 v189, v189
v_mul_f32 v186, v186, v58
ds_read_b128 v[176:179], v201 offset:28672
s_waitcnt lgkmcnt(4)
v_mfma_f32_32x32x16_bf16 v[32:47], v[64:67], a[96:99], v[32:47]
buffer_load_dwordx4 a[48:51], v3, s[20:23], s50 offen offset:2048 sc1
)ASM"
R"ASM(v_rcp_f32 v190, v190
ds_read_b128 v[180:183], v201 offset:29696
s_waitcnt lgkmcnt(4)
v_mfma_f32_32x32x16_bf16 v[32:47], v[164:167], a[100:103], v[32:47]
v_mul_f32 v187, v187, v59
v_rcp_f32 v191, v191
ds_read_b128 v[64:67], v201 offset:30720
s_waitcnt lgkmcnt(4)
v_mfma_f32_32x32x16_bf16 v[32:47], v[168:171], a[104:107], v[32:47]
buffer_load_dwordx4 a[52:55], v3, s[20:23], s51 offen offset:2048 sc1
v_mul_f32 v188, v188, v60
v_cvt_pk_bf16_f32 v192, v184, v185
ds_read_b128 v[164:167], v201 offset:31744
s_waitcnt lgkmcnt(4)
v_mfma_f32_32x32x16_bf16 v[32:47], v[172:175], a[108:111], v[32:47]
v_mul_f32 v189, v189, v61
v_cvt_pk_bf16_f32 v193, v186, v187
ds_read_b128 v[168:171], v202 offset:8192
s_waitcnt lgkmcnt(4)
v_mfma_f32_32x32x16_bf16 v[32:47], v[176:179], a[112:115], v[32:47]
buffer_load_dwordx4 a[56:59], v3, s[20:23], s50 offen offset:3072 sc1
v_mul_f32 v190, v190, v62
v_mul_f32 v191, v191, v63
s_waitcnt lgkmcnt(3)
v_mfma_f32_32x32x16_bf16 v[32:47], v[180:183], a[116:119], v[32:47]
v_cvt_pk_bf16_f32 v194, v188, v189
v_cvt_pk_bf16_f32 v195, v190, v191
s_waitcnt lgkmcnt(2)
v_mfma_f32_32x32x16_bf16 v[32:47], v[64:67], a[120:123], v[32:47]
buffer_load_dwordx4 a[60:63], v3, s[20:23], s51 offen offset:3072 sc1
s_waitcnt lgkmcnt(1)
v_mfma_f32_32x32x16_bf16 v[32:47], v[164:167], a[124:127], v[32:47]
)ASM"
R"ASM(s_add_u32 s84, s73, 81920
buffer_store_dwordx4 v[192:195], v3, s[20:23], s84 offen sc1
s_waitcnt lgkmcnt(0)
v_mfma_f32_32x32x16_bf16 v[32:47], v[168:171], v[196:199], v[32:47]
s_mov_b32 s67, 0
Lf2_52_%=:
s_waitcnt vmcnt(16)
v_accvgpr_read_b32 v26, a3
s_waitcnt vmcnt(15)
v_accvgpr_read_b32 v8, a7
v_max_u32 v26, v26, v8
v_cmp_eq_u32 vcc, -1, v26
s_cbranch_vccz Lcok_62_%=
s_call_b64 s[44:45], Lhl0_55_%=
Lcok_62_%=:
v_mfma_f32_32x32x16_bf16 v[10:25], a[128:131], a[0:3], 0
s_nop 2
v_mul_f32 v184, v29, v32
v_mul_f32 v185, v29, v33
v_mul_f32 v186, v29, v34
v_mul_f32 v187, v29, v35
v_mfma_f32_32x32x16_bf16 v[164:179], a[132:135], a[0:3], 0
s_add_u32 s50, s48, 32768
buffer_load_dwordx4 a[64:67], v3, s[20:23], s50 offen offset:0 sc1
s_waitcnt vmcnt(15)
v_accvgpr_read_b32 v26, a11
s_waitcnt vmcnt(14)
v_accvgpr_read_b32 v8, a15
v_exp_f32 v184, v184
v_mul_f32 v188, v29, v36
v_mfma_f32_32x32x16_bf16 v[48:63], a[128:131], a[4:7], 0
v_max_u32 v26, v26, v8
)ASM"
R"ASM(v_cmp_eq_u32 vcc, -1, v26
v_exp_f32 v185, v185
v_mul_f32 v189, v29, v37
v_mfma_f32_32x32x16_bf16 v[196:211], a[132:135], a[4:7], 0
s_add_u32 s51, s49, 32768
buffer_load_dwordx4 a[68:71], v3, s[20:23], s51 offen offset:0 sc1
s_cbranch_vccz Lcok_63_%=
s_call_b64 s[44:45], Lhl0_55_%=
Lcok_63_%=:
v_exp_f32 v186, v186
v_mul_f32 v190, v29, v38
v_mfma_f32_32x32x16_bf16 v[10:25], a[136:139], a[8:11], v[10:25]
v_exp_f32 v187, v187
v_mul_f32 v191, v29, v39
v_fma_f32 v184, v184, v28, v28
v_mfma_f32_32x32x16_bf16 v[164:179], a[140:143], a[8:11], v[164:179]
buffer_load_dwordx4 a[72:75], v3, s[20:23], s50 offen offset:1024 sc1
s_waitcnt vmcnt(15)
v_accvgpr_read_b32 v26, a19
s_waitcnt vmcnt(14)
v_accvgpr_read_b32 v8, a23
v_exp_f32 v188, v188
v_fma_f32 v185, v185, v28, v28
v_mfma_f32_32x32x16_bf16 v[48:63], a[136:139], a[12:15], v[48:63]
v_max_u32 v26, v26, v8
v_cmp_eq_u32 vcc, -1, v26
v_exp_f32 v189, v189
v_fma_f32 v186, v186, v28, v28
v_mfma_f32_32x32x16_bf16 v[196:211], a[140:143], a[12:15], v[196:211]
buffer_load_dwordx4 a[76:79], v3, s[20:23], s51 offen offset:1024 sc1
s_cbranch_vccz Lcok_64_%=
s_call_b64 s[44:45], Lhl0_55_%=
)ASM"
R"ASM(Lcok_64_%=:
v_exp_f32 v190, v190
v_fma_f32 v187, v187, v28, v28
v_mfma_f32_32x32x16_bf16 v[10:25], a[144:147], a[16:19], v[10:25]
v_exp_f32 v191, v191
v_rcp_f32 v184, v184
v_mfma_f32_32x32x16_bf16 v[164:179], a[148:151], a[16:19], v[164:179]
buffer_load_dwordx4 a[80:83], v3, s[20:23], s50 offen offset:2048 sc1
s_waitcnt vmcnt(15)
v_accvgpr_read_b32 v26, a27
s_waitcnt vmcnt(14)
v_accvgpr_read_b32 v8, a31
v_fma_f32 v188, v188, v28, v28
v_rcp_f32 v185, v185
v_fma_f32 v189, v189, v28, v28
v_mfma_f32_32x32x16_bf16 v[48:63], a[144:147], a[20:23], v[48:63]
v_max_u32 v26, v26, v8
v_cmp_eq_u32 vcc, -1, v26
v_rcp_f32 v186, v186
v_fma_f32 v190, v190, v28, v28
v_mfma_f32_32x32x16_bf16 v[196:211], a[148:151], a[20:23], v[196:211]
buffer_load_dwordx4 a[84:87], v3, s[20:23], s51 offen offset:2048 sc1
s_cbranch_vccz Lcok_65_%=
s_call_b64 s[44:45], Lhl0_55_%=
Lcok_65_%=:
v_rcp_f32 v187, v187
v_fma_f32 v191, v191, v28, v28
v_mul_f32 v184, v184, v40
v_mfma_f32_32x32x16_bf16 v[10:25], a[152:155], a[24:27], v[10:25]
v_rcp_f32 v188, v188
v_mul_f32 v185, v185, v41
v_mfma_f32_32x32x16_bf16 v[164:179], a[156:159], a[24:27], v[164:179]
)ASM"
R"ASM(buffer_load_dwordx4 a[88:91], v3, s[20:23], s50 offen offset:3072 sc1
s_waitcnt vmcnt(15)
v_accvgpr_read_b32 v26, a35
s_waitcnt vmcnt(14)
v_accvgpr_read_b32 v8, a39
v_rcp_f32 v189, v189
v_mul_f32 v186, v186, v42
v_mfma_f32_32x32x16_bf16 v[48:63], a[152:155], a[28:31], v[48:63]
v_max_u32 v26, v26, v8
v_cmp_eq_u32 vcc, -1, v26
v_rcp_f32 v190, v190
v_mul_f32 v187, v187, v43
v_mfma_f32_32x32x16_bf16 v[196:211], a[156:159], a[28:31], v[196:211]
buffer_load_dwordx4 a[92:95], v3, s[20:23], s51 offen offset:3072 sc1
s_cbranch_vccz Lcok_66_%=
s_call_b64 s[44:45], Lhl1_56_%=
Lcok_66_%=:
v_rcp_f32 v191, v191
v_mul_f32 v188, v188, v44
v_cvt_pk_bf16_f32 v192, v184, v185
v_mfma_f32_32x32x16_bf16 v[10:25], a[160:163], a[32:35], v[10:25]
v_mul_f32 v189, v189, v45
v_cvt_pk_bf16_f32 v193, v186, v187
v_mul_f32 v190, v190, v46
v_mul_f32 v191, v191, v47
v_mfma_f32_32x32x16_bf16 v[164:179], a[164:167], a[32:35], v[164:179]
s_add_u32 s50, s48, 49152
buffer_load_dwordx4 a[96:99], v3, s[20:23], s50 offen offset:0 sc1
s_waitcnt vmcnt(15)
v_accvgpr_read_b32 v26, a43
s_waitcnt vmcnt(14)
v_accvgpr_read_b32 v8, a47
)ASM"
R"ASM(v_cvt_pk_bf16_f32 v194, v188, v189
v_cvt_pk_bf16_f32 v195, v190, v191
v_mfma_f32_32x32x16_bf16 v[48:63], a[160:163], a[36:39], v[48:63]
v_max_u32 v26, v26, v8
v_cmp_eq_u32 vcc, -1, v26
s_add_u32 s84, s73, 98304
buffer_store_dwordx4 v[192:195], v3, s[20:23], s84 offen sc1
v_mfma_f32_32x32x16_bf16 v[196:211], a[164:167], a[36:39], v[196:211]
s_add_u32 s51, s49, 49152
buffer_load_dwordx4 a[100:103], v3, s[20:23], s51 offen offset:0 sc1
s_cbranch_vccz Lcok_67_%=
s_call_b64 s[44:45], Lhl1_56_%=
Lcok_67_%=:
v_mfma_f32_32x32x16_bf16 v[10:25], a[168:171], a[40:43], v[10:25]
v_mfma_f32_32x32x16_bf16 v[164:179], a[172:175], a[40:43], v[164:179]
buffer_load_dwordx4 a[104:107], v3, s[20:23], s50 offen offset:1024 sc1
s_waitcnt vmcnt(16)
v_accvgpr_read_b32 v26, a51
s_waitcnt vmcnt(15)
v_accvgpr_read_b32 v8, a55
v_mfma_f32_32x32x16_bf16 v[48:63], a[168:171], a[44:47], v[48:63]
v_max_u32 v26, v26, v8
v_cmp_eq_u32 vcc, -1, v26
v_mfma_f32_32x32x16_bf16 v[196:211], a[172:175], a[44:47], v[196:211]
buffer_load_dwordx4 a[108:111], v3, s[20:23], s51 offen offset:1024 sc1
s_cbranch_vccz Lcok_68_%=
s_call_b64 s[44:45], Lhl1_56_%=
Lcok_68_%=:
v_mfma_f32_32x32x16_bf16 v[10:25], a[176:179], a[48:51], v[10:25]
v_mfma_f32_32x32x16_bf16 v[164:179], a[180:183], a[48:51], v[164:179]
buffer_load_dwordx4 a[112:115], v3, s[20:23], s50 offen offset:2048 sc1
s_waitcnt vmcnt(16)
)ASM"
R"ASM(v_accvgpr_read_b32 v26, a59
s_waitcnt vmcnt(15)
v_accvgpr_read_b32 v8, a63
v_mfma_f32_32x32x16_bf16 v[48:63], a[176:179], a[52:55], v[48:63]
v_max_u32 v26, v26, v8
v_cmp_eq_u32 vcc, -1, v26
v_mfma_f32_32x32x16_bf16 v[196:211], a[180:183], a[52:55], v[196:211]
buffer_load_dwordx4 a[116:119], v3, s[20:23], s51 offen offset:2048 sc1
s_cbranch_vccz Lcok_69_%=
s_call_b64 s[44:45], Lhl1_56_%=
Lcok_69_%=:
v_mfma_f32_32x32x16_bf16 v[10:25], a[184:187], a[56:59], v[10:25]
v_mfma_f32_32x32x16_bf16 v[164:179], a[188:191], a[56:59], v[164:179]
buffer_load_dwordx4 a[120:123], v3, s[20:23], s50 offen offset:3072 sc1
s_waitcnt vmcnt(15)
v_accvgpr_read_b32 v26, a67
s_waitcnt vmcnt(14)
v_accvgpr_read_b32 v8, a71
v_mfma_f32_32x32x16_bf16 v[48:63], a[184:187], a[60:63], v[48:63]
v_max_u32 v26, v26, v8
v_cmp_eq_u32 vcc, -1, v26
v_mfma_f32_32x32x16_bf16 v[196:211], a[188:191], a[60:63], v[196:211]
buffer_load_dwordx4 a[124:127], v3, s[20:23], s51 offen offset:3072 sc1
s_cbranch_vccz Lcok_70_%=
s_call_b64 s[44:45], Lhl2_57_%=
Lcok_70_%=:
v_mfma_f32_32x32x16_bf16 v[10:25], a[192:195], a[64:67], v[10:25]
v_mfma_f32_32x32x16_bf16 v[164:179], a[196:199], a[64:67], v[164:179]
s_add_u32 s50, s48, 65536
buffer_load_dwordx4 a[0:3], v3, s[20:23], s50 offen offset:0 sc1
s_waitcnt vmcnt(15)
v_accvgpr_read_b32 v26, a75
)ASM"
R"ASM(s_waitcnt vmcnt(14)
v_accvgpr_read_b32 v8, a79
v_mfma_f32_32x32x16_bf16 v[48:63], a[192:195], a[68:71], v[48:63]
v_max_u32 v26, v26, v8
v_cmp_eq_u32 vcc, -1, v26
v_mfma_f32_32x32x16_bf16 v[196:211], a[196:199], a[68:71], v[196:211]
s_add_u32 s51, s49, 65536
buffer_load_dwordx4 a[4:7], v3, s[20:23], s51 offen offset:0 sc1
s_cbranch_vccz Lcok_71_%=
s_call_b64 s[44:45], Lhl2_57_%=
Lcok_71_%=:
v_mfma_f32_32x32x16_bf16 v[10:25], a[200:203], a[72:75], v[10:25]
v_mfma_f32_32x32x16_bf16 v[164:179], a[204:207], a[72:75], v[164:179]
buffer_load_dwordx4 a[8:11], v3, s[20:23], s50 offen offset:1024 sc1
s_waitcnt vmcnt(15)
v_accvgpr_read_b32 v26, a83
s_waitcnt vmcnt(14)
v_accvgpr_read_b32 v8, a87
v_mfma_f32_32x32x16_bf16 v[48:63], a[200:203], a[76:79], v[48:63]
v_max_u32 v26, v26, v8
v_cmp_eq_u32 vcc, -1, v26
v_mfma_f32_32x32x16_bf16 v[196:211], a[204:207], a[76:79], v[196:211]
buffer_load_dwordx4 a[12:15], v3, s[20:23], s51 offen offset:1024 sc1
s_cbranch_vccz Lcok_72_%=
s_call_b64 s[44:45], Lhl2_57_%=
Lcok_72_%=:
v_mfma_f32_32x32x16_bf16 v[10:25], a[208:211], a[80:83], v[10:25]
v_mfma_f32_32x32x16_bf16 v[164:179], a[212:215], a[80:83], v[164:179]
buffer_load_dwordx4 a[16:19], v3, s[20:23], s50 offen offset:2048 sc1
s_waitcnt vmcnt(15)
v_accvgpr_read_b32 v26, a91
s_waitcnt vmcnt(14)
)ASM"
R"ASM(v_accvgpr_read_b32 v8, a95
v_mfma_f32_32x32x16_bf16 v[48:63], a[208:211], a[84:87], v[48:63]
v_max_u32 v26, v26, v8
v_cmp_eq_u32 vcc, -1, v26
v_mfma_f32_32x32x16_bf16 v[196:211], a[212:215], a[84:87], v[196:211]
buffer_load_dwordx4 a[20:23], v3, s[20:23], s51 offen offset:2048 sc1
s_cbranch_vccz Lcok_73_%=
s_call_b64 s[44:45], Lhl2_57_%=
Lcok_73_%=:
v_mfma_f32_32x32x16_bf16 v[10:25], a[216:219], a[88:91], v[10:25]
v_mfma_f32_32x32x16_bf16 v[164:179], a[220:223], a[88:91], v[164:179]
buffer_load_dwordx4 a[24:27], v3, s[20:23], s50 offen offset:3072 sc1
s_waitcnt vmcnt(15)
v_accvgpr_read_b32 v26, a99
s_waitcnt vmcnt(13)
v_accvgpr_read_b32 v8, a103
v_mfma_f32_32x32x16_bf16 v[48:63], a[216:219], a[92:95], v[48:63]
v_max_u32 v26, v26, v8
v_cmp_eq_u32 vcc, -1, v26
v_mfma_f32_32x32x16_bf16 v[196:211], a[220:223], a[92:95], v[196:211]
buffer_load_dwordx4 a[28:31], v3, s[20:23], s51 offen offset:3072 sc1
s_cbranch_vccz Lcok_74_%=
s_call_b64 s[44:45], Lhl3_58_%=
Lcok_74_%=:
v_mfma_f32_32x32x16_bf16 v[10:25], a[224:227], a[96:99], v[10:25]
v_mfma_f32_32x32x16_bf16 v[164:179], a[228:231], a[96:99], v[164:179]
s_add_u32 s50, s48, 81920
buffer_load_dwordx4 a[32:35], v3, s[20:23], s50 offen offset:0 sc1
s_waitcnt vmcnt(14)
v_accvgpr_read_b32 v26, a107
s_waitcnt vmcnt(13)
v_accvgpr_read_b32 v8, a111
)ASM"
R"ASM(v_mfma_f32_32x32x16_bf16 v[48:63], a[224:227], a[100:103], v[48:63]
v_max_u32 v26, v26, v8
v_cmp_eq_u32 vcc, -1, v26
v_mfma_f32_32x32x16_bf16 v[196:211], a[228:231], a[100:103], v[196:211]
s_add_u32 s51, s49, 81920
buffer_load_dwordx4 a[36:39], v3, s[20:23], s51 offen offset:0 sc1
s_cbranch_vccz Lcok_75_%=
s_call_b64 s[44:45], Lhl3_58_%=
Lcok_75_%=:
v_mfma_f32_32x32x16_bf16 v[10:25], a[232:235], a[104:107], v[10:25]
v_mfma_f32_32x32x16_bf16 v[164:179], a[236:239], a[104:107], v[164:179]
buffer_load_dwordx4 a[40:43], v3, s[20:23], s50 offen offset:1024 sc1
s_waitcnt vmcnt(14)
v_accvgpr_read_b32 v26, a115
s_waitcnt vmcnt(13)
v_accvgpr_read_b32 v8, a119
v_mfma_f32_32x32x16_bf16 v[48:63], a[232:235], a[108:111], v[48:63]
v_max_u32 v26, v26, v8
v_cmp_eq_u32 vcc, -1, v26
v_mfma_f32_32x32x16_bf16 v[196:211], a[236:239], a[108:111], v[196:211]
buffer_load_dwordx4 a[44:47], v3, s[20:23], s51 offen offset:1024 sc1
s_cbranch_vccz Lcok_76_%=
s_call_b64 s[44:45], Lhl3_58_%=
Lcok_76_%=:
v_mfma_f32_32x32x16_bf16 v[10:25], a[240:243], a[112:115], v[10:25]
v_mfma_f32_32x32x16_bf16 v[164:179], a[244:247], a[112:115], v[164:179]
buffer_load_dwordx4 a[48:51], v3, s[20:23], s50 offen offset:2048 sc1
s_waitcnt vmcnt(14)
v_accvgpr_read_b32 v26, a123
s_waitcnt vmcnt(13)
v_accvgpr_read_b32 v8, a127
v_mfma_f32_32x32x16_bf16 v[48:63], a[240:243], a[116:119], v[48:63]
)ASM"
R"ASM(v_max_u32 v26, v26, v8
v_cmp_eq_u32 vcc, -1, v26
v_mfma_f32_32x32x16_bf16 v[196:211], a[244:247], a[116:119], v[196:211]
buffer_load_dwordx4 a[52:55], v3, s[20:23], s51 offen offset:2048 sc1
s_cbranch_vccz Lcok_77_%=
s_call_b64 s[44:45], Lhl3_58_%=
Lcok_77_%=:
v_mfma_f32_32x32x16_bf16 v[10:25], a[248:251], a[120:123], v[10:25]
v_mfma_f32_32x32x16_bf16 v[164:179], a[252:255], a[120:123], v[164:179]
buffer_load_dwordx4 a[56:59], v3, s[20:23], s50 offen offset:3072 sc1
s_waitcnt vmcnt(14)
v_accvgpr_read_b32 v26, a3
s_waitcnt vmcnt(13)
v_accvgpr_read_b32 v8, a7
v_mfma_f32_32x32x16_bf16 v[48:63], a[248:251], a[124:127], v[48:63]
v_max_u32 v26, v26, v8
v_cmp_eq_u32 vcc, -1, v26
v_mfma_f32_32x32x16_bf16 v[196:211], a[252:255], a[124:127], v[196:211]
buffer_load_dwordx4 a[60:63], v3, s[20:23], s51 offen offset:3072 sc1
s_cbranch_vccz Lcok_78_%=
s_call_b64 s[44:45], Lhl4_59_%=
Lcok_78_%=:
v_mfma_f32_32x32x16_bf16 v[10:25], v[68:71], a[0:3], v[10:25]
s_xor_b32 s80, s13, 1
v_mfma_f32_32x32x16_bf16 v[164:179], v[72:75], a[0:3], v[164:179]
s_waitcnt vmcnt(13)
v_accvgpr_read_b32 v26, a11
s_waitcnt vmcnt(12)
v_accvgpr_read_b32 v8, a15
s_lshl_b32 s80, s80, 12
v_mfma_f32_32x32x16_bf16 v[48:63], v[68:71], a[4:7], v[48:63]
v_max_u32 v26, v26, v8
)ASM"
R"ASM(v_cmp_eq_u32 vcc, -1, v26
s_add_u32 s80, s80, 98304
v_mfma_f32_32x32x16_bf16 v[196:211], v[72:75], a[4:7], v[196:211]
s_cbranch_vccz Lcok_79_%=
s_call_b64 s[44:45], Lhl4_59_%=
Lcok_79_%=:
v_add_u32 v65, s80, v3
v_mfma_f32_32x32x16_bf16 v[10:25], v[76:79], a[8:11], v[10:25]
s_xor_b32 s80, s13, 2
v_mfma_f32_32x32x16_bf16 v[164:179], v[80:83], a[8:11], v[164:179]
s_waitcnt vmcnt(11)
v_accvgpr_read_b32 v26, a19
s_waitcnt vmcnt(10)
v_accvgpr_read_b32 v8, a23
s_lshl_b32 s80, s80, 12
v_mfma_f32_32x32x16_bf16 v[48:63], v[76:79], a[12:15], v[48:63]
v_max_u32 v26, v26, v8
v_cmp_eq_u32 vcc, -1, v26
s_add_u32 s80, s80, 114688
v_mfma_f32_32x32x16_bf16 v[196:211], v[80:83], a[12:15], v[196:211]
s_cbranch_vccz Lcok_80_%=
s_call_b64 s[44:45], Lhl4_59_%=
Lcok_80_%=:
v_add_u32 v66, s80, v3
v_mfma_f32_32x32x16_bf16 v[10:25], v[84:87], a[16:19], v[10:25]
s_xor_b32 s80, s13, 3
v_mfma_f32_32x32x16_bf16 v[164:179], v[88:91], a[16:19], v[164:179]
s_add_u32 s50, s48, 98304
buffer_load_dwordx4 a[64:67], v3, s[20:23], s50 offen offset:0 sc1
s_waitcnt vmcnt(10)
v_accvgpr_read_b32 v26, a27
s_waitcnt vmcnt(9)
)ASM"
R"ASM(v_accvgpr_read_b32 v8, a31
s_lshl_b32 s80, s80, 12
v_mfma_f32_32x32x16_bf16 v[48:63], v[84:87], a[20:23], v[48:63]
v_max_u32 v26, v26, v8
v_cmp_eq_u32 vcc, -1, v26
s_add_u32 s80, s80, 145408
v_mfma_f32_32x32x16_bf16 v[196:211], v[88:91], a[20:23], v[196:211]
s_add_u32 s51, s49, 98304
buffer_load_dwordx4 a[68:71], v3, s[20:23], s51 offen offset:0 sc1
s_cbranch_vccz Lcok_81_%=
s_call_b64 s[44:45], Lhl4_59_%=
Lcok_81_%=:
v_add_u32 v67, s80, v3
v_mfma_f32_32x32x16_bf16 v[10:25], v[92:95], a[24:27], v[10:25]
s_lshl_b32 s80, s13, 12
v_mfma_f32_32x32x16_bf16 v[164:179], v[96:99], a[24:27], v[164:179]
buffer_load_dwordx4 a[72:75], v3, s[20:23], s50 offen offset:1024 sc1
s_waitcnt vmcnt(10)
v_accvgpr_read_b32 v26, a35
s_waitcnt vmcnt(9)
v_accvgpr_read_b32 v8, a39
s_add_u32 s80, s80, 98304
v_mfma_f32_32x32x16_bf16 v[48:63], v[92:95], a[28:31], v[48:63]
v_max_u32 v26, v26, v8
v_cmp_eq_u32 vcc, -1, v26
v_add_u32 v64, s80, v3
v_mfma_f32_32x32x16_bf16 v[196:211], v[96:99], a[28:31], v[196:211]
buffer_load_dwordx4 a[76:79], v3, s[20:23], s51 offen offset:1024 sc1
s_cbranch_vccz Lcok_82_%=
s_call_b64 s[44:45], Lhl5_60_%=
Lcok_82_%=:
v_mfma_f32_32x32x16_bf16 v[10:25], v[100:103], a[32:35], v[10:25]
)ASM"
R"ASM(v_mfma_f32_32x32x16_bf16 v[164:179], v[104:107], a[32:35], v[164:179]
buffer_load_dwordx4 a[80:83], v3, s[20:23], s50 offen offset:2048 sc1
s_waitcnt vmcnt(10)
v_accvgpr_read_b32 v26, a43
s_waitcnt vmcnt(9)
v_accvgpr_read_b32 v8, a47
v_mfma_f32_32x32x16_bf16 v[48:63], v[100:103], a[36:39], v[48:63]
v_max_u32 v26, v26, v8
v_cmp_eq_u32 vcc, -1, v26
v_mfma_f32_32x32x16_bf16 v[196:211], v[104:107], a[36:39], v[196:211]
buffer_load_dwordx4 a[84:87], v3, s[20:23], s51 offen offset:2048 sc1
s_cbranch_vccz Lcok_83_%=
s_call_b64 s[44:45], Lhl5_60_%=
Lcok_83_%=:
v_mfma_f32_32x32x16_bf16 v[10:25], v[108:111], a[40:43], v[10:25]
v_mfma_f32_32x32x16_bf16 v[164:179], v[112:115], a[40:43], v[164:179]
buffer_load_dwordx4 a[88:91], v3, s[20:23], s50 offen offset:3072 sc1
s_waitcnt vmcnt(10)
v_accvgpr_read_b32 v26, a51
s_waitcnt vmcnt(9)
v_accvgpr_read_b32 v8, a55
v_mfma_f32_32x32x16_bf16 v[48:63], v[108:111], a[44:47], v[48:63]
v_max_u32 v26, v26, v8
v_cmp_eq_u32 vcc, -1, v26
v_mfma_f32_32x32x16_bf16 v[196:211], v[112:115], a[44:47], v[196:211]
buffer_load_dwordx4 a[92:95], v3, s[20:23], s51 offen offset:3072 sc1
s_cbranch_vccz Lcok_84_%=
s_call_b64 s[44:45], Lhl5_60_%=
Lcok_84_%=:
v_mfma_f32_32x32x16_bf16 v[10:25], v[116:119], a[48:51], v[10:25]
v_mfma_f32_32x32x16_bf16 v[164:179], v[120:123], a[48:51], v[164:179]
s_waitcnt vmcnt(9)
)ASM"
R"ASM(v_accvgpr_read_b32 v26, a59
s_waitcnt vmcnt(8)
v_accvgpr_read_b32 v8, a63
v_mfma_f32_32x32x16_bf16 v[48:63], v[116:119], a[52:55], v[48:63]
v_max_u32 v26, v26, v8
v_cmp_eq_u32 vcc, -1, v26
v_mfma_f32_32x32x16_bf16 v[196:211], v[120:123], a[52:55], v[196:211]
s_cbranch_vccz Lcok_85_%=
s_call_b64 s[44:45], Lhl5_60_%=
Lcok_85_%=:
v_mfma_f32_32x32x16_bf16 v[10:25], v[124:127], a[56:59], v[10:25]
v_mfma_f32_32x32x16_bf16 v[164:179], v[128:131], a[56:59], v[164:179]
s_waitcnt vmcnt(7)
v_accvgpr_read_b32 v26, a67
s_waitcnt vmcnt(6)
v_accvgpr_read_b32 v8, a71
v_mfma_f32_32x32x16_bf16 v[48:63], v[124:127], a[60:63], v[48:63]
v_max_u32 v26, v26, v8
v_cmp_eq_u32 vcc, -1, v26
v_mfma_f32_32x32x16_bf16 v[196:211], v[128:131], a[60:63], v[196:211]
s_cbranch_vccz Lcok_86_%=
s_call_b64 s[44:45], Lhl6_61_%=
Lcok_86_%=:
s_waitcnt vmcnt(5)
v_accvgpr_read_b32 v26, a75
s_waitcnt vmcnt(4)
v_accvgpr_read_b32 v8, a79
v_max_u32 v26, v26, v8
v_cmp_eq_u32 vcc, -1, v26
s_cbranch_vccz Lcok_87_%=
s_call_b64 s[44:45], Lhl6_61_%=
Lcok_87_%=:
)ASM"
R"ASM(s_waitcnt vmcnt(3)
v_accvgpr_read_b32 v26, a83
s_waitcnt vmcnt(2)
v_accvgpr_read_b32 v8, a87
v_max_u32 v26, v26, v8
v_cmp_eq_u32 vcc, -1, v26
s_cbranch_vccz Lcok_88_%=
s_call_b64 s[44:45], Lhl6_61_%=
Lcok_88_%=:
s_waitcnt vmcnt(1)
v_accvgpr_read_b32 v26, a91
s_waitcnt vmcnt(0)
v_accvgpr_read_b32 v8, a95
v_max_u32 v26, v26, v8
v_cmp_eq_u32 vcc, -1, v26
s_cbranch_vccz Lcok_89_%=
s_call_b64 s[44:45], Lhl6_61_%=
Lcok_89_%=:
v_mfma_f32_32x32x16_bf16 v[196:211], v[136:139], a[68:71], v[196:211]
v_mfma_f32_32x32x16_bf16 v[196:211], v[144:147], a[76:79], v[196:211]
v_mfma_f32_32x32x16_bf16 v[196:211], v[152:155], a[84:87], v[196:211]
v_mfma_f32_32x32x16_bf16 v[196:211], v[160:163], a[92:95], v[196:211]
v_mfma_f32_32x32x16_bf16 v[164:179], v[136:139], a[64:67], v[164:179]
v_mfma_f32_32x32x16_bf16 v[164:179], v[144:147], a[72:75], v[164:179]
v_mfma_f32_32x32x16_bf16 v[164:179], v[152:155], a[80:83], v[164:179]
s_nop 8
ds_write_b128 v67, v[196:199] offset:0
ds_write_b128 v67, v[200:203] offset:1024
v_mfma_f32_32x32x16_bf16 v[164:179], v[160:163], a[88:91], v[164:179]
ds_write_b128 v67, v[204:207] offset:2048
ds_write_b128 v67, v[208:211] offset:3072
v_mfma_f32_32x32x16_bf16 v[48:63], v[132:135], a[68:71], v[48:63]
)ASM"
R"ASM(v_mfma_f32_32x32x16_bf16 v[48:63], v[140:143], a[76:79], v[48:63]
v_mfma_f32_32x32x16_bf16 v[48:63], v[148:151], a[84:87], v[48:63]
s_nop 6
v_cmp_u_f32 vcc, v164, v196
s_cbranch_vccnz Lredoh_53_%=
Lredohx_54_%=:
ds_read_b128 v[32:35], v9 offset:10240
ds_read_b128 v[36:39], v9 offset:10272
ds_read_b128 v[40:43], v9 offset:10304
ds_read_b128 v[44:47], v9 offset:10336
ds_write_b128 v66, v[164:167] offset:0
ds_write_b128 v66, v[168:171] offset:1024
v_mfma_f32_32x32x16_bf16 v[48:63], v[156:159], a[92:95], v[48:63]
ds_write_b128 v66, v[172:175] offset:2048
ds_write_b128 v66, v[176:179] offset:3072
v_mfma_f32_32x32x16_bf16 v[10:25], v[132:135], a[64:67], v[10:25]
v_mfma_f32_32x32x16_bf16 v[10:25], v[140:143], a[72:75], v[10:25]
v_mfma_f32_32x32x16_bf16 v[10:25], v[148:151], a[80:83], v[10:25]
s_nop 6
ds_write_b128 v65, v[48:51] offset:0
ds_write_b128 v65, v[52:55] offset:1024
v_mfma_f32_32x32x16_bf16 v[10:25], v[156:159], a[88:91], v[10:25]
ds_write_b128 v65, v[56:59] offset:2048
ds_write_b128 v65, v[60:63] offset:3072
s_waitcnt lgkmcnt(0)
s_barrier
ds_read_b128 v[48:51], v64 offset:0
ds_read_b128 v[52:55], v64 offset:1024
ds_read_b128 v[56:59], v64 offset:2048
ds_read_b128 v[60:63], v64 offset:3072
ds_read_b128 v[164:167], v64 offset:16384
ds_read_b128 v[168:171], v64 offset:17408
)ASM"
R"ASM(ds_read_b128 v[172:175], v64 offset:18432
ds_read_b128 v[176:179], v64 offset:19456
ds_read_b128 v[196:199], v64 offset:47104
ds_read_b128 v[200:203], v64 offset:48128
ds_read_b128 v[204:207], v64 offset:49152
ds_read_b128 v[208:211], v64 offset:50176
s_waitcnt lgkmcnt(11)
v_pk_add_f32 v[10:11], v[10:11], v[48:49]
v_pk_add_f32 v[12:13], v[12:13], v[50:51]
s_waitcnt lgkmcnt(10)
v_pk_add_f32 v[14:15], v[14:15], v[52:53]
v_pk_add_f32 v[16:17], v[16:17], v[54:55]
s_waitcnt lgkmcnt(9)
v_pk_add_f32 v[18:19], v[18:19], v[56:57]
v_pk_add_f32 v[20:21], v[20:21], v[58:59]
s_waitcnt lgkmcnt(8)
v_pk_add_f32 v[22:23], v[22:23], v[60:61]
v_pk_add_f32 v[24:25], v[24:25], v[62:63]
s_waitcnt lgkmcnt(7)
v_pk_add_f32 v[10:11], v[10:11], v[164:165]
v_pk_add_f32 v[12:13], v[12:13], v[166:167]
s_waitcnt lgkmcnt(6)
v_pk_add_f32 v[14:15], v[14:15], v[168:169]
v_pk_add_f32 v[16:17], v[16:17], v[170:171]
s_waitcnt lgkmcnt(5)
v_pk_add_f32 v[18:19], v[18:19], v[172:173]
v_pk_add_f32 v[20:21], v[20:21], v[174:175]
s_waitcnt lgkmcnt(4)
v_pk_add_f32 v[22:23], v[22:23], v[176:177]
v_pk_add_f32 v[24:25], v[24:25], v[178:179]
s_waitcnt lgkmcnt(3)
v_pk_add_f32 v[10:11], v[10:11], v[196:197]
)ASM"
R"ASM(v_pk_add_f32 v[12:13], v[12:13], v[198:199]
s_waitcnt lgkmcnt(2)
v_pk_add_f32 v[14:15], v[14:15], v[200:201]
v_pk_add_f32 v[16:17], v[16:17], v[202:203]
s_waitcnt lgkmcnt(1)
v_pk_add_f32 v[18:19], v[18:19], v[204:205]
v_pk_add_f32 v[20:21], v[20:21], v[206:207]
s_waitcnt lgkmcnt(0)
v_pk_add_f32 v[22:23], v[22:23], v[208:209]
v_pk_add_f32 v[24:25], v[24:25], v[210:211]
v_pk_fma_f32 v[236:237], v[32:33], v[10:11], v[236:237]
v_pk_fma_f32 v[238:239], v[34:35], v[12:13], v[238:239]
v_pk_fma_f32 v[240:241], v[36:37], v[14:15], v[240:241]
v_pk_fma_f32 v[242:243], v[38:39], v[16:17], v[242:243]
v_pk_fma_f32 v[244:245], v[40:41], v[18:19], v[244:245]
v_pk_fma_f32 v[246:247], v[42:43], v[20:21], v[246:247]
v_pk_fma_f32 v[248:249], v[44:45], v[22:23], v[248:249]
v_pk_fma_f32 v[250:251], v[46:47], v[24:25], v[250:251]
v_cvt_pk_bf16_f32 v48, v236, v237
v_cvt_pk_bf16_f32 v49, v238, v239
v_cvt_pk_bf16_f32 v50, v240, v241
v_cvt_pk_bf16_f32 v51, v242, v243
v_cvt_pk_bf16_f32 v52, v244, v245
v_cvt_pk_bf16_f32 v53, v246, v247
v_cvt_pk_bf16_f32 v54, v248, v249
v_cvt_pk_bf16_f32 v55, v250, v251
v_permlane32_swap_b32 v48, v50
v_permlane32_swap_b32 v49, v51
v_permlane32_swap_b32 v52, v54
v_permlane32_swap_b32 v53, v55
buffer_store_dwordx4 v[48:51], v30, s[28:31], s60 offen offset:0
buffer_store_dwordx4 v[52:55], v30, s[28:31], s60 offen offset:32
)ASM"
R"ASM(s_branch LEND_%=
Lslowo0_35_%=:
s_mov_b32 s52, 0
Lsol_90_%=:
s_add_u32 s80, s54, 0
buffer_load_dwordx4 v[172:175], v3, s[20:23], s80 offen offset:0 sc1
buffer_load_dwordx4 v[176:179], v3, s[20:23], s80 offen offset:1024 sc1
buffer_load_dwordx4 v[180:183], v3, s[20:23], s80 offen offset:2048 sc1
buffer_load_dwordx4 v[184:187], v3, s[20:23], s80 offen offset:3072 sc1
s_waitcnt vmcnt(0)
v_max3_u32 v8, v175, v179, v183
v_max_u32 v8, v8, v187
v_cmp_eq_u32 vcc, -1, v8
s_cbranch_vccz Lsox_91_%=
s_add_u32 s52, s52, 1
s_cmp_lt_u32 s52, 0x4000
s_cbranch_scc1 Lsol_90_%=
Lsox_91_%=:
s_nop 7
s_setpc_b64 s[44:45]
Lslowo1_36_%=:
s_mov_b32 s52, 0
Lsol_92_%=:
s_add_u32 s80, s54, 4096
buffer_load_dwordx4 v[188:191], v3, s[20:23], s80 offen offset:0 sc1
buffer_load_dwordx4 v[192:195], v3, s[20:23], s80 offen offset:1024 sc1
buffer_load_dwordx4 v[196:199], v3, s[20:23], s80 offen offset:2048 sc1
buffer_load_dwordx4 v[200:203], v3, s[20:23], s80 offen offset:3072 sc1
s_waitcnt vmcnt(0)
v_max3_u32 v8, v191, v195, v199
v_max_u32 v8, v8, v203
v_cmp_eq_u32 vcc, -1, v8
)ASM"
R"ASM(s_cbranch_vccz Lsox_93_%=
s_add_u32 s52, s52, 1
s_cmp_lt_u32 s52, 0x4000
s_cbranch_scc1 Lsol_92_%=
Lsox_93_%=:
s_nop 7
s_setpc_b64 s[44:45]
Lslowo2_37_%=:
s_mov_b32 s52, 0
Lsol_94_%=:
s_add_u32 s80, s54, 8192
buffer_load_dwordx4 v[204:207], v3, s[20:23], s80 offen offset:0 sc1
buffer_load_dwordx4 v[208:211], v3, s[20:23], s80 offen offset:1024 sc1
buffer_load_dwordx4 v[212:215], v3, s[20:23], s80 offen offset:2048 sc1
buffer_load_dwordx4 v[216:219], v3, s[20:23], s80 offen offset:3072 sc1
s_waitcnt vmcnt(0)
v_max3_u32 v8, v207, v211, v215
v_max_u32 v8, v8, v219
v_cmp_eq_u32 vcc, -1, v8
s_cbranch_vccz Lsox_95_%=
s_add_u32 s52, s52, 1
s_cmp_lt_u32 s52, 0x4000
s_cbranch_scc1 Lsol_94_%=
Lsox_95_%=:
s_nop 7
s_setpc_b64 s[44:45]
Lslowo3_38_%=:
s_mov_b32 s52, 0
Lsol_96_%=:
s_add_u32 s80, s54, 12288
buffer_load_dwordx4 v[220:223], v3, s[20:23], s80 offen offset:0 sc1
buffer_load_dwordx4 v[224:227], v3, s[20:23], s80 offen offset:1024 sc1
)ASM"
R"ASM(buffer_load_dwordx4 v[228:231], v3, s[20:23], s80 offen offset:2048 sc1
buffer_load_dwordx4 v[232:235], v3, s[20:23], s80 offen offset:3072 sc1
s_waitcnt vmcnt(0)
v_max3_u32 v8, v223, v227, v231
v_max_u32 v8, v8, v235
v_cmp_eq_u32 vcc, -1, v8
s_cbranch_vccz Lsox_97_%=
s_add_u32 s52, s52, 1
s_cmp_lt_u32 s52, 0x4000
s_cbranch_scc1 Lsol_96_%=
Lsox_97_%=:
s_nop 7
s_setpc_b64 s[44:45]
Lredoo_39_%=:
s_add_u32 s67, s67, 1
s_cmp_gt_u32 s67, 0x100
s_cbranch_scc1 Lredox_45_%=
s_waitcnt lgkmcnt(0)
s_add_u32 s80, s54, 0
buffer_load_dwordx4 v[172:175], v3, s[20:23], s80 offen offset:0 sc1
buffer_load_dwordx4 v[176:179], v3, s[20:23], s80 offen offset:1024 sc1
buffer_load_dwordx4 v[180:183], v3, s[20:23], s80 offen offset:2048 sc1
buffer_load_dwordx4 v[184:187], v3, s[20:23], s80 offen offset:3072 sc1
s_add_u32 s80, s54, 4096
buffer_load_dwordx4 v[188:191], v3, s[20:23], s80 offen offset:0 sc1
buffer_load_dwordx4 v[192:195], v3, s[20:23], s80 offen offset:1024 sc1
buffer_load_dwordx4 v[196:199], v3, s[20:23], s80 offen offset:2048 sc1
buffer_load_dwordx4 v[200:203], v3, s[20:23], s80 offen offset:3072 sc1
s_add_u32 s80, s54, 8192
buffer_load_dwordx4 v[204:207], v3, s[20:23], s80 offen offset:0 sc1
buffer_load_dwordx4 v[208:211], v3, s[20:23], s80 offen offset:1024 sc1
buffer_load_dwordx4 v[212:215], v3, s[20:23], s80 offen offset:2048 sc1
)ASM"
R"ASM(buffer_load_dwordx4 v[216:219], v3, s[20:23], s80 offen offset:3072 sc1
s_add_u32 s80, s54, 12288
buffer_load_dwordx4 v[220:223], v3, s[20:23], s80 offen offset:0 sc1
buffer_load_dwordx4 v[224:227], v3, s[20:23], s80 offen offset:1024 sc1
buffer_load_dwordx4 v[228:231], v3, s[20:23], s80 offen offset:2048 sc1
buffer_load_dwordx4 v[232:235], v3, s[20:23], s80 offen offset:3072 sc1
s_waitcnt vmcnt(0) lgkmcnt(0)
s_branch Lchko_40_%=
Lslowy_47_%=:
s_mov_b32 s52, 0
s_mov_b32 s53, 0
v_max_u32 v26, v10, v11
v_cmp_eq_u32 vcc, -1, v26
s_cbranch_vccnz Lsyk_99_%=
s_add_u32 s80, s55, 0
buffer_load_dwordx4 a[0:3], v3, s[20:23], s80 offen offset:0 sc1
buffer_load_dwordx4 a[4:7], v3, s[20:23], s80 offen offset:1024 sc1
buffer_load_dwordx4 a[8:11], v3, s[20:23], s80 offen offset:2048 sc1
buffer_load_dwordx4 a[12:15], v3, s[20:23], s80 offen offset:3072 sc1
s_branch Lsyn_100_%=
Lsyk_99_%=:
s_bitset1_b32 s53, 0
Lsyn_100_%=:
v_max_u32 v26, v12, v13
v_cmp_eq_u32 vcc, -1, v26
s_cbranch_vccnz Lsyk_101_%=
s_add_u32 s80, s55, 4096
buffer_load_dwordx4 a[16:19], v3, s[20:23], s80 offen offset:0 sc1
buffer_load_dwordx4 a[20:23], v3, s[20:23], s80 offen offset:1024 sc1
buffer_load_dwordx4 a[24:27], v3, s[20:23], s80 offen offset:2048 sc1
buffer_load_dwordx4 a[28:31], v3, s[20:23], s80 offen offset:3072 sc1
s_branch Lsyn_102_%=
)ASM"
R"ASM(Lsyk_101_%=:
s_bitset1_b32 s53, 1
Lsyn_102_%=:
v_max_u32 v26, v14, v15
v_cmp_eq_u32 vcc, -1, v26
s_cbranch_vccnz Lsyk_103_%=
s_add_u32 s80, s55, 8192
buffer_load_dwordx4 a[32:35], v3, s[20:23], s80 offen offset:0 sc1
buffer_load_dwordx4 a[36:39], v3, s[20:23], s80 offen offset:1024 sc1
buffer_load_dwordx4 a[40:43], v3, s[20:23], s80 offen offset:2048 sc1
buffer_load_dwordx4 a[44:47], v3, s[20:23], s80 offen offset:3072 sc1
s_branch Lsyn_104_%=
Lsyk_103_%=:
s_bitset1_b32 s53, 2
Lsyn_104_%=:
v_max_u32 v26, v16, v17
v_cmp_eq_u32 vcc, -1, v26
s_cbranch_vccnz Lsyk_105_%=
s_add_u32 s80, s55, 12288
buffer_load_dwordx4 a[48:51], v3, s[20:23], s80 offen offset:0 sc1
buffer_load_dwordx4 a[52:55], v3, s[20:23], s80 offen offset:1024 sc1
buffer_load_dwordx4 a[56:59], v3, s[20:23], s80 offen offset:2048 sc1
buffer_load_dwordx4 a[60:63], v3, s[20:23], s80 offen offset:3072 sc1
s_branch Lsyn_106_%=
Lsyk_105_%=:
s_bitset1_b32 s53, 3
Lsyn_106_%=:
v_max_u32 v26, v18, v19
v_cmp_eq_u32 vcc, -1, v26
s_cbranch_vccnz Lsyk_107_%=
s_add_u32 s80, s55, 16384
buffer_load_dwordx4 a[64:67], v3, s[20:23], s80 offen offset:0 sc1
)ASM"
R"ASM(buffer_load_dwordx4 a[68:71], v3, s[20:23], s80 offen offset:1024 sc1
buffer_load_dwordx4 a[72:75], v3, s[20:23], s80 offen offset:2048 sc1
buffer_load_dwordx4 a[76:79], v3, s[20:23], s80 offen offset:3072 sc1
s_branch Lsyn_108_%=
Lsyk_107_%=:
s_bitset1_b32 s53, 4
Lsyn_108_%=:
v_max_u32 v26, v20, v21
v_cmp_eq_u32 vcc, -1, v26
s_cbranch_vccnz Lsyk_109_%=
s_add_u32 s80, s55, 20480
buffer_load_dwordx4 a[80:83], v3, s[20:23], s80 offen offset:0 sc1
buffer_load_dwordx4 a[84:87], v3, s[20:23], s80 offen offset:1024 sc1
buffer_load_dwordx4 a[88:91], v3, s[20:23], s80 offen offset:2048 sc1
buffer_load_dwordx4 a[92:95], v3, s[20:23], s80 offen offset:3072 sc1
s_branch Lsyn_110_%=
Lsyk_109_%=:
s_bitset1_b32 s53, 5
Lsyn_110_%=:
v_max_u32 v26, v22, v23
v_cmp_eq_u32 vcc, -1, v26
s_cbranch_vccnz Lsyk_111_%=
s_add_u32 s80, s55, 24576
buffer_load_dwordx4 a[96:99], v3, s[20:23], s80 offen offset:0 sc1
buffer_load_dwordx4 a[100:103], v3, s[20:23], s80 offen offset:1024 sc1
buffer_load_dwordx4 a[104:107], v3, s[20:23], s80 offen offset:2048 sc1
buffer_load_dwordx4 a[108:111], v3, s[20:23], s80 offen offset:3072 sc1
s_branch Lsyn_112_%=
Lsyk_111_%=:
s_bitset1_b32 s53, 6
Lsyn_112_%=:
v_max_u32 v26, v24, v25
)ASM"
R"ASM(v_cmp_eq_u32 vcc, -1, v26
s_cbranch_vccnz Lsyk_113_%=
s_add_u32 s80, s55, 28672
buffer_load_dwordx4 a[112:115], v3, s[20:23], s80 offen offset:0 sc1
buffer_load_dwordx4 a[116:119], v3, s[20:23], s80 offen offset:1024 sc1
buffer_load_dwordx4 a[120:123], v3, s[20:23], s80 offen offset:2048 sc1
buffer_load_dwordx4 a[124:127], v3, s[20:23], s80 offen offset:3072 sc1
s_branch Lsyn_114_%=
Lsyk_113_%=:
s_bitset1_b32 s53, 7
Lsyn_114_%=:
Lsyl_98_%=:
s_bitcmp1_b32 s53, 0
s_cbranch_scc0 Lsyk_115_%=
buffer_load_dwordx2 v[10:11], v8, s[20:23], s56 offen offset:0 sc1
s_add_u32 s80, s55, 0
buffer_load_dwordx4 a[0:3], v3, s[20:23], s80 offen offset:0 sc1
buffer_load_dwordx4 a[4:7], v3, s[20:23], s80 offen offset:1024 sc1
buffer_load_dwordx4 a[8:11], v3, s[20:23], s80 offen offset:2048 sc1
buffer_load_dwordx4 a[12:15], v3, s[20:23], s80 offen offset:3072 sc1
Lsyk_115_%=:
s_bitcmp1_b32 s53, 1
s_cbranch_scc0 Lsyk_116_%=
buffer_load_dwordx2 v[12:13], v8, s[20:23], s56 offen offset:512 sc1
s_add_u32 s80, s55, 4096
buffer_load_dwordx4 a[16:19], v3, s[20:23], s80 offen offset:0 sc1
buffer_load_dwordx4 a[20:23], v3, s[20:23], s80 offen offset:1024 sc1
buffer_load_dwordx4 a[24:27], v3, s[20:23], s80 offen offset:2048 sc1
buffer_load_dwordx4 a[28:31], v3, s[20:23], s80 offen offset:3072 sc1
Lsyk_116_%=:
s_bitcmp1_b32 s53, 2
s_cbranch_scc0 Lsyk_117_%=
)ASM"
R"ASM(buffer_load_dwordx2 v[14:15], v8, s[20:23], s56 offen offset:1024 sc1
s_add_u32 s80, s55, 8192
buffer_load_dwordx4 a[32:35], v3, s[20:23], s80 offen offset:0 sc1
buffer_load_dwordx4 a[36:39], v3, s[20:23], s80 offen offset:1024 sc1
buffer_load_dwordx4 a[40:43], v3, s[20:23], s80 offen offset:2048 sc1
buffer_load_dwordx4 a[44:47], v3, s[20:23], s80 offen offset:3072 sc1
Lsyk_117_%=:
s_bitcmp1_b32 s53, 3
s_cbranch_scc0 Lsyk_118_%=
buffer_load_dwordx2 v[16:17], v8, s[20:23], s56 offen offset:1536 sc1
s_add_u32 s80, s55, 12288
buffer_load_dwordx4 a[48:51], v3, s[20:23], s80 offen offset:0 sc1
buffer_load_dwordx4 a[52:55], v3, s[20:23], s80 offen offset:1024 sc1
buffer_load_dwordx4 a[56:59], v3, s[20:23], s80 offen offset:2048 sc1
buffer_load_dwordx4 a[60:63], v3, s[20:23], s80 offen offset:3072 sc1
Lsyk_118_%=:
s_bitcmp1_b32 s53, 4
s_cbranch_scc0 Lsyk_119_%=
buffer_load_dwordx2 v[18:19], v8, s[20:23], s56 offen offset:2048 sc1
s_add_u32 s80, s55, 16384
buffer_load_dwordx4 a[64:67], v3, s[20:23], s80 offen offset:0 sc1
buffer_load_dwordx4 a[68:71], v3, s[20:23], s80 offen offset:1024 sc1
buffer_load_dwordx4 a[72:75], v3, s[20:23], s80 offen offset:2048 sc1
buffer_load_dwordx4 a[76:79], v3, s[20:23], s80 offen offset:3072 sc1
Lsyk_119_%=:
s_bitcmp1_b32 s53, 5
s_cbranch_scc0 Lsyk_120_%=
buffer_load_dwordx2 v[20:21], v8, s[20:23], s56 offen offset:2560 sc1
s_add_u32 s80, s55, 20480
buffer_load_dwordx4 a[80:83], v3, s[20:23], s80 offen offset:0 sc1
buffer_load_dwordx4 a[84:87], v3, s[20:23], s80 offen offset:1024 sc1
buffer_load_dwordx4 a[88:91], v3, s[20:23], s80 offen offset:2048 sc1
)ASM"
R"ASM(buffer_load_dwordx4 a[92:95], v3, s[20:23], s80 offen offset:3072 sc1
Lsyk_120_%=:
s_bitcmp1_b32 s53, 6
s_cbranch_scc0 Lsyk_121_%=
buffer_load_dwordx2 v[22:23], v8, s[20:23], s56 offen offset:3072 sc1
s_add_u32 s80, s55, 24576
buffer_load_dwordx4 a[96:99], v3, s[20:23], s80 offen offset:0 sc1
buffer_load_dwordx4 a[100:103], v3, s[20:23], s80 offen offset:1024 sc1
buffer_load_dwordx4 a[104:107], v3, s[20:23], s80 offen offset:2048 sc1
buffer_load_dwordx4 a[108:111], v3, s[20:23], s80 offen offset:3072 sc1
Lsyk_121_%=:
s_bitcmp1_b32 s53, 7
s_cbranch_scc0 Lsyk_122_%=
buffer_load_dwordx2 v[24:25], v8, s[20:23], s56 offen offset:3584 sc1
s_add_u32 s80, s55, 28672
buffer_load_dwordx4 a[112:115], v3, s[20:23], s80 offen offset:0 sc1
buffer_load_dwordx4 a[116:119], v3, s[20:23], s80 offen offset:1024 sc1
buffer_load_dwordx4 a[120:123], v3, s[20:23], s80 offen offset:2048 sc1
buffer_load_dwordx4 a[124:127], v3, s[20:23], s80 offen offset:3072 sc1
Lsyk_122_%=:
s_waitcnt vmcnt(0)
s_bitcmp1_b32 s53, 0
s_cbranch_scc0 Lsyk_123_%=
v_max_u32 v26, v10, v11
v_cmp_eq_u32 vcc, -1, v26
s_cbranch_vccnz Lsyk_123_%=
s_bitset0_b32 s53, 0
Lsyk_123_%=:
s_bitcmp1_b32 s53, 1
s_cbranch_scc0 Lsyk_124_%=
v_max_u32 v26, v12, v13
v_cmp_eq_u32 vcc, -1, v26
)ASM"
R"ASM(s_cbranch_vccnz Lsyk_124_%=
s_bitset0_b32 s53, 1
Lsyk_124_%=:
s_bitcmp1_b32 s53, 2
s_cbranch_scc0 Lsyk_125_%=
v_max_u32 v26, v14, v15
v_cmp_eq_u32 vcc, -1, v26
s_cbranch_vccnz Lsyk_125_%=
s_bitset0_b32 s53, 2
Lsyk_125_%=:
s_bitcmp1_b32 s53, 3
s_cbranch_scc0 Lsyk_126_%=
v_max_u32 v26, v16, v17
v_cmp_eq_u32 vcc, -1, v26
s_cbranch_vccnz Lsyk_126_%=
s_bitset0_b32 s53, 3
Lsyk_126_%=:
s_bitcmp1_b32 s53, 4
s_cbranch_scc0 Lsyk_127_%=
v_max_u32 v26, v18, v19
v_cmp_eq_u32 vcc, -1, v26
s_cbranch_vccnz Lsyk_127_%=
s_bitset0_b32 s53, 4
Lsyk_127_%=:
s_bitcmp1_b32 s53, 5
s_cbranch_scc0 Lsyk_128_%=
v_max_u32 v26, v20, v21
v_cmp_eq_u32 vcc, -1, v26
s_cbranch_vccnz Lsyk_128_%=
s_bitset0_b32 s53, 5
Lsyk_128_%=:
s_bitcmp1_b32 s53, 6
)ASM"
R"ASM(s_cbranch_scc0 Lsyk_129_%=
v_max_u32 v26, v22, v23
v_cmp_eq_u32 vcc, -1, v26
s_cbranch_vccnz Lsyk_129_%=
s_bitset0_b32 s53, 6
Lsyk_129_%=:
s_bitcmp1_b32 s53, 7
s_cbranch_scc0 Lsyk_130_%=
v_max_u32 v26, v24, v25
v_cmp_eq_u32 vcc, -1, v26
s_cbranch_vccnz Lsyk_130_%=
s_bitset0_b32 s53, 7
Lsyk_130_%=:
s_cmp_eq_u32 s53, 0
s_cbranch_scc1 Lydone_50_%=
s_add_u32 s52, s52, 1
s_cmp_lt_u32 s52, 0x4000
s_cbranch_scc1 Lsyl_98_%=
s_branch Lydone_50_%=
Lredoy_48_%=:
s_add_u32 s67, s67, 1
s_cmp_gt_u32 s67, 0x100
s_cbranch_scc1 Lredoyx_49_%=
s_waitcnt vmcnt(0) lgkmcnt(0)
s_branch Lyr1_46_%=
Lhl0_55_%=:
s_mov_b32 s52, 0
Lhs_131_%=:
s_add_u32 s88, s48, 0
buffer_load_dwordx4 a[0:3], v3, s[20:23], s88 offen offset:0 sc1
s_add_u32 s89, s49, 0
buffer_load_dwordx4 a[4:7], v3, s[20:23], s89 offen offset:0 sc1
)ASM"
R"ASM(buffer_load_dwordx4 a[8:11], v3, s[20:23], s88 offen offset:1024 sc1
buffer_load_dwordx4 a[12:15], v3, s[20:23], s89 offen offset:1024 sc1
buffer_load_dwordx4 a[16:19], v3, s[20:23], s88 offen offset:2048 sc1
buffer_load_dwordx4 a[20:23], v3, s[20:23], s89 offen offset:2048 sc1
buffer_load_dwordx4 a[24:27], v3, s[20:23], s88 offen offset:3072 sc1
buffer_load_dwordx4 a[28:31], v3, s[20:23], s89 offen offset:3072 sc1
s_add_u32 s88, s48, 16384
buffer_load_dwordx4 a[32:35], v3, s[20:23], s88 offen offset:0 sc1
s_add_u32 s89, s49, 16384
buffer_load_dwordx4 a[36:39], v3, s[20:23], s89 offen offset:0 sc1
buffer_load_dwordx4 a[40:43], v3, s[20:23], s88 offen offset:1024 sc1
buffer_load_dwordx4 a[44:47], v3, s[20:23], s89 offen offset:1024 sc1
buffer_load_dwordx4 a[48:51], v3, s[20:23], s88 offen offset:2048 sc1
buffer_load_dwordx4 a[52:55], v3, s[20:23], s89 offen offset:2048 sc1
buffer_load_dwordx4 a[56:59], v3, s[20:23], s88 offen offset:3072 sc1
buffer_load_dwordx4 a[60:63], v3, s[20:23], s89 offen offset:3072 sc1
s_waitcnt vmcnt(0)
v_accvgpr_read_b32 v212, a3
v_accvgpr_read_b32 v213, a7
v_accvgpr_read_b32 v214, a11
v_accvgpr_read_b32 v215, a15
v_accvgpr_read_b32 v216, a19
v_accvgpr_read_b32 v217, a23
v_accvgpr_read_b32 v218, a27
v_accvgpr_read_b32 v219, a31
v_max3_u32 v26, v212, v213, v214
v_max3_u32 v26, v26, v215, v216
v_max3_u32 v26, v26, v217, v218
v_max_u32 v26, v26, v219
v_cmp_eq_u32 vcc, -1, v26
s_cbranch_vccz Lhx_132_%=
s_add_u32 s52, s52, 1
)ASM"
R"ASM(s_cmp_lt_u32 s52, 0x4000
s_cbranch_scc1 Lhs_131_%=
Lhx_132_%=:
s_nop 7
s_setpc_b64 s[44:45]
Lhl1_56_%=:
s_mov_b32 s52, 0
Lhs_133_%=:
s_add_u32 s88, s48, 16384
buffer_load_dwordx4 a[32:35], v3, s[20:23], s88 offen offset:0 sc1
s_add_u32 s89, s49, 16384
buffer_load_dwordx4 a[36:39], v3, s[20:23], s89 offen offset:0 sc1
buffer_load_dwordx4 a[40:43], v3, s[20:23], s88 offen offset:1024 sc1
buffer_load_dwordx4 a[44:47], v3, s[20:23], s89 offen offset:1024 sc1
buffer_load_dwordx4 a[48:51], v3, s[20:23], s88 offen offset:2048 sc1
buffer_load_dwordx4 a[52:55], v3, s[20:23], s89 offen offset:2048 sc1
buffer_load_dwordx4 a[56:59], v3, s[20:23], s88 offen offset:3072 sc1
buffer_load_dwordx4 a[60:63], v3, s[20:23], s89 offen offset:3072 sc1
s_add_u32 s88, s48, 32768
buffer_load_dwordx4 a[64:67], v3, s[20:23], s88 offen offset:0 sc1
s_add_u32 s89, s49, 32768
buffer_load_dwordx4 a[68:71], v3, s[20:23], s89 offen offset:0 sc1
buffer_load_dwordx4 a[72:75], v3, s[20:23], s88 offen offset:1024 sc1
buffer_load_dwordx4 a[76:79], v3, s[20:23], s89 offen offset:1024 sc1
buffer_load_dwordx4 a[80:83], v3, s[20:23], s88 offen offset:2048 sc1
buffer_load_dwordx4 a[84:87], v3, s[20:23], s89 offen offset:2048 sc1
buffer_load_dwordx4 a[88:91], v3, s[20:23], s88 offen offset:3072 sc1
buffer_load_dwordx4 a[92:95], v3, s[20:23], s89 offen offset:3072 sc1
s_waitcnt vmcnt(0)
v_accvgpr_read_b32 v212, a35
v_accvgpr_read_b32 v213, a39
v_accvgpr_read_b32 v214, a43
)ASM"
R"ASM(v_accvgpr_read_b32 v215, a47
v_accvgpr_read_b32 v216, a51
v_accvgpr_read_b32 v217, a55
v_accvgpr_read_b32 v218, a59
v_accvgpr_read_b32 v219, a63
v_max3_u32 v26, v212, v213, v214
v_max3_u32 v26, v26, v215, v216
v_max3_u32 v26, v26, v217, v218
v_max_u32 v26, v26, v219
v_cmp_eq_u32 vcc, -1, v26
s_cbranch_vccz Lhx_134_%=
s_add_u32 s52, s52, 1
s_cmp_lt_u32 s52, 0x4000
s_cbranch_scc1 Lhs_133_%=
Lhx_134_%=:
s_nop 7
s_setpc_b64 s[44:45]
Lhl2_57_%=:
s_mov_b32 s52, 0
Lhs_135_%=:
s_add_u32 s88, s48, 32768
buffer_load_dwordx4 a[64:67], v3, s[20:23], s88 offen offset:0 sc1
s_add_u32 s89, s49, 32768
buffer_load_dwordx4 a[68:71], v3, s[20:23], s89 offen offset:0 sc1
buffer_load_dwordx4 a[72:75], v3, s[20:23], s88 offen offset:1024 sc1
buffer_load_dwordx4 a[76:79], v3, s[20:23], s89 offen offset:1024 sc1
buffer_load_dwordx4 a[80:83], v3, s[20:23], s88 offen offset:2048 sc1
buffer_load_dwordx4 a[84:87], v3, s[20:23], s89 offen offset:2048 sc1
buffer_load_dwordx4 a[88:91], v3, s[20:23], s88 offen offset:3072 sc1
buffer_load_dwordx4 a[92:95], v3, s[20:23], s89 offen offset:3072 sc1
s_add_u32 s88, s48, 49152
buffer_load_dwordx4 a[96:99], v3, s[20:23], s88 offen offset:0 sc1
)ASM"
R"ASM(s_add_u32 s89, s49, 49152
buffer_load_dwordx4 a[100:103], v3, s[20:23], s89 offen offset:0 sc1
buffer_load_dwordx4 a[104:107], v3, s[20:23], s88 offen offset:1024 sc1
buffer_load_dwordx4 a[108:111], v3, s[20:23], s89 offen offset:1024 sc1
buffer_load_dwordx4 a[112:115], v3, s[20:23], s88 offen offset:2048 sc1
buffer_load_dwordx4 a[116:119], v3, s[20:23], s89 offen offset:2048 sc1
buffer_load_dwordx4 a[120:123], v3, s[20:23], s88 offen offset:3072 sc1
buffer_load_dwordx4 a[124:127], v3, s[20:23], s89 offen offset:3072 sc1
s_waitcnt vmcnt(0)
v_accvgpr_read_b32 v212, a67
v_accvgpr_read_b32 v213, a71
v_accvgpr_read_b32 v214, a75
v_accvgpr_read_b32 v215, a79
v_accvgpr_read_b32 v216, a83
v_accvgpr_read_b32 v217, a87
v_accvgpr_read_b32 v218, a91
v_accvgpr_read_b32 v219, a95
v_max3_u32 v26, v212, v213, v214
v_max3_u32 v26, v26, v215, v216
v_max3_u32 v26, v26, v217, v218
v_max_u32 v26, v26, v219
v_cmp_eq_u32 vcc, -1, v26
s_cbranch_vccz Lhx_136_%=
s_add_u32 s52, s52, 1
s_cmp_lt_u32 s52, 0x4000
s_cbranch_scc1 Lhs_135_%=
Lhx_136_%=:
s_nop 7
s_setpc_b64 s[44:45]
Lhl3_58_%=:
s_mov_b32 s52, 0
Lhs_137_%=:
)ASM"
R"ASM(s_add_u32 s88, s48, 49152
buffer_load_dwordx4 a[96:99], v3, s[20:23], s88 offen offset:0 sc1
s_add_u32 s89, s49, 49152
buffer_load_dwordx4 a[100:103], v3, s[20:23], s89 offen offset:0 sc1
buffer_load_dwordx4 a[104:107], v3, s[20:23], s88 offen offset:1024 sc1
buffer_load_dwordx4 a[108:111], v3, s[20:23], s89 offen offset:1024 sc1
buffer_load_dwordx4 a[112:115], v3, s[20:23], s88 offen offset:2048 sc1
buffer_load_dwordx4 a[116:119], v3, s[20:23], s89 offen offset:2048 sc1
buffer_load_dwordx4 a[120:123], v3, s[20:23], s88 offen offset:3072 sc1
buffer_load_dwordx4 a[124:127], v3, s[20:23], s89 offen offset:3072 sc1
s_add_u32 s88, s48, 65536
buffer_load_dwordx4 a[0:3], v3, s[20:23], s88 offen offset:0 sc1
s_add_u32 s89, s49, 65536
buffer_load_dwordx4 a[4:7], v3, s[20:23], s89 offen offset:0 sc1
buffer_load_dwordx4 a[8:11], v3, s[20:23], s88 offen offset:1024 sc1
buffer_load_dwordx4 a[12:15], v3, s[20:23], s89 offen offset:1024 sc1
buffer_load_dwordx4 a[16:19], v3, s[20:23], s88 offen offset:2048 sc1
buffer_load_dwordx4 a[20:23], v3, s[20:23], s89 offen offset:2048 sc1
buffer_load_dwordx4 a[24:27], v3, s[20:23], s88 offen offset:3072 sc1
buffer_load_dwordx4 a[28:31], v3, s[20:23], s89 offen offset:3072 sc1
s_waitcnt vmcnt(0)
v_accvgpr_read_b32 v212, a99
v_accvgpr_read_b32 v213, a103
v_accvgpr_read_b32 v214, a107
v_accvgpr_read_b32 v215, a111
v_accvgpr_read_b32 v216, a115
v_accvgpr_read_b32 v217, a119
v_accvgpr_read_b32 v218, a123
v_accvgpr_read_b32 v219, a127
v_max3_u32 v26, v212, v213, v214
v_max3_u32 v26, v26, v215, v216
v_max3_u32 v26, v26, v217, v218
)ASM"
R"ASM(v_max_u32 v26, v26, v219
v_cmp_eq_u32 vcc, -1, v26
s_cbranch_vccz Lhx_138_%=
s_add_u32 s52, s52, 1
s_cmp_lt_u32 s52, 0x4000
s_cbranch_scc1 Lhs_137_%=
Lhx_138_%=:
s_nop 7
s_setpc_b64 s[44:45]
Lhl4_59_%=:
s_mov_b32 s52, 0
Lhs_139_%=:
s_add_u32 s88, s48, 65536
buffer_load_dwordx4 a[0:3], v3, s[20:23], s88 offen offset:0 sc1
s_add_u32 s89, s49, 65536
buffer_load_dwordx4 a[4:7], v3, s[20:23], s89 offen offset:0 sc1
buffer_load_dwordx4 a[8:11], v3, s[20:23], s88 offen offset:1024 sc1
buffer_load_dwordx4 a[12:15], v3, s[20:23], s89 offen offset:1024 sc1
buffer_load_dwordx4 a[16:19], v3, s[20:23], s88 offen offset:2048 sc1
buffer_load_dwordx4 a[20:23], v3, s[20:23], s89 offen offset:2048 sc1
buffer_load_dwordx4 a[24:27], v3, s[20:23], s88 offen offset:3072 sc1
buffer_load_dwordx4 a[28:31], v3, s[20:23], s89 offen offset:3072 sc1
s_add_u32 s88, s48, 81920
buffer_load_dwordx4 a[32:35], v3, s[20:23], s88 offen offset:0 sc1
s_add_u32 s89, s49, 81920
buffer_load_dwordx4 a[36:39], v3, s[20:23], s89 offen offset:0 sc1
buffer_load_dwordx4 a[40:43], v3, s[20:23], s88 offen offset:1024 sc1
buffer_load_dwordx4 a[44:47], v3, s[20:23], s89 offen offset:1024 sc1
buffer_load_dwordx4 a[48:51], v3, s[20:23], s88 offen offset:2048 sc1
buffer_load_dwordx4 a[52:55], v3, s[20:23], s89 offen offset:2048 sc1
buffer_load_dwordx4 a[56:59], v3, s[20:23], s88 offen offset:3072 sc1
buffer_load_dwordx4 a[60:63], v3, s[20:23], s89 offen offset:3072 sc1
)ASM"
R"ASM(s_waitcnt vmcnt(0)
v_accvgpr_read_b32 v212, a3
v_accvgpr_read_b32 v213, a7
v_accvgpr_read_b32 v214, a11
v_accvgpr_read_b32 v215, a15
v_accvgpr_read_b32 v216, a19
v_accvgpr_read_b32 v217, a23
v_accvgpr_read_b32 v218, a27
v_accvgpr_read_b32 v219, a31
v_max3_u32 v26, v212, v213, v214
v_max3_u32 v26, v26, v215, v216
v_max3_u32 v26, v26, v217, v218
v_max_u32 v26, v26, v219
v_cmp_eq_u32 vcc, -1, v26
s_cbranch_vccz Lhx_140_%=
s_add_u32 s52, s52, 1
s_cmp_lt_u32 s52, 0x4000
s_cbranch_scc1 Lhs_139_%=
Lhx_140_%=:
s_nop 7
s_setpc_b64 s[44:45]
Lhl5_60_%=:
s_mov_b32 s52, 0
Lhs_141_%=:
s_add_u32 s88, s48, 81920
buffer_load_dwordx4 a[32:35], v3, s[20:23], s88 offen offset:0 sc1
s_add_u32 s89, s49, 81920
buffer_load_dwordx4 a[36:39], v3, s[20:23], s89 offen offset:0 sc1
buffer_load_dwordx4 a[40:43], v3, s[20:23], s88 offen offset:1024 sc1
buffer_load_dwordx4 a[44:47], v3, s[20:23], s89 offen offset:1024 sc1
buffer_load_dwordx4 a[48:51], v3, s[20:23], s88 offen offset:2048 sc1
buffer_load_dwordx4 a[52:55], v3, s[20:23], s89 offen offset:2048 sc1
)ASM"
R"ASM(buffer_load_dwordx4 a[56:59], v3, s[20:23], s88 offen offset:3072 sc1
buffer_load_dwordx4 a[60:63], v3, s[20:23], s89 offen offset:3072 sc1
s_add_u32 s88, s48, 98304
buffer_load_dwordx4 a[64:67], v3, s[20:23], s88 offen offset:0 sc1
s_add_u32 s89, s49, 98304
buffer_load_dwordx4 a[68:71], v3, s[20:23], s89 offen offset:0 sc1
buffer_load_dwordx4 a[72:75], v3, s[20:23], s88 offen offset:1024 sc1
buffer_load_dwordx4 a[76:79], v3, s[20:23], s89 offen offset:1024 sc1
buffer_load_dwordx4 a[80:83], v3, s[20:23], s88 offen offset:2048 sc1
buffer_load_dwordx4 a[84:87], v3, s[20:23], s89 offen offset:2048 sc1
buffer_load_dwordx4 a[88:91], v3, s[20:23], s88 offen offset:3072 sc1
buffer_load_dwordx4 a[92:95], v3, s[20:23], s89 offen offset:3072 sc1
s_waitcnt vmcnt(0)
v_accvgpr_read_b32 v212, a35
v_accvgpr_read_b32 v213, a39
v_accvgpr_read_b32 v214, a43
v_accvgpr_read_b32 v215, a47
v_accvgpr_read_b32 v216, a51
v_accvgpr_read_b32 v217, a55
v_accvgpr_read_b32 v218, a59
v_accvgpr_read_b32 v219, a63
v_max3_u32 v26, v212, v213, v214
v_max3_u32 v26, v26, v215, v216
v_max3_u32 v26, v26, v217, v218
v_max_u32 v26, v26, v219
v_cmp_eq_u32 vcc, -1, v26
s_cbranch_vccz Lhx_142_%=
s_add_u32 s52, s52, 1
s_cmp_lt_u32 s52, 0x4000
s_cbranch_scc1 Lhs_141_%=
Lhx_142_%=:
s_nop 7
)ASM"
R"ASM(s_setpc_b64 s[44:45]
Lhl6_61_%=:
s_mov_b32 s52, 0
Lhs_143_%=:
s_add_u32 s88, s48, 98304
buffer_load_dwordx4 a[64:67], v3, s[20:23], s88 offen offset:0 sc1
s_add_u32 s89, s49, 98304
buffer_load_dwordx4 a[68:71], v3, s[20:23], s89 offen offset:0 sc1
buffer_load_dwordx4 a[72:75], v3, s[20:23], s88 offen offset:1024 sc1
buffer_load_dwordx4 a[76:79], v3, s[20:23], s89 offen offset:1024 sc1
buffer_load_dwordx4 a[80:83], v3, s[20:23], s88 offen offset:2048 sc1
buffer_load_dwordx4 a[84:87], v3, s[20:23], s89 offen offset:2048 sc1
buffer_load_dwordx4 a[88:91], v3, s[20:23], s88 offen offset:3072 sc1
buffer_load_dwordx4 a[92:95], v3, s[20:23], s89 offen offset:3072 sc1
s_waitcnt vmcnt(0)
v_accvgpr_read_b32 v212, a67
v_accvgpr_read_b32 v213, a71
v_accvgpr_read_b32 v214, a75
v_accvgpr_read_b32 v215, a79
v_accvgpr_read_b32 v216, a83
v_accvgpr_read_b32 v217, a87
v_accvgpr_read_b32 v218, a91
v_accvgpr_read_b32 v219, a95
v_max3_u32 v26, v212, v213, v214
v_max3_u32 v26, v26, v215, v216
v_max3_u32 v26, v26, v217, v218
v_max_u32 v26, v26, v219
v_cmp_eq_u32 vcc, -1, v26
s_cbranch_vccz Lhx_144_%=
s_add_u32 s52, s52, 1
s_cmp_lt_u32 s52, 0x4000
s_cbranch_scc1 Lhs_143_%=
)ASM"
R"ASM(Lhx_144_%=:
s_nop 7
s_setpc_b64 s[44:45]
Lredoh_53_%=:
s_add_u32 s67, s67, 1
s_cmp_gt_u32 s67, 0x100
s_cbranch_scc1 Lredohx_54_%=
s_add_u32 s88, s48, 0
buffer_load_dwordx4 a[0:3], v3, s[20:23], s88 offen offset:0 sc1
s_add_u32 s89, s49, 0
buffer_load_dwordx4 a[4:7], v3, s[20:23], s89 offen offset:0 sc1
buffer_load_dwordx4 a[8:11], v3, s[20:23], s88 offen offset:1024 sc1
buffer_load_dwordx4 a[12:15], v3, s[20:23], s89 offen offset:1024 sc1
buffer_load_dwordx4 a[16:19], v3, s[20:23], s88 offen offset:2048 sc1
buffer_load_dwordx4 a[20:23], v3, s[20:23], s89 offen offset:2048 sc1
buffer_load_dwordx4 a[24:27], v3, s[20:23], s88 offen offset:3072 sc1
buffer_load_dwordx4 a[28:31], v3, s[20:23], s89 offen offset:3072 sc1
s_add_u32 s88, s48, 16384
buffer_load_dwordx4 a[32:35], v3, s[20:23], s88 offen offset:0 sc1
s_add_u32 s89, s49, 16384
buffer_load_dwordx4 a[36:39], v3, s[20:23], s89 offen offset:0 sc1
buffer_load_dwordx4 a[40:43], v3, s[20:23], s88 offen offset:1024 sc1
buffer_load_dwordx4 a[44:47], v3, s[20:23], s89 offen offset:1024 sc1
buffer_load_dwordx4 a[48:51], v3, s[20:23], s88 offen offset:2048 sc1
buffer_load_dwordx4 a[52:55], v3, s[20:23], s89 offen offset:2048 sc1
buffer_load_dwordx4 a[56:59], v3, s[20:23], s88 offen offset:3072 sc1
buffer_load_dwordx4 a[60:63], v3, s[20:23], s89 offen offset:3072 sc1
s_waitcnt vmcnt(0) lgkmcnt(0)
s_branch Lf2_52_%=
LEND_%=:
LCODEEND_%=:
)ASM"
    : "+v"(tid)
    : "s"(__builtin_amdgcn_kernarg_segment_ptr()), "s"(blockIdx.x)
    : "v1","v2","v3","v4","v5","v6","v7","v8","v9","v10","v11","v12","v13","v14","v15","v16","v17","v18","v19","v20","v21","v22","v23","v24","v25","v26","v27","v28","v29","v30","v31","v32","v33","v34","v35","v36","v37","v38","v39","v40","v41","v42","v43","v44","v45","v46","v47","v48","v49","v50","v51","v52","v53","v54","v55","v56","v57","v58","v59","v60","v61","v62","v63","v64","v65","v66","v67","v68","v69","v70","v71","v72","v73","v74","v75","v76","v77","v78","v79","v80","v81","v82","v83","v84","v85","v86","v87","v88","v89","v90","v91","v92","v93","v94","v95","v96","v97","v98","v99","v100","v101","v102","v103","v104","v105","v106","v107","v108","v109","v110","v111","v112","v113","v114","v115","v116","v117","v118","v119","v120","v121","v122","v123","v124","v125","v126","v127","v128","v129","v130","v131","v132","v133","v134","v135","v136","v137","v138","v139","v140","v141","v142","v143","v144","v145","v146","v147","v148","v149","v150","v151","v152","v153","v154","v155","v156","v157","v158","v159","v160","v161","v162","v163","v164","v165","v166","v167","v168","v169","v170","v171","v172","v173","v174","v175","v176","v177","v178","v179","v180","v181","v182","v183","v184","v185","v186","v187","v188","v189","v190","v191","v192","v193","v194","v195","v196","v197","v198","v199","v200","v201","v202","v203","v204","v205","v206","v207","v208","v209","v210","v211","v212","v213","v214","v215","v216","v217","v218","v219","v220","v221","v222","v223","v224","v225","v226","v227","v228","v229","v230","v231","v232","v233","v234","v235","v236","v237","v238","v239","v240","v241","v242","v243","v244","v245","v246","v247","v248","v249","v250","v251","v252","v253","v254","v255","a0","a1","a2","a3","a4","a5","a6","a7","a8","a9","a10","a11","a12","a13","a14","a15","a16","a17","a18","a19","a20","a21","a22","a23","a24","a25","a26","a27","a28","a29","a30","a31","a32","a33","a34","a35","a36","a37","a38","a39","a40","a41","a42","a43","a44","a45","a46","a47","a48","a49","a50","a51","a52","a53","a54","a55","a56","a57","a58","a59","a60","a61","a62","a63","a64","a65","a66","a67","a68","a69","a70","a71","a72","a73","a74","a75","a76","a77","a78","a79","a80","a81","a82","a83","a84","a85","a86","a87","a88","a89","a90","a91","a92","a93","a94","a95","a96","a97","a98","a99","a100","a101","a102","a103","a104","a105","a106","a107","a108","a109","a110","a111","a112","a113","a114","a115","a116","a117","a118","a119","a120","a121","a122","a123","a124","a125","a126","a127","a128","a129","a130","a131","a132","a133","a134","a135","a136","a137","a138","a139","a140","a141","a142","a143","a144","a145","a146","a147","a148","a149","a150","a151","a152","a153","a154","a155","a156","a157","a158","a159","a160","a161","a162","a163","a164","a165","a166","a167","a168","a169","a170","a171","a172","a173","a174","a175","a176","a177","a178","a179","a180","a181","a182","a183","a184","a185","a186","a187","a188","a189","a190","a191","a192","a193","a194","a195","a196","a197","a198","a199","a200","a201","a202","a203","a204","a205","a206","a207","a208","a209","a210","a211","a212","a213","a214","a215","a216","a217","a218","a219","a220","a221","a222","a223","a224","a225","a226","a227","a228","a229","a230","a231","a232","a233","a234","a235","a236","a237","a238","a239","a240","a241","a242","a243","a244","a245","a246","a247","a248","a249","a250","a251","a252","a253","a254","a255","s8","s9","s10","s11","s12","s13","s14","s15","s16","s17","s18","s19","s20","s21","s22","s23","s24","s25","s26","s27","s28","s29","s30","s31","s32","s33","s34","s35","s36","s37","s38","s39","s40","s41","s42","s43","s44","s45","s46","s47","s48","s49","s50","s51","s52","s53","s54","s55","s56","s57","s58","s59","s60","s61","s62","s63","s64","s65","s66","s67","s68","s69","s70","s71","s72","s73","s74","s75","s76","s77","s78","s79","s80","s81","s82","s83","s84","s85","s86","s87","s88","s89","s90","s91","s92","s93","s94","s95","s96","s97","s98","s99","s100","s101", "vcc", "scc", "m0", "exec", "memory");
}

extern "C" void kernel_launch(void* const* d_in, const int* in_sizes, int n_in, void* d_out, int out_size, void* d_ws, size_t ws_size, hipStream_t stream) {
    (void)in_sizes; (void)n_in; (void)out_size; (void)ws_size;
    KArgs a;
    a.x = d_in[0]; a.wq = d_in[1]; a.wk = d_in[2]; a.wv = d_in[3]; a.wo = d_in[4];
    a.wg = d_in[6]; a.wu = d_in[7]; a.wd = d_in[8]; a.c = d_in[9]; a.wm = d_in[10];
    a.out = d_out; a.ws = d_ws;
    (void)hipFuncSetAttribute((const void*)fused_block, hipFuncAttributeMaxDynamicSharedMemorySize, 163840);
    fused_block<<<256, 256, 163840, stream>>>(a);
}
```
